```python
import torch

def _softmax(x, axis=-1):
    return torch.softmax(x, dim=axis)
def _gelu(x):
    return torch.nn.functional.gelu(x, approximate="tanh")
def _silu(x):
    return torch.nn.functional.silu(x)
def _sigmoid(x):
    return torch.sigmoid(x)
def _layernorm(x, axis=-1, eps=1e-5):
    m = x.mean(dim=axis, keepdim=True)
    v = x.var(dim=axis, keepdim=True, unbiased=False)
    return (x - m) / torch.sqrt(v + eps)
def _scatter_add(tgt, idx, src, axis=0):
    return tgt.clone().index_add_(axis, idx, src)
def _segment_sum(src, seg, n):
    out = torch.zeros((n,) + src.shape[1:], dtype=src.dtype)
    return out.index_add_(0, seg, src)
def _pad(x, pw):
    flat = tuple(v for pair in reversed(pw) for v in pair)
    return torch.nn.functional.pad(x, flat)

kernel_name = 'gated_mix__linear_kernel_hedgehog__linear_kernel_hedgehog__prompt'

def setup_inputs():

    _w = (lambda a: (lambda t: t.to(torch.bfloat16) if t.is_floating_point() else t)(torch.as_tensor(a)))
    def _A_setup():
        B, S, D, H = 1, 2048, 448, 16
        Dh = D // H
        return {
            "x": _w((lambda *s: torch.randn(*s, dtype=torch.bfloat16) * 0.05)(B, S, D)),
            "wqkv": _w((lambda *s: torch.randn(*s, dtype=torch.bfloat16) * 0.8)(D, 3 * D)),
            "w_phi": _w((lambda *s: torch.randn(*s, dtype=torch.bfloat16) * 0.8)(H, Dh, Dh) * (Dh ** -0.5)),
            "wo": _w((lambda *s: torch.randn(*s, dtype=torch.bfloat16) * 0.05)(D, D)),
        }
    _A_in = _A_setup()

    def _B_setup():
        B, S, D, H = 1, 2048, 448, 16
        Dh = D // H
        return {
            "x": _w((lambda *s: torch.randn(*s, dtype=torch.bfloat16) * 0.05)(B, S, D)),
            "wqkv": _w((lambda *s: torch.randn(*s, dtype=torch.bfloat16) * 0.8)(D, 3 * D)),
            "w_phi": _w((lambda *s: torch.randn(*s, dtype=torch.bfloat16) * 0.8)(H, Dh, Dh) * (Dh ** -0.5)),
            "wo": _w((lambda *s: torch.randn(*s, dtype=torch.bfloat16) * 0.05)(D, D)),
        }
    _B_in = _B_setup()

    return {
        "A_x": _A_in["x"],
        "A_wqkv": _A_in["wqkv"],
        "A_w_phi": _A_in["w_phi"],
        "A_wo": _A_in["wo"],
        "B_x": _B_in["x"],
        "B_wqkv": _B_in["wqkv"],
        "B_w_phi": _B_in["w_phi"],
        "B_wo": _B_in["wo"],
        "gate_w": _w((lambda *s: torch.randn(*s, dtype=torch.bfloat16) * 0.05)(448) * 16.0),
    }

def reference(A_x, A_wqkv, A_w_phi, A_wo, B_x, B_wqkv, B_w_phi, B_wo, gate_w):
    def _A_ref(x, wqkv, w_phi, wo):
        B, S, D = x.shape
        H = 16; Dh = D // H
        qkv = (lambda x, *d: x.permute(*d))((x @ wqkv).reshape(B, S, 3, H, Dh), 2, 0, 3, 1, 4)
        q, k, v = qkv[0], qkv[1], qkv[2]
        pq = torch.einsum("bhsd,hdf->bhsf", q, w_phi)
        pk = torch.einsum("bhsd,hdf->bhsf", k, w_phi)
        phi_q = torch.concatenate([_softmax(pq, axis=-1), _softmax(-pq, axis=-1)], axis=-1)
        phi_k = torch.concatenate([_softmax(pk, axis=-1), _softmax(-pk, axis=-1)], axis=-1)
        kv = torch.einsum("bhsf,bhsd->bhfd", phi_k, v)
        z  = torch.einsum("bhsf->bhf", phi_k)
        num = torch.einsum("bhqf,bhfd->bhqd", phi_q, kv)
        den = torch.einsum("bhqf,bhf->bhq", phi_q, z)[..., None] + 1e-6
        o = num / den
        return (lambda x, *d: x.permute(*d))(o, 0, 2, 1, 3).reshape(B, S, D) @ wo

    def _B_ref(x, wqkv, w_phi, wo):
        B, S, D = x.shape
        H = 16; Dh = D // H
        qkv = (lambda x, *d: x.permute(*d))((x @ wqkv).reshape(B, S, 3, H, Dh), 2, 0, 3, 1, 4)
        q, k, v = qkv[0], qkv[1], qkv[2]
        pq = torch.einsum("bhsd,hdf->bhsf", q, w_phi)
        pk = torch.einsum("bhsd,hdf->bhsf", k, w_phi)
        phi_q = torch.concatenate([_softmax(pq, axis=-1), _softmax(-pq, axis=-1)], axis=-1)
        phi_k = torch.concatenate([_softmax(pk, axis=-1), _softmax(-pk, axis=-1)], axis=-1)
        kv = torch.einsum("bhsf,bhsd->bhfd", phi_k, v)
        z  = torch.einsum("bhsf->bhf", phi_k)
        num = torch.einsum("bhqf,bhfd->bhqd", phi_q, kv)
        den = torch.einsum("bhqf,bhf->bhq", phi_q, z)[..., None] + 1e-6
        o = num / den
        return (lambda x, *d: x.permute(*d))(o, 0, 2, 1, 3).reshape(B, S, D) @ wo

    x = A_x
    g = _sigmoid((x * gate_w).sum(-1, keepdims=True))
    a = _A_ref(x, A_wqkv, A_w_phi, A_wo)
    b = _B_ref(x, B_wqkv, B_w_phi, B_wo)
    return g * a + (1 - g) * b
```

```cpp
#include <hip/hip_runtime.h>
#include <stdint.h>

typedef short bf16x8 __attribute__((ext_vector_type(8)));
typedef short s16x4 __attribute__((ext_vector_type(4)));
typedef short v4i16_t __attribute__((ext_vector_type(4)));
typedef __bf16 bf2_t __attribute__((ext_vector_type(2)));
typedef float f32x2 __attribute__((ext_vector_type(2)));
typedef float f32x4 __attribute__((ext_vector_type(4)));
typedef float f32x16 __attribute__((ext_vector_type(16)));
typedef unsigned u32x2 __attribute__((ext_vector_type(2)));
typedef unsigned u32x4 __attribute__((ext_vector_type(4)));
typedef unsigned long long u64;
#define LAS __attribute__((address_space(3)))
#define GAS __attribute__((address_space(1)))
#define DI __device__ __forceinline__
#define MFMA(a, b, c) __builtin_amdgcn_mfma_f32_32x32x16_bf16((a), (b), (c), 0, 0, 0)

constexpr int S_ = 2048, D_ = 448, DH = 28;
constexpr unsigned XROW = D_ * 2;
constexpr unsigned WROW = 3 * D_ * 2;
constexpr unsigned WS_EPOCH = 0;
constexpr unsigned WS_F1 = 4096;
constexpr unsigned WS_F2 = WS_F1 + 32 * 4 * 8 * 128;
constexpr unsigned WS_KVP = WS_F2 + 32 * 32 * 128;
constexpr unsigned WS_O = WS_KVP + 32 * 8 * 4 * 1024;
constexpr unsigned WS_END = WS_O + 32 * 32 * 4 * 1024;
constexpr unsigned WS_STAMP = 8u << 20;
constexpr unsigned SLOT = 45056, SLOT_W = 32768;
constexpr unsigned L_WPHI = 3 * SLOT;
constexpr unsigned L_GRAW = L_WPHI + 2048;
constexpr unsigned L_GT = L_GRAW + 1024;
constexpr unsigned L_PHI = 0;
constexpr unsigned L_KVA = 32768;
constexpr unsigned L_WO = 49152;
constexpr unsigned L_TOTAL = 163840;
static_assert(L_GT + 4096 <= L_TOTAL, "lds");
static_assert(L_WO + 896 * 128 == L_TOTAL, "lds");

struct Params {
    const void* x; const void* wqkvA; const void* wqkvB; const void* wphiA; const void* wphiB;
    const void* woA; const void* woB; const void* gate; void* out; void* ws;
};

typedef __amdgpu_buffer_rsrc_t rsrc_t;
DI rsrc_t mk_rsrc(const void* p, unsigned bytes) { return __builtin_amdgcn_make_buffer_rsrc((void*)p, (short)0, (int)bytes, 0x00020000); }
DI unsigned rfl(unsigned v) { return (unsigned)__builtin_amdgcn_readfirstlane((int)v); }
DI void dma16(rsrc_t r, unsigned voff, unsigned soff, unsigned lds) {
    asm volatile("s_mov_b32 m0, %0\n\ts_nop 0\n\tbuffer_load_dwordx4 %1, %2, %3 offen lds" ::"s"(lds), "v"(voff), "s"(r), "s"(soff) : "memory");
}
#define WAIT_VM(n) asm volatile("s_waitcnt vmcnt(" #n ")" ::: "memory")
#define WAIT_VM_LGKM_BAR(n) asm volatile("s_waitcnt vmcnt(" #n ") lgkmcnt(0)\n\ts_barrier" ::: "memory")
#define LGKM_BAR() asm volatile("s_waitcnt lgkmcnt(0)\n\ts_barrier" ::: "memory")

extern __shared__ __attribute__((aligned(16))) char smem_dyn[];
DI LAS char* lds_at(unsigned off) { return (LAS char*)(uintptr_t)off; }
DI bf16x8 lds_b128(unsigned a) { return *(const LAS bf16x8*)lds_at(a); }
DI s16x4 lds_tr(unsigned a) { return __builtin_bit_cast(s16x4, __builtin_amdgcn_ds_read_tr16_b64_v4i16((LAS v4i16_t*)lds_at(a))); }
DI bf16x8 tr_frag(unsigned a0, unsigned a1) { const s16x4 lo = lds_tr(a0), hi = lds_tr(a1); return __builtin_shufflevector(lo, hi, 0, 1, 2, 3, 4, 5, 6, 7); }
DI unsigned cvtpk(float lo, float hi) { const f32x2 v = {lo, hi}; return __builtin_bit_cast(unsigned, __builtin_convertvector(v, bf2_t)); }
DI bf16x8 pack8(const f32x16& x, int s) {
    const u32x4 p = {cvtpk(x[8 * s], x[8 * s + 1]), cvtpk(x[8 * s + 2], x[8 * s + 3]), cvtpk(x[8 * s + 4], x[8 * s + 5]), cvtpk(x[8 * s + 6], x[8 * s + 7])};
    return __builtin_bit_cast(bf16x8, p);
}
DI float swap_lo(float v) { auto r = __builtin_amdgcn_permlane32_swap(__float_as_uint(v), __float_as_uint(v), false, false); return __uint_as_float(r[0]); }
DI float swap_hi(float v) { auto r = __builtin_amdgcn_permlane32_swap(__float_as_uint(v), __float_as_uint(v), false, false); return __uint_as_float(r[1]); }
DI float xhalf_max(float v) { auto r = __builtin_amdgcn_permlane32_swap(__float_as_uint(v), __float_as_uint(v), false, false); return __builtin_fmaxf(__uint_as_float(r[0]), __uint_as_float(r[1])); }
DI float xhalf_min(float v) { auto r = __builtin_amdgcn_permlane32_swap(__float_as_uint(v), __float_as_uint(v), false, false); return __builtin_fminf(__uint_as_float(r[0]), __uint_as_float(r[1])); }
DI float xhalf_sum(float v) { auto r = __builtin_amdgcn_permlane32_swap(__float_as_uint(v), __float_as_uint(v), false, false); return __uint_as_float(r[0]) + __uint_as_float(r[1]); }
constexpr float L2E = 1.4426950408889634f;
constexpr unsigned ONE2 = 0x3F803F80u;
constexpr unsigned SPIN_CAP = 1u << 16;

#define STAMP(i) do { } while (0)

DI void exp_pair(const f32x16& pp, float (&ep)[14], float (&en)[14]) {
    float mx = __builtin_fmaxf(pp[0], pp[1]), mn = __builtin_fminf(pp[0], pp[1]);
#pragma unroll
    for (int i = 2; i < 14; i += 2) { mx = __builtin_fmaxf(__builtin_fmaxf(mx, pp[i]), pp[i + 1]); mn = __builtin_fminf(__builtin_fminf(mn, pp[i]), pp[i + 1]); }
    mx = xhalf_max(mx); mn = xhalf_min(mn);
    const float cp = -mx * L2E, cn = mn * L2E;
#pragma unroll
    for (int i = 0; i < 14; ++i) { ep[i] = __builtin_amdgcn_exp2f(__builtin_fmaf(pp[i], L2E, cp)); en[i] = __builtin_amdgcn_exp2f(__builtin_fmaf(pp[i], -L2E, cn)); }
}

DI void wait_flags(const GAS u64* f, u64 tag, bool active) {
    for (unsigned spins = 0; spins < SPIN_CAP; ++spins) {
        u64 v = tag;
        if (active) v = __hip_atomic_load(f, __ATOMIC_RELAXED, __HIP_MEMORY_SCOPE_AGENT);
        if (__all(v == tag)) break;
        __builtin_amdgcn_s_sleep(1);
    }
    asm volatile("" ::: "memory");
}

__global__ void __launch_bounds__(256, 1) hedgehog_mix_kernel(Params p) {
    const int tid = threadIdx.x, lane = tid & 63;
    const unsigned w = rfl(tid >> 6);
    const int r = lane & 31, h2 = lane >> 5, i16 = lane & 15, q4 = i16 >> 2, p4 = i16 & 3, blk16 = (lane >> 4) & 1;
    const unsigned xcd = blockIdx.x & 7, j = blockIdx.x >> 3;
    const unsigned tb = (xcd & 1) * 4 + (j & 3), bh = (xcd >> 1) * 8 + (j >> 2), br = bh >> 4, h = bh & 15;
    STAMP(0);
    const u64 tag = *(const GAS u64*)((const char*)p.ws + WS_EPOCH) + 1;

    const rsrc_t rx = mk_rsrc(p.x, S_ * XROW);
    const rsrc_t rw = mk_rsrc(br ? p.wqkvB : p.wqkvA, D_ * WROW);
    const rsrc_t rws = mk_rsrc(p.ws, WS_END);

    const unsigned vx0 = (lane >> 3) * XROW + 16 * ((lane & 7) ^ (lane >> 4));
    const unsigned vx1 = (lane >> 3) * XROW + 16 * ((lane & 7) ^ (4 + (lane >> 4)));
    const unsigned vw = (lane >> 2) * WROW + 16 * (lane & 3);
    const unsigned sx = (tb * 256 + w * 64) * XROW;
    const unsigned sw = w * 16 * WROW + h * 56 - 8 * (h & 1);
    auto issue = [&](unsigned s, unsigned slot) {
        const unsigned lb = slot * SLOT;
        dma16(rw, vw, sw + s * 64 * WROW + 896, lb + SLOT_W + w * 1024);
        dma16(rw, vw, sw + s * 64 * WROW + 1792, lb + SLOT_W + 4096 + w * 1024);
        dma16(rw, vw, sw + s * 64 * WROW, lb + SLOT_W + 8192 + w * 1024);
#pragma unroll
        for (int pc = 0; pc < 8; ++pc) dma16(rx, (pc & 1) ? vx1 : vx0, sx + pc * 8 * XROW + s * 128, lb + w * 8192 + pc * 1024);
    };
    {
        const rsrc_t rp = mk_rsrc(br ? p.wphiB : p.wphiA, 16 * DH * DH * 2);
        const rsrc_t rg = mk_rsrc(p.gate, XROW);
        if (w < 2) dma16(rp, lane * 16, h * (DH * DH * 2) + w * 1024, L_WPHI + w * 1024);
        else dma16(rg, lane * 16, 0, L_GRAW);
    }
    issue(0, 0); issue(1, 1); issue(2, 2);
    STAMP(1);
    WAIT_VM_LGKM_BAR(22);
    STAMP(2);
    for (int k = tid; k < D_; k += 256) {
        const unsigned g = *(const LAS unsigned short*)lds_at(L_GRAW + 2 * k);
        *(LAS u32x2*)lds_at(L_GT + (k & 63) * 64 + (k >> 6) * 8) = (u32x2){g, 0u};
    }
    bf16x8 aphi[2];
    {
        const int hr = (r >> 2) & 1, reg = (r & 3) + 4 * (r >> 3), f = 14 * hr + (reg < 14 ? reg : 0);
#pragma unroll
        for (int s = 0; s < 2; ++s)
#pragma unroll
            for (int jj = 0; jj < 8; ++jj) {
                const int d = 16 * s + 8 * (jj >> 2) + 4 * h2 + (jj & 3);
                const bool ok = reg < 14 && d < DH;
                const unsigned short v = *(const LAS unsigned short*)lds_at(L_WPHI + 2 * ((d < DH ? d : 0) * DH + f));
                aphi[s][jj] = ok ? (short)v : (short)0;
            }
    }
    LGKM_BAR();

    const int cq = 4 * blk16 + p4;
    const unsigned rowpart = (8 * h2 + q4) * 64;
    const unsigned laneW = SLOT_W + rowpart + ((8 * (h & 1) + 8 * cq) & 63);
    const unsigned laneX = w * 8192 + r * 128 + 16 * (h2 ^ ((r >> 1) & 7));
    f32x16 kT[2], qT[2], vv[2];
#pragma unroll
    for (int b = 0; b < 2; ++b)
#pragma unroll
        for (int i = 0; i < 16; ++i) { kT[b][i] = 0.f; qT[b][i] = 0.f; vv[b][i] = 0.f; }
    auto compute = [&](unsigned s, unsigned slot) {
        const unsigned lb = slot * SLOT;
        const unsigned aW = laneW + lb, aQ = cq == 7 ? L_GT + rowpart + 8 * s : aW + 8192;
#pragma unroll
        for (int ks = 0; ks < 4; ++ks) {
            const unsigned aX = (laneX ^ (32 * ks)) + lb;
            const bf16x8 x0 = lds_b128(aX), x1 = lds_b128(aX + 4096);
            const bf16x8 fk = tr_frag(aW + ks * 1024, aW + ks * 1024 + 256);
            const bf16x8 fv = tr_frag(aW + 4096 + ks * 1024, aW + 4096 + ks * 1024 + 256);
            const bf16x8 fq = tr_frag(aQ + ks * 1024, aQ + ks * 1024 + 256);
            kT[0] = MFMA(fk, x0, kT[0]); kT[1] = MFMA(fk, x1, kT[1]);
            vv[0] = MFMA(x0, fv, vv[0]); vv[1] = MFMA(x1, fv, vv[1]);
            qT[0] = MFMA(fq, x0, qT[0]); qT[1] = MFMA(fq, x1, qT[1]);
        }
    };
    compute(0, 0);
    WAIT_VM_LGKM_BAR(11); issue(3, 0); compute(1, 1);
    WAIT_VM_LGKM_BAR(11); issue(4, 1); compute(2, 2);
    WAIT_VM_LGKM_BAR(11); issue(5, 2); compute(3, 0);
    WAIT_VM_LGKM_BAR(11); issue(6, 0); compute(4, 1);
    WAIT_VM_LGKM_BAR(11); compute(5, 2);
    WAIT_VM_LGKM_BAR(0); compute(6, 0);
    STAMP(3);
    LGKM_BAR();

    const unsigned phiW = L_PHI + w * 8192;
    bf16x8 vp[2][2];
#pragma unroll
    for (int b = 0; b < 2; ++b)
#pragma unroll
        for (int s = 0; s < 2; ++s) {
            u32x4 t = __builtin_bit_cast(u32x4, pack8(vv[b], s));
            if (r == 28) t = (u32x4){ONE2, ONE2, ONE2, ONE2};
            vp[b][s] = __builtin_bit_cast(bf16x8, t);
        }
    const f32x16 zero16 = {};
#pragma unroll
    for (int b = 0; b < 2; ++b) {
        f32x16 pk = MFMA(aphi[0], pack8(kT[b], 0), zero16);
        pk = MFMA(aphi[1], pack8(kT[b], 1), pk);
        float ep[14], en[14];
        exp_pair(pk, ep, en);
        float sp = ep[0], sn = en[0];
#pragma unroll
        for (int i = 1; i < 14; ++i) { sp += ep[i]; sn += en[i]; }
        const float ip = __builtin_amdgcn_rcpf(xhalf_sum(sp)), in = __builtin_amdgcn_rcpf(xhalf_sum(sn));
        const unsigned base = phiW + b * 4096 + r * 64;
#pragma unroll
        for (int g = 0; g < 4; ++g) {
            const unsigned a = base + 8 * ((2 * g + h2) ^ ((r >> 1) & 7));
            const u32x2 dp = {cvtpk(ep[4 * g] * ip, ep[4 * g + 1] * ip), g < 3 ? cvtpk(ep[(4 * g + 2) % 14] * ip, ep[(4 * g + 3) % 14] * ip) : 0u};
            const u32x2 dn = {cvtpk(en[4 * g] * in, en[4 * g + 1] * in), g < 3 ? cvtpk(en[(4 * g + 2) % 14] * in, en[(4 * g + 3) % 14] * in) : 0u};
            *(LAS u32x2*)lds_at(a) = dp;
            *(LAS u32x2*)lds_at(a + 2048) = dn;
        }
    }
    f32x16 kvp = zero16, kvn = zero16;
    {
        const unsigned t0 = 4 * h2 + q4;
        const unsigned a0 = phiW + t0 * 64 + 8 * (cq ^ ((t0 >> 1) & 7));
        const unsigned a1 = (a0 ^ 32) + 8 * 64;
#pragma unroll
        for (int b = 0; b < 2; ++b)
#pragma unroll
            for (int s = 0; s < 2; ++s) {
                const unsigned o = b * 4096 + s * 1024;
                kvp = MFMA(tr_frag(a0 + o, a1 + o), vp[b][s], kvp);
                kvn = MFMA(tr_frag(a0 + o + 2048, a1 + o + 2048), vp[b][s], kvn);
            }
    }
    asm volatile("s_waitcnt lgkmcnt(0)" ::: "memory");
#pragma unroll
    for (int f = 0; f < 4; ++f)
#pragma unroll
        for (int hh = 0; hh < 2; ++hh) {
            const f32x16& t = f < 2 ? kvp : kvn;
            const int o = 8 * (f & 1) + 4 * hh;
            *(LAS f32x4*)lds_at(phiW + f * 2048 + hh * 1024 + lane * 16) = (f32x4){t[o], t[o + 1], t[o + 2], t[o + 3]};
        }
    LGKM_BAR();
    {
        f32x4 lo = {0.f, 0.f, 0.f, 0.f}, hi = lo;
#pragma unroll
        for (int u = 0; u < 4; ++u) {
            lo += *(const LAS f32x4*)lds_at(L_PHI + u * 8192 + w * 2048 + lane * 16);
            hi += *(const LAS f32x4*)lds_at(L_PHI + u * 8192 + w * 2048 + 1024 + lane * 16);
        }
        const u32x4 o = {cvtpk(lo[0], lo[1]), cvtpk(lo[2], lo[3]), cvtpk(hi[0], hi[1]), cvtpk(hi[2], hi[3])};
        __builtin_amdgcn_raw_buffer_store_b128(o, rws, lane * 16, WS_KVP + ((bh * 8 + tb) * 4 + w) * 1024, 16);
    }
    STAMP(4);
    WAIT_VM(0);
    if (lane == 0) __hip_atomic_store((GAS u64*)((char*)p.ws + WS_F1 + ((bh * 4 + w) * 8 + tb) * 128), tag, __ATOMIC_RELAXED, __HIP_MEMORY_SCOPE_AGENT);
    STAMP(5);

    const unsigned og = xcd * 4 + j / 7, oc = j % 7;
    const bool has_out = j < 28;
    if (has_out) {
        const rsrc_t ro = mk_rsrc((w >> 1) ? p.woB : p.woA, D_ * XROW);
        const unsigned vo = (lane >> 3) * XROW + 16 * ((lane & 7) ^ (4 * ((lane >> 4) & 1)));
#pragma unroll 4
        for (unsigned n = 0; n < 28; ++n) {
            const unsigned R = ((w & 1) * 28 + n) * 8;
            dma16(ro, vo, R * XROW + oc * 128, L_WO + ((w >> 1) * 448 + R) * 128);
        }
    }

    bf16x8 eq[2][2][2];
    float gs[2];
#pragma unroll
    for (int b = 0; b < 2; ++b) {
        f32x16 pq = MFMA(aphi[0], pack8(qT[b], 0), zero16);
        pq = MFMA(aphi[1], pack8(qT[b], 1), pq);
        float ep[14], en[14];
        exp_pair(pq, ep, en);
        const u32x4 p0 = {cvtpk(ep[0], ep[1]), cvtpk(ep[2], ep[3]), cvtpk(ep[4], ep[5]), cvtpk(ep[6], ep[7])};
        const u32x4 p1 = {cvtpk(ep[8], ep[9]), cvtpk(ep[10], ep[11]), cvtpk(ep[12], ep[13]), 0u};
        const u32x4 n0 = {cvtpk(en[0], en[1]), cvtpk(en[2], en[3]), cvtpk(en[4], en[5]), cvtpk(en[6], en[7])};
        const u32x4 n1 = {cvtpk(en[8], en[9]), cvtpk(en[10], en[11]), cvtpk(en[12], en[13]), 0u};
        eq[b][0][0] = __builtin_bit_cast(bf16x8, p0); eq[b][0][1] = __builtin_bit_cast(bf16x8, p1);
        eq[b][1][0] = __builtin_bit_cast(bf16x8, n0); eq[b][1][1] = __builtin_bit_cast(bf16x8, n1);
        const float lg = swap_hi(qT[b][12]);
        gs[b] = __builtin_amdgcn_rcpf(1.f + __builtin_amdgcn_exp2f(br ? lg * L2E : -lg * L2E));
    }
    STAMP(6);

    wait_flags((const GAS u64*)((const char*)p.ws + WS_F1 + ((bh * 4 + w) * 8 + (lane & 7)) * 128), tag, lane < 8);
    STAMP(7);
    {
        const int hr = (r >> 2) & 1, reg = (r & 3) + 4 * (r >> 3);
        const int col = reg < 14 ? 14 * hr + reg : 28;
        const unsigned vo = (h2 * 32 + col) * 16;
        u32x4 part[8];
#pragma unroll
        for (int u = 0; u < 8; ++u) part[u] = __builtin_amdgcn_raw_buffer_load_b128(rws, vo, WS_KVP + ((bh * 8 + u) * 4 + w) * 1024, 16);
        float acc[8];
#pragma unroll
        for (int i = 0; i < 8; ++i) acc[i] = 0.f;
#pragma unroll
        for (int u = 0; u < 8; ++u)
#pragma unroll
            for (int i = 0; i < 4; ++i) { acc[2 * i] += __uint_as_float(part[u][i] << 16); acc[2 * i + 1] += __uint_as_float(part[u][i] & 0xffff0000u); }
        u32x4 o = {cvtpk(acc[0], acc[1]), cvtpk(acc[2], acc[3]), cvtpk(acc[4], acc[5]), cvtpk(acc[6], acc[7])};
        if (reg == 15) o = (u32x4){ONE2, ONE2, ONE2, ONE2};
        *(LAS u32x4*)lds_at(L_KVA + w * 1024 + lane * 16) = o;
    }
    WAIT_VM_LGKM_BAR(0);
    STAMP(8);
    {
        bf16x8 akv[4];
#pragma unroll
        for (int f = 0; f < 4; ++f) akv[f] = lds_b128(L_KVA + f * 1024 + lane * 16);
#pragma unroll
        for (int b = 0; b < 2; ++b) {
            f32x16 np = MFMA(akv[0], eq[b][0][0], zero16); np = MFMA(akv[1], eq[b][0][1], np);
            f32x16 nn = MFMA(akv[2], eq[b][1][0], zero16); nn = MFMA(akv[3], eq[b][1][1], nn);
            const float ia = __builtin_amdgcn_rcpf(np[15]), ib = __builtin_amdgcn_rcpf(nn[15]);
            const float den = np[14] * ia + nn[14] * ib + 1e-6f;
            const float sc = gs[b] * __builtin_amdgcn_rcpf(den);
            const float ca = ia * sc, cb = ib * sc;
            float o[14];
#pragma unroll
            for (int i = 0; i < 14; ++i) o[i] = np[i] * ca + nn[i] * cb;
            const u32x4 o0 = {cvtpk(o[0], o[1]), cvtpk(o[2], o[3]), cvtpk(o[4], o[5]), cvtpk(o[6], o[7])};
            const u32x4 o1 = {cvtpk(o[8], o[9]), cvtpk(o[10], o[11]), cvtpk(o[12], o[13]), 0u};
            const unsigned so = WS_O + (((tb * 4 + w) * 32 + bh) * 4 + b * 2) * 1024;
            __builtin_amdgcn_raw_buffer_store_b128(o0, rws, lane * 16, so, 16);
            __builtin_amdgcn_raw_buffer_store_b128(o1, rws, lane * 16, so + 1024, 16);
        }
    }
    STAMP(9);
    WAIT_VM(0);
    if (lane == 0) __hip_atomic_store((GAS u64*)((char*)p.ws + WS_F2 + ((tb * 4 + w) * 32 + bh) * 128), tag, __ATOMIC_RELAXED, __HIP_MEMORY_SCOPE_AGENT);
    STAMP(10);
    if (!has_out) return;

    const unsigned tk = w & 1, kh = w >> 1;
    wait_flags((const GAS u64*)((const char*)p.ws + WS_F2 + (og * 32 + kh * 16 + (lane & 15)) * 128), tag, lane < 16);
    STAMP(11);
    if (blockIdx.x == 0 && tid == 0) __hip_atomic_store((GAS u64*)((char*)p.ws + WS_EPOCH), tag, __ATOMIC_RELAXED, __HIP_MEMORY_SCOPE_AGENT);
    f32x16 mine = zero16, other = zero16;
    {
        u32x4 of[16][2];
#pragma unroll
        for (int i = 0; i < 16; ++i)
#pragma unroll
            for (int s = 0; s < 2; ++s)
                of[i][s] = __builtin_amdgcn_raw_buffer_load_b128(rws, lane * 16, WS_O + (((og * 32 + kh * 16 + i) * 2 + tk) * 2 + s) * 1024, 16);
        const unsigned colp = 32 * blk16 + 8 * p4;
        const unsigned r0 = kh * 448 + 14 * h2 + q4, r1 = kh * 448 + 14 * h2 + (q4 < 1 ? q4 : 1);
        const unsigned a0 = L_WO + r0 * 128 + 64 * (kh ^ ((r0 >> 1) & 1)) + colp;
        const unsigned a1 = L_WO + r1 * 128 + 64 * (kh ^ ((r1 >> 1) & 1)) + colp;
#pragma unroll
        for (int i = 0; i < 16; ++i) {
            const unsigned o = i * 28 * 128;
            const bf16x8 b0 = __builtin_bit_cast(bf16x8, of[i][0]), b1 = __builtin_bit_cast(bf16x8, of[i][1]);
            mine = MFMA(tr_frag(a0 + o, a0 + o + 512), b0, mine);
            other = MFMA(tr_frag((a0 ^ 64) + o, (a0 ^ 64) + o + 512), b0, other);
            mine = MFMA(tr_frag(a0 + o + 1024, a1 + o + 1536), b1, mine);
            other = MFMA(tr_frag((a0 ^ 64) + o + 1024, (a1 ^ 64) + o + 1536), b1, other);
        }
    }
    STAMP(12);
#pragma unroll
    for (int g = 0; g < 4; ++g)
        *(LAS f32x4*)lds_at(L_PHI + w * 4096 + g * 1024 + lane * 16) = (f32x4){other[4 * g], other[4 * g + 1], other[4 * g + 2], other[4 * g + 3]};
    LGKM_BAR();
    {
        unsigned d[8];
#pragma unroll
        for (int g = 0; g < 4; ++g) {
            const f32x4 t = *(const LAS f32x4*)lds_at(L_PHI + (w ^ 2) * 4096 + g * 1024 + lane * 16);
            d[2 * g] = cvtpk(mine[4 * g] + t[0], mine[4 * g + 1] + t[1]);
            d[2 * g + 1] = cvtpk(mine[4 * g + 2] + t[2], mine[4 * g + 3] + t[3]);
        }
        const rsrc_t rout = mk_rsrc(p.out, S_ * XROW);
        const unsigned vo = r * XROW + h2 * 16, so = (og * 64 + tk * 32) * XROW + oc * 128 + kh * 64;
#pragma unroll
        for (int k = 0; k < 4; k += 2) {
            auto sx0 = __builtin_amdgcn_permlane32_swap(d[2 * k], d[2 * k + 2], false, false);
            auto sx1 = __builtin_amdgcn_permlane32_swap(d[2 * k + 1], d[2 * k + 3], false, false);
            const u32x4 o = {sx0[0], sx1[0], sx0[1], sx1[1]};
            __builtin_amdgcn_raw_buffer_store_b128(o, rout, vo, so + 16 * k, 0);
        }
    }
    STAMP(13);
}

extern "C" void kernel_launch(void* const* d_in, const int* in_sizes, int n_in, void* d_out, int out_size, void* d_ws, size_t ws_size,
                              hipStream_t stream) {
    Params p;
    p.x = d_in[0]; p.wqkvA = d_in[1]; p.wphiA = d_in[2]; p.woA = d_in[3];
    p.wqkvB = d_in[5]; p.wphiB = d_in[6]; p.woB = d_in[7]; p.gate = d_in[8];
    p.out = d_out; p.ws = d_ws;
    hipFuncSetAttribute((const void*)hedgehog_mix_kernel, hipFuncAttributeMaxDynamicSharedMemorySize, L_TOTAL);
    hedgehog_mix_kernel<<<256, 256, L_TOTAL, stream>>>(p);
}
```

```cpp
#include <hip/hip_runtime.h>
#include <stdint.h>

typedef short bf16x8 __attribute__((ext_vector_type(8)));
typedef short s16x4 __attribute__((ext_vector_type(4)));
typedef short v4i16_t __attribute__((ext_vector_type(4)));
typedef __bf16 bf2_t __attribute__((ext_vector_type(2)));
typedef float f32x2 __attribute__((ext_vector_type(2)));
typedef float f32x4 __attribute__((ext_vector_type(4)));
typedef float f32x16 __attribute__((ext_vector_type(16)));
typedef unsigned u32x2 __attribute__((ext_vector_type(2)));
typedef unsigned u32x4 __attribute__((ext_vector_type(4)));
typedef unsigned long long u64;
#define LAS __attribute__((address_space(3)))
#define GAS __attribute__((address_space(1)))
#define DI __device__ __forceinline__
#define MFMA(a, b, c) __builtin_amdgcn_mfma_f32_32x32x16_bf16((a), (b), (c), 0, 0, 0)

constexpr int S_ = 2048, D_ = 448, DH = 28;
constexpr unsigned XROW = D_ * 2;
constexpr unsigned WROW = 3 * D_ * 2;
constexpr unsigned WS_EPOCH = 0;
constexpr unsigned WS_F1 = 4096;
constexpr unsigned WS_F2 = WS_F1 + 32 * 4 * 8 * 128;
constexpr unsigned WS_KVP = WS_F2 + 32 * 32 * 128;
constexpr unsigned WS_O = WS_KVP + 32 * 8 * 4 * 1024;
constexpr unsigned WS_END = WS_O + 32 * 32 * 4 * 1024;
constexpr unsigned WS_STAMP = 8u << 20;
constexpr unsigned SLOT = 45056, SLOT_W = 32768;
constexpr unsigned L_GT = 0;
constexpr unsigned L_RING = 4096;
constexpr unsigned L_WPHI = L_RING + 3 * SLOT;
constexpr unsigned L_PHI = L_RING;
constexpr unsigned L_KVA = L_RING + SLOT_W;
constexpr unsigned L_WO = L_RING + SLOT;
constexpr unsigned L_TOTAL = 163840;
static_assert(L_WPHI + 2048 <= L_TOTAL, "lds");
static_assert(L_WO + 896 * 128 == L_TOTAL, "lds");

struct Params {
    const void* x; const void* wqkvA; const void* wqkvB; const void* wphiA; const void* wphiB;
    const void* woA; const void* woB; const void* gate; void* out; void* ws;
};

typedef __amdgpu_buffer_rsrc_t rsrc_t;
DI rsrc_t mk_rsrc(const void* p, unsigned bytes) { return __builtin_amdgcn_make_buffer_rsrc((void*)p, (short)0, (int)bytes, 0x00020000); }
DI unsigned rfl(unsigned v) { return (unsigned)__builtin_amdgcn_readfirstlane((int)v); }
DI void dma16(rsrc_t r, unsigned voff, unsigned soff, unsigned lds) {
    asm volatile("s_mov_b32 m0, %0\n\ts_nop 0\n\tbuffer_load_dwordx4 %1, %2, %3 offen lds" ::"s"(lds), "v"(voff), "s"(r), "s"(soff) : "memory");
}
#define WAIT_VM(n) asm volatile("s_waitcnt vmcnt(" #n ")" ::: "memory")
#define WAIT_VM_LGKM_BAR(n) asm volatile("s_waitcnt vmcnt(" #n ") lgkmcnt(0)\n\ts_barrier" ::: "memory")
#define LGKM_BAR() asm volatile("s_waitcnt lgkmcnt(0)\n\ts_barrier" ::: "memory")

extern __shared__ __attribute__((aligned(16))) char smem_dyn[];
DI LAS char* lds_at(unsigned off) { return (LAS char*)(uintptr_t)off; }
DI bf16x8 lds_b128(unsigned a) { return *(const LAS bf16x8*)lds_at(a); }
DI s16x4 lds_tr(unsigned a) { return __builtin_bit_cast(s16x4, __builtin_amdgcn_ds_read_tr16_b64_v4i16((LAS v4i16_t*)lds_at(a))); }
DI bf16x8 tr_frag(unsigned a0, unsigned a1) { const s16x4 lo = lds_tr(a0), hi = lds_tr(a1); return __builtin_shufflevector(lo, hi, 0, 1, 2, 3, 4, 5, 6, 7); }
DI unsigned cvtpk(float lo, float hi) { const f32x2 v = {lo, hi}; return __builtin_bit_cast(unsigned, __builtin_convertvector(v, bf2_t)); }
DI bf16x8 pack8(const f32x16& x, int s) {
    const u32x4 p = {cvtpk(x[8 * s], x[8 * s + 1]), cvtpk(x[8 * s + 2], x[8 * s + 3]), cvtpk(x[8 * s + 4], x[8 * s + 5]), cvtpk(x[8 * s + 6], x[8 * s + 7])};
    return __builtin_bit_cast(bf16x8, p);
}
DI float swap_lo(float v) { auto r = __builtin_amdgcn_permlane32_swap(__float_as_uint(v), __float_as_uint(v), false, false); return __uint_as_float(r[0]); }
DI float swap_hi(float v) { auto r = __builtin_amdgcn_permlane32_swap(__float_as_uint(v), __float_as_uint(v), false, false); return __uint_as_float(r[1]); }
DI float xhalf_max(float v) { auto r = __builtin_amdgcn_permlane32_swap(__float_as_uint(v), __float_as_uint(v), false, false); return __builtin_fmaxf(__uint_as_float(r[0]), __uint_as_float(r[1])); }
DI float xhalf_min(float v) { auto r = __builtin_amdgcn_permlane32_swap(__float_as_uint(v), __float_as_uint(v), false, false); return __builtin_fminf(__uint_as_float(r[0]), __uint_as_float(r[1])); }
DI float xhalf_sum(float v) { auto r = __builtin_amdgcn_permlane32_swap(__float_as_uint(v), __float_as_uint(v), false, false); return __uint_as_float(r[0]) + __uint_as_float(r[1]); }
constexpr float L2E = 1.4426950408889634f;
constexpr unsigned ONE2 = 0x3F803F80u;
constexpr unsigned SPIN_CAP = 1u << 16;

#define STAMP(i) do { } while (0)

DI void exp_pair(const f32x16& pp, float (&ep)[14], float (&en)[14]) {
    float mx = __builtin_fmaxf(pp[0], pp[1]), mn = __builtin_fminf(pp[0], pp[1]);
#pragma unroll
    for (int i = 2; i < 14; i += 2) { mx = __builtin_fmaxf(__builtin_fmaxf(mx, pp[i]), pp[i + 1]); mn = __builtin_fminf(__builtin_fminf(mn, pp[i]), pp[i + 1]); }
    mx = xhalf_max(mx); mn = xhalf_min(mn);
    const float cp = -mx * L2E, cn = mn * L2E;
#pragma unroll
    for (int i = 0; i < 14; ++i) { ep[i] = __builtin_amdgcn_exp2f(__builtin_fmaf(pp[i], L2E, cp)); en[i] = __builtin_amdgcn_exp2f(__builtin_fmaf(pp[i], -L2E, cn)); }
}

DI void wait_flags(const GAS u64* f, u64 tag, bool active) {
    for (unsigned spins = 0; spins < SPIN_CAP; ++spins) {
        u64 v = tag;
        if (active) v = __hip_atomic_load(f, __ATOMIC_RELAXED, __HIP_MEMORY_SCOPE_AGENT);
        if (__all(v == tag)) break;
        __builtin_amdgcn_s_sleep(1);
    }
    asm volatile("" ::: "memory");
}

#define SB() __builtin_amdgcn_sched_barrier(0)
struct Frag { bf16x8 x0, x1, fk, fv, fq; };

__global__ void __launch_bounds__(512, 2) hedgehog_mix_kernel(Params p) {
    const int tid = threadIdx.x, lane = tid & 63;
    const unsigned w = rfl(tid >> 6), cw = w & 3;
    const int r = lane & 31, h2 = lane >> 5, i16 = lane & 15, q4 = i16 >> 2, p4 = i16 & 3, blk16 = (lane >> 4) & 1;
    const unsigned xcd = blockIdx.x & 7, j = blockIdx.x >> 3;
    const unsigned tb = (xcd & 1) * 4 + (j & 3), bh = (xcd >> 1) * 8 + (j >> 2), br = bh >> 4, h = bh & 15;
    const unsigned og = xcd * 4 + j / 7, oc = j % 7;
    const bool has_out = j < 28;
    STAMP(0);
    const rsrc_t rws = mk_rsrc(p.ws, WS_END);
    const f32x16 zero16 = {};

    if (w >= 4) {
        const rsrc_t rx = mk_rsrc(p.x, S_ * XROW);
        const rsrc_t rw = mk_rsrc(br ? p.wqkvB : p.wqkvA, D_ * WROW);
        const unsigned vx0 = (lane >> 3) * XROW + 16 * ((lane & 7) ^ (lane >> 4));
        const unsigned vx1 = (lane >> 3) * XROW + 16 * ((lane & 7) ^ (4 + (lane >> 4)));
        const unsigned vw = (lane >> 2) * WROW + 16 * (lane & 3);
        const unsigned sx = (tb * 256 + cw * 64) * XROW;
        const unsigned sw = cw * 16 * WROW + h * 56 - 8 * (h & 1);
        auto issue = [&](unsigned s, unsigned slot) {
            const unsigned lb = L_RING + slot * SLOT;
            dma16(rw, vw, sw + s * 64 * WROW + 896, lb + SLOT_W + cw * 1024);
            dma16(rw, vw, sw + s * 64 * WROW + 1792, lb + SLOT_W + 4096 + cw * 1024);
            dma16(rw, vw, sw + s * 64 * WROW, lb + SLOT_W + 8192 + cw * 1024);
#pragma unroll
            for (int pc = 0; pc < 8; ++pc) dma16(rx, (pc & 1) ? vx1 : vx0, sx + pc * 8 * XROW + s * 128, lb + cw * 8192 + pc * 1024);
        };
        issue(0, 0); issue(1, 1); issue(2, 2);
        STAMP(1);
        WAIT_VM_LGKM_BAR(22);
        STAMP(2);
        WAIT_VM_LGKM_BAR(11);
        issue(3, 0); WAIT_VM_LGKM_BAR(11);
        issue(4, 1); WAIT_VM_LGKM_BAR(11);
        issue(5, 2); WAIT_VM_LGKM_BAR(11);
        issue(6, 0); WAIT_VM_LGKM_BAR(11);
        WAIT_VM_LGKM_BAR(0);
        STAMP(3);
        const u64 tag = *(const GAS u64*)((const char*)p.ws + WS_EPOCH) + 1;
        if (has_out) {
            const rsrc_t ro = mk_rsrc((cw >> 1) ? p.woB : p.woA, D_ * XROW);
            const unsigned vo = (lane >> 3) * XROW + 16 * ((lane & 7) ^ (4 * ((lane >> 4) & 1)));
#pragma unroll 4
            for (unsigned n = 0; n < 28; ++n) {
                const unsigned R = ((cw & 1) * 28 + n) * 8;
                dma16(ro, vo, R * XROW + oc * 128, L_WO + ((cw >> 1) * 448 + R) * 128);
            }
        }
        STAMP(4);
        LGKM_BAR();
        STAMP(5);
        {
            f32x4 lo = {0.f, 0.f, 0.f, 0.f}, hi = lo;
#pragma unroll
            for (int u = 0; u < 4; ++u) {
                lo += *(const LAS f32x4*)lds_at(L_PHI + u * 8192 + cw * 2048 + lane * 16);
                hi += *(const LAS f32x4*)lds_at(L_PHI + u * 8192 + cw * 2048 + 1024 + lane * 16);
            }
            const u32x4 o = {cvtpk(lo[0], lo[1]), cvtpk(lo[2], lo[3]), cvtpk(hi[0], hi[1]), cvtpk(hi[2], hi[3])};
            __builtin_amdgcn_raw_buffer_store_b128(o, rws, lane * 16, WS_KVP + ((bh * 8 + tb) * 4 + cw) * 1024, 16);
        }
        WAIT_VM(0);
        if (lane == 0) __hip_atomic_store((GAS u64*)((char*)p.ws + WS_F1 + ((bh * 4 + cw) * 8 + tb) * 128), tag, __ATOMIC_RELAXED, __HIP_MEMORY_SCOPE_AGENT);
        STAMP(6);
        wait_flags((const GAS u64*)((const char*)p.ws + WS_F1 + ((bh * 4 + cw) * 8 + (lane & 7)) * 128), tag, lane < 8);
        STAMP(7);
        {
            const int hr = (r >> 2) & 1, reg = (r & 3) + 4 * (r >> 3);
            const int col = reg < 14 ? 14 * hr + reg : 28;
            const unsigned vo = (h2 * 32 + col) * 16;
            u32x4 part[8];
#pragma unroll
            for (int u = 0; u < 8; ++u) part[u] = __builtin_amdgcn_raw_buffer_load_b128(rws, vo, WS_KVP + ((bh * 8 + u) * 4 + cw) * 1024, 16);
            SB();
            float acc[8];
#pragma unroll
            for (int i = 0; i < 8; ++i) acc[i] = 0.f;
#pragma unroll
            for (int u = 0; u < 8; ++u)
#pragma unroll
                for (int i = 0; i < 4; ++i) { acc[2 * i] += __uint_as_float(part[u][i] << 16); acc[2 * i + 1] += __uint_as_float(part[u][i] & 0xffff0000u); }
            u32x4 o = {cvtpk(acc[0], acc[1]), cvtpk(acc[2], acc[3]), cvtpk(acc[4], acc[5]), cvtpk(acc[6], acc[7])};
            if (reg == 15) o = (u32x4){ONE2, ONE2, ONE2, ONE2};
            *(LAS u32x4*)lds_at(L_KVA + cw * 1024 + lane * 16) = o;
        }
        STAMP(8);
        WAIT_VM_LGKM_BAR(0);
        return;
    }

    unsigned tlb_sink = 0;
    {
        const rsrc_t rA = mk_rsrc(p.woA, D_ * XROW), rB = mk_rsrc(p.woB, D_ * XROW), rO = mk_rsrc(p.out, S_ * XROW);
        const unsigned o64 = (lane + 64 * (blockIdx.x & 0)) * 65536;
        tlb_sink ^= __builtin_amdgcn_raw_buffer_load_b32(rws, o64, 0, 0);
        tlb_sink ^= __builtin_amdgcn_raw_buffer_load_b32(rws, o64, 64 * 65536, 0);
        tlb_sink ^= __builtin_amdgcn_raw_buffer_load_b32(rA, o64, 0, 0);
        tlb_sink ^= __builtin_amdgcn_raw_buffer_load_b32(rB, o64, 0, 0);
        tlb_sink ^= __builtin_amdgcn_raw_buffer_load_b32(rO, o64, 0, 0);
        asm volatile("" :: "v"(tlb_sink));
    }
    {
        const rsrc_t rg = mk_rsrc(p.gate, XROW);
        const rsrc_t rp = mk_rsrc(br ? p.wphiB : p.wphiA, 16 * DH * DH * 2);
        const unsigned g0 = __builtin_amdgcn_raw_buffer_load_b16(rg, tid * 2, 0, 0), g1 = __builtin_amdgcn_raw_buffer_load_b16(rg, tid * 2 + 512, 0, 0);
        const u32x4 wp = __builtin_amdgcn_raw_buffer_load_b128(rp, tid * 16, h * (DH * DH * 2), 0);
        *(LAS u32x2*)lds_at(L_GT + (tid & 63) * 64 + (tid >> 6) * 8) = (u32x2){g0, 0u};
        *(LAS u32x2*)lds_at(L_GT + (tid & 63) * 64 + (4 + (tid >> 6)) * 8) = (u32x2){g1, 0u};
        if (tid < 128) *(LAS u32x4*)lds_at(L_WPHI + tid * 16) = wp;
    }
    LGKM_BAR();
    STAMP(2);
    bf16x8 aphi[2];
    {
        const int hr = (r >> 2) & 1, reg = (r & 3) + 4 * (r >> 3), f = 14 * hr + (reg < 14 ? reg : 0);
#pragma unroll
        for (int s = 0; s < 2; ++s)
#pragma unroll
            for (int jj = 0; jj < 8; ++jj) {
                const int d = 16 * s + 8 * (jj >> 2) + 4 * h2 + (jj & 3);
                const bool ok = reg < 14 && d < DH;
                const unsigned short v = *(const LAS unsigned short*)lds_at(L_WPHI + 2 * ((d < DH ? d : 0) * DH + f));
                aphi[s][jj] = ok ? (short)v : (short)0;
            }
    }
    const int cq = 4 * blk16 + p4;
    const unsigned rowpart = (8 * h2 + q4) * 64;
    const unsigned laneW = L_RING + SLOT_W + rowpart + ((8 * (h & 1) + 8 * cq) & 63);
    const unsigned laneX = L_RING + cw * 8192 + r * 128 + 16 * (h2 ^ ((r >> 1) & 7));
    f32x16 kT[2], qT[2], vv[2];
#pragma unroll
    for (int b = 0; b < 2; ++b) { kT[b] = zero16; qT[b] = zero16; vv[b] = zero16; }
    {
        unsigned aX[4], aW, aQ;
        auto addr = [&](unsigned s) {
            const unsigned lb = (s % 3) * SLOT;
#pragma unroll
            for (int ks = 0; ks < 4; ++ks) { aX[ks] = (laneX ^ (32 * ks)) + lb; asm volatile("" : "+v"(aX[ks])); }
            aW = laneW + lb; aQ = cq == 7 ? L_GT + rowpart + 8 * s : aW + 8192;
            asm volatile("" : "+v"(aW), "+v"(aQ));
        };
        Frag F[2];
#define RD_X(f, ks) do { f.x0 = lds_b128(aX[ks]); } while (0)
#define RD_X1(f, ks) do { f.x1 = lds_b128(aX[ks] + 4096); } while (0)
#define RD_K(f, ks) do { f.fk = tr_frag(aW + ks * 1024, aW + ks * 1024 + 256); } while (0)
#define RD_V(f, ks) do { f.fv = tr_frag(aW + 4096 + ks * 1024, aW + 4096 + ks * 1024 + 256); } while (0)
#define RD_Q(f, ks) do { f.fq = tr_frag(aQ + ks * 1024, aQ + ks * 1024 + 256); } while (0)
        addr(0);
        RD_X(F[0], 0); RD_K(F[0], 0); RD_X1(F[0], 0); RD_V(F[0], 0); RD_Q(F[0], 0); SB();
#pragma unroll
        for (int n = 0; n < 28; ++n) {
            Frag& c = F[n & 1];
            Frag& x = F[(n + 1) & 1];
            const int nks = (n + 1) & 3;
            const bool more = n + 1 < 28;
            kT[0] = MFMA(c.fk, c.x0, kT[0]); SB();
            if (more && nks == 0) { LGKM_BAR(); addr((n + 1) >> 2); SB(); }
            if (more) { RD_X(x, nks); RD_K(x, nks); } SB();
            kT[1] = MFMA(c.fk, c.x1, kT[1]); SB();
            if (more) { RD_X1(x, nks); } SB();
            vv[0] = MFMA(c.x0, c.fv, vv[0]); SB();
            if (more) { RD_V(x, nks); } SB();
            vv[1] = MFMA(c.x1, c.fv, vv[1]); SB();
            if (more) { RD_Q(x, nks); } SB();
            qT[0] = MFMA(c.fq, c.x0, qT[0]); SB();
            qT[1] = MFMA(c.fq, c.x1, qT[1]); SB();
        }
    }
    STAMP(3);

    const unsigned phiW = L_PHI + cw * 8192;
    bf16x8 vp[2][2];
#pragma unroll
    for (int b = 0; b < 2; ++b)
#pragma unroll
        for (int s = 0; s < 2; ++s) {
            u32x4 t = __builtin_bit_cast(u32x4, pack8(vv[b], s));
            if (r == 28) t = (u32x4){ONE2, ONE2, ONE2, ONE2};
            vp[b][s] = __builtin_bit_cast(bf16x8, t);
        }
#pragma unroll
    for (int b = 0; b < 2; ++b) {
        f32x16 pk = MFMA(aphi[0], pack8(kT[b], 0), zero16);
        pk = MFMA(aphi[1], pack8(kT[b], 1), pk);
        float ep[14], en[14];
        exp_pair(pk, ep, en);
        f32x2 sm = {ep[0], en[0]};
#pragma unroll
        for (int i = 1; i < 14; ++i) sm += (f32x2){ep[i], en[i]};
        const float ip = __builtin_amdgcn_rcpf(xhalf_sum(sm[0])), in = __builtin_amdgcn_rcpf(xhalf_sum(sm[1]));
        const unsigned base = phiW + b * 4096 + r * 64;
#pragma unroll
        for (int g = 0; g < 4; ++g) {
            const unsigned a = base + 8 * ((2 * g + h2) ^ ((r >> 1) & 7));
            const u32x2 dp = {cvtpk(ep[4 * g] * ip, ep[4 * g + 1] * ip), g < 3 ? cvtpk(ep[(4 * g + 2) % 14] * ip, ep[(4 * g + 3) % 14] * ip) : 0u};
            const u32x2 dn = {cvtpk(en[4 * g] * in, en[4 * g + 1] * in), g < 3 ? cvtpk(en[(4 * g + 2) % 14] * in, en[(4 * g + 3) % 14] * in) : 0u};
            *(LAS u32x2*)lds_at(a) = dp;
            *(LAS u32x2*)lds_at(a + 2048) = dn;
        }
    }
    f32x16 kvp = zero16, kvn = zero16;
    {
        const unsigned t0 = 4 * h2 + q4;
        const unsigned a0 = phiW + t0 * 64 + 8 * (cq ^ ((t0 >> 1) & 7));
        const unsigned a1 = (a0 ^ 32) + 8 * 64;
#pragma unroll
        for (int b = 0; b < 2; ++b)
#pragma unroll
            for (int s = 0; s < 2; ++s) {
                const unsigned o = b * 4096 + s * 1024;
                kvp = MFMA(tr_frag(a0 + o, a1 + o), vp[b][s], kvp);
                kvn = MFMA(tr_frag(a0 + o + 2048, a1 + o + 2048), vp[b][s], kvn);
            }
    }
    asm volatile("s_waitcnt lgkmcnt(0)" ::: "memory");
#pragma unroll
    for (int f = 0; f < 4; ++f)
#pragma unroll
        for (int hh = 0; hh < 2; ++hh) {
            const f32x16& t = f < 2 ? kvp : kvn;
            const int o = 8 * (f & 1) + 4 * hh;
            *(LAS f32x4*)lds_at(phiW + f * 2048 + hh * 1024 + lane * 16) = (f32x4){t[o], t[o + 1], t[o + 2], t[o + 3]};
        }
    LGKM_BAR();
    STAMP(5);

    bf16x8 eq[2][2][2];
    float gs[2];
#pragma unroll
    for (int b = 0; b < 2; ++b) {
        f32x16 pq = MFMA(aphi[0], pack8(qT[b], 0), zero16);
        pq = MFMA(aphi[1], pack8(qT[b], 1), pq);
        float ep[14], en[14];
        exp_pair(pq, ep, en);
        const u32x4 p0 = {cvtpk(ep[0], ep[1]), cvtpk(ep[2], ep[3]), cvtpk(ep[4], ep[5]), cvtpk(ep[6], ep[7])};
        const u32x4 p1 = {cvtpk(ep[8], ep[9]), cvtpk(ep[10], ep[11]), cvtpk(ep[12], ep[13]), 0u};
        const u32x4 n0 = {cvtpk(en[0], en[1]), cvtpk(en[2], en[3]), cvtpk(en[4], en[5]), cvtpk(en[6], en[7])};
        const u32x4 n1 = {cvtpk(en[8], en[9]), cvtpk(en[10], en[11]), cvtpk(en[12], en[13]), 0u};
        eq[b][0][0] = __builtin_bit_cast(bf16x8, p0); eq[b][0][1] = __builtin_bit_cast(bf16x8, p1);
        eq[b][1][0] = __builtin_bit_cast(bf16x8, n0); eq[b][1][1] = __builtin_bit_cast(bf16x8, n1);
        const float lg = swap_hi(qT[b][12]);
        gs[b] = __builtin_amdgcn_rcpf(1.f + __builtin_amdgcn_exp2f(br ? lg * L2E : -lg * L2E));
    }
#pragma unroll
    for (int b = 0; b < 2; ++b) asm volatile("" : "+v"(eq[b][0][0]), "+v"(eq[b][0][1]), "+v"(eq[b][1][0]), "+v"(eq[b][1][1]), "+v"(gs[b]));
    const u64 tag = *(const GAS u64*)((const char*)p.ws + WS_EPOCH) + 1;
    STAMP(6);
    LGKM_BAR();
    STAMP(8);
    {
        bf16x8 akv[4];
#pragma unroll
        for (int f = 0; f < 4; ++f) akv[f] = lds_b128(L_KVA + f * 1024 + lane * 16);
#pragma unroll
        for (int b = 0; b < 2; ++b) {
            f32x16 np = MFMA(akv[0], eq[b][0][0], zero16); np = MFMA(akv[1], eq[b][0][1], np);
            f32x16 nn = MFMA(akv[2], eq[b][1][0], zero16); nn = MFMA(akv[3], eq[b][1][1], nn);
            const float ia = __builtin_amdgcn_rcpf(np[15]), ib = __builtin_amdgcn_rcpf(nn[15]);
            const float den = np[14] * ia + nn[14] * ib + 1e-6f;
            const float sc = gs[b] * __builtin_amdgcn_rcpf(den);
            const float ca = ia * sc, cb = ib * sc;
            float o[14];
#pragma unroll
            for (int i = 0; i < 14; ++i) o[i] = np[i] * ca + nn[i] * cb;
            const u32x4 o0 = {cvtpk(o[0], o[1]), cvtpk(o[2], o[3]), cvtpk(o[4], o[5]), cvtpk(o[6], o[7])};
            const u32x4 o1 = {cvtpk(o[8], o[9]), cvtpk(o[10], o[11]), cvtpk(o[12], o[13]), 0u};
            const unsigned so = WS_O + (((tb * 4 + cw) * 32 + bh) * 4 + b * 2) * 1024;
            __builtin_amdgcn_raw_buffer_store_b128(o0, rws, lane * 16, so, 16);
            __builtin_amdgcn_raw_buffer_store_b128(o1, rws, lane * 16, so + 1024, 16);
        }
    }
    STAMP(9);
    WAIT_VM(0);
    if (lane == 0) __hip_atomic_store((GAS u64*)((char*)p.ws + WS_F2 + ((tb * 4 + cw) * 32 + bh) * 128), tag, __ATOMIC_RELAXED, __HIP_MEMORY_SCOPE_AGENT);
    STAMP(10);
    if (!has_out) return;

    const unsigned tk = cw & 1, kh = cw >> 1;
    wait_flags((const GAS u64*)((const char*)p.ws + WS_F2 + (og * 32 + kh * 16 + (lane & 15)) * 128), tag, lane < 16);
    STAMP(11);
    if (blockIdx.x == 0 && tid == 0) __hip_atomic_store((GAS u64*)((char*)p.ws + WS_EPOCH), tag, __ATOMIC_RELAXED, __HIP_MEMORY_SCOPE_AGENT);
    f32x16 mine = zero16, other = zero16;
    {
        u32x4 of[16][2];
#pragma unroll
        for (int i = 0; i < 16; ++i)
#pragma unroll
            for (int s = 0; s < 2; ++s)
                of[i][s] = __builtin_amdgcn_raw_buffer_load_b128(rws, lane * 16, WS_O + (((og * 32 + kh * 16 + i) * 2 + tk) * 2 + s) * 1024, 16);
        SB();
        const unsigned colp = 32 * blk16 + 8 * p4;
        const unsigned r0 = kh * 448 + 14 * h2 + q4, r1 = kh * 448 + 14 * h2 + (q4 < 1 ? q4 : 1);
        unsigned a0 = L_WO + r0 * 128 + 64 * (kh ^ ((r0 >> 1) & 1)) + colp;
        unsigned a1 = L_WO + r1 * 128 + 64 * (kh ^ ((r1 >> 1) & 1)) + colp;
        unsigned a2 = a0 ^ 64, a3 = a1 ^ 64;
        asm volatile("" : "+v"(a0), "+v"(a1), "+v"(a2), "+v"(a3));
        bf16x8 A[2][4];
        auto rd = [&](bf16x8 (&f)[4], int i) {
            const unsigned o = i * 28 * 128;
            f[0] = tr_frag(a0 + o, a0 + o + 512); f[1] = tr_frag(a2 + o, a2 + o + 512);
            f[2] = tr_frag(a0 + o + 1024, a1 + o + 1536); f[3] = tr_frag(a2 + o + 1024, a3 + o + 1536);
        };
        rd(A[0], 0); SB();
#pragma unroll
        for (int i = 0; i < 16; ++i) {
            if (i + 1 < 16) rd(A[(i + 1) & 1], i + 1);
            SB();
            const bf16x8 b0 = __builtin_bit_cast(bf16x8, of[i][0]), b1 = __builtin_bit_cast(bf16x8, of[i][1]);
            mine = MFMA(A[i & 1][0], b0, mine); other = MFMA(A[i & 1][1], b0, other);
            mine = MFMA(A[i & 1][2], b1, mine); other = MFMA(A[i & 1][3], b1, other);
            SB();
        }
    }
    STAMP(12);
#pragma unroll
    for (int g = 0; g < 4; ++g)
        *(LAS f32x4*)lds_at(L_PHI + cw * 4096 + g * 1024 + lane * 16) = (f32x4){other[4 * g], other[4 * g + 1], other[4 * g + 2], other[4 * g + 3]};
    LGKM_BAR();
    {
        unsigned d[8];
#pragma unroll
        for (int g = 0; g < 4; ++g) {
            const f32x4 t = *(const LAS f32x4*)lds_at(L_PHI + (cw ^ 2) * 4096 + g * 1024 + lane * 16);
            d[2 * g] = cvtpk(mine[4 * g] + t[0], mine[4 * g + 1] + t[1]);
            d[2 * g + 1] = cvtpk(mine[4 * g + 2] + t[2], mine[4 * g + 3] + t[3]);
        }
        const rsrc_t rout = mk_rsrc(p.out, S_ * XROW);
        const unsigned vo = r * XROW + h2 * 16, so = (og * 64 + tk * 32) * XROW + oc * 128 + kh * 64;
#pragma unroll
        for (int k = 0; k < 4; k += 2) {
            auto sx0 = __builtin_amdgcn_permlane32_swap(d[2 * k], d[2 * k + 2], false, false);
            auto sx1 = __builtin_amdgcn_permlane32_swap(d[2 * k + 1], d[2 * k + 3], false, false);
            const u32x4 o = {sx0[0], sx1[0], sx0[1], sx1[1]};
            __builtin_amdgcn_raw_buffer_store_b128(o, rout, vo, so + 16 * k, 0);
        }
    }
    STAMP(13);
}

extern "C" void kernel_launch(void* const* d_in, const int* in_sizes, int n_in, void* d_out, int out_size, void* d_ws, size_t ws_size,
                              hipStream_t stream) {
    Params p;
    p.x = d_in[0]; p.wqkvA = d_in[1]; p.wphiA = d_in[2]; p.woA = d_in[3];
    p.wqkvB = d_in[5]; p.wphiB = d_in[6]; p.woB = d_in[7]; p.gate = d_in[8];
    p.out = d_out; p.ws = d_ws;
    (void)hipFuncSetAttribute((const void*)hedgehog_mix_kernel, hipFuncAttributeMaxDynamicSharedMemorySize, L_TOTAL);
    hedgehog_mix_kernel<<<256, 512, L_TOTAL, stream>>>(p);
}
```

```cpp
#include <hip/hip_runtime.h>
#include <stdint.h>

typedef short bf16x8 __attribute__((ext_vector_type(8)));
typedef short s16x4 __attribute__((ext_vector_type(4)));
typedef short v4i16_t __attribute__((ext_vector_type(4)));
typedef __bf16 bf2_t __attribute__((ext_vector_type(2)));
typedef float f32x2 __attribute__((ext_vector_type(2)));
typedef float f32x4 __attribute__((ext_vector_type(4)));
typedef float f32x16 __attribute__((ext_vector_type(16)));
typedef unsigned u32x2 __attribute__((ext_vector_type(2)));
typedef unsigned u32x4 __attribute__((ext_vector_type(4)));
typedef unsigned long long u64;
#define LAS __attribute__((address_space(3)))
#define GAS __attribute__((address_space(1)))
#define DI __device__ __forceinline__
#define MFMA(a, b, c) __builtin_amdgcn_mfma_f32_32x32x16_bf16((a), (b), (c), 0, 0, 0)

constexpr int S_ = 2048, D_ = 448, DH = 28;
constexpr unsigned XROW = D_ * 2;
constexpr unsigned WROW = 3 * D_ * 2;
constexpr unsigned WS_EPOCH = 0;
constexpr unsigned WS_F1 = 4096;
constexpr unsigned WS_F2 = WS_F1 + 32 * 4 * 8 * 128;
constexpr unsigned WS_KVP = WS_F2 + 32 * 32 * 128;
constexpr unsigned WS_O = WS_KVP + 32 * 8 * 4 * 1024;
constexpr unsigned WS_END = WS_O + 32 * 32 * 4 * 1024;
constexpr unsigned WS_STAMP = 8u << 20;
constexpr unsigned SLOT = 45056, SLOT_W = 32768;
constexpr unsigned L_GT = 0;
constexpr unsigned L_RING = 4096;
constexpr unsigned L_WPHI = L_RING + 3 * SLOT;
constexpr unsigned L_PHI = L_RING;
constexpr unsigned L_KVA = L_RING + SLOT_W;
constexpr unsigned L_WO = L_RING + SLOT;
constexpr unsigned L_TOTAL = 163840;
static_assert(L_WPHI + 2048 <= L_TOTAL, "lds");
static_assert(L_WO + 896 * 128 == L_TOTAL, "lds");

struct Params {
    const void* x; const void* wqkvA; const void* wqkvB; const void* wphiA; const void* wphiB;
    const void* woA; const void* woB; const void* gate; void* out; void* ws;
};

typedef __amdgpu_buffer_rsrc_t rsrc_t;
DI rsrc_t mk_rsrc(const void* p, unsigned bytes) { return __builtin_amdgcn_make_buffer_rsrc((void*)p, (short)0, (int)bytes, 0x00020000); }
DI unsigned rfl(unsigned v) { return (unsigned)__builtin_amdgcn_readfirstlane((int)v); }
DI void dma16(rsrc_t r, unsigned voff, unsigned soff, unsigned lds) {
    asm volatile("s_mov_b32 m0, %0\n\ts_nop 0\n\tbuffer_load_dwordx4 %1, %2, %3 offen lds" ::"s"(lds), "v"(voff), "s"(r), "s"(soff) : "memory");
}
#define WAIT_VM(n) asm volatile("s_waitcnt vmcnt(" #n ")" ::: "memory")
#define WAIT_VM_LGKM_BAR(n) asm volatile("s_waitcnt vmcnt(" #n ") lgkmcnt(0)\n\ts_barrier" ::: "memory")
#define LGKM_BAR() asm volatile("s_waitcnt lgkmcnt(0)\n\ts_barrier" ::: "memory")

extern __shared__ __attribute__((aligned(16))) char smem_dyn[];
DI LAS char* lds_at(unsigned off) { return (LAS char*)(uintptr_t)off; }
DI bf16x8 lds_b128(unsigned a) { return *(const LAS bf16x8*)lds_at(a); }
DI s16x4 lds_tr(unsigned a) { return __builtin_bit_cast(s16x4, __builtin_amdgcn_ds_read_tr16_b64_v4i16((LAS v4i16_t*)lds_at(a))); }
DI bf16x8 tr_frag(unsigned a0, unsigned a1) { const s16x4 lo = lds_tr(a0), hi = lds_tr(a1); return __builtin_shufflevector(lo, hi, 0, 1, 2, 3, 4, 5, 6, 7); }
DI unsigned cvtpk(float lo, float hi) { const f32x2 v = {lo, hi}; return __builtin_bit_cast(unsigned, __builtin_convertvector(v, bf2_t)); }
DI bf16x8 pack8(const f32x16& x, int s) {
    const u32x4 p = {cvtpk(x[8 * s], x[8 * s + 1]), cvtpk(x[8 * s + 2], x[8 * s + 3]), cvtpk(x[8 * s + 4], x[8 * s + 5]), cvtpk(x[8 * s + 6], x[8 * s + 7])};
    return __builtin_bit_cast(bf16x8, p);
}
DI float swap_lo(float v) { auto r = __builtin_amdgcn_permlane32_swap(__float_as_uint(v), __float_as_uint(v), false, false); return __uint_as_float(r[0]); }
DI float swap_hi(float v) { auto r = __builtin_amdgcn_permlane32_swap(__float_as_uint(v), __float_as_uint(v), false, false); return __uint_as_float(r[1]); }
DI float xhalf_max(float v) { auto r = __builtin_amdgcn_permlane32_swap(__float_as_uint(v), __float_as_uint(v), false, false); return __builtin_fmaxf(__uint_as_float(r[0]), __uint_as_float(r[1])); }
DI float xhalf_min(float v) { auto r = __builtin_amdgcn_permlane32_swap(__float_as_uint(v), __float_as_uint(v), false, false); return __builtin_fminf(__uint_as_float(r[0]), __uint_as_float(r[1])); }
DI float xhalf_sum(float v) { auto r = __builtin_amdgcn_permlane32_swap(__float_as_uint(v), __float_as_uint(v), false, false); return __uint_as_float(r[0]) + __uint_as_float(r[1]); }
constexpr float L2E = 1.4426950408889634f;
constexpr unsigned ONE2 = 0x3F803F80u;
constexpr unsigned SPIN_CAP = 1u << 16;

#define STAMP(i) do { } while (0)

DI void exp_pair(const f32x16& pp, float (&ep)[14], float (&en)[14]) {
    float mx = __builtin_fmaxf(pp[0], pp[1]), mn = __builtin_fminf(pp[0], pp[1]);
#pragma unroll
    for (int i = 2; i < 14; i += 2) { mx = __builtin_fmaxf(__builtin_fmaxf(mx, pp[i]), pp[i + 1]); mn = __builtin_fminf(__builtin_fminf(mn, pp[i]), pp[i + 1]); }
    mx = xhalf_max(mx); mn = xhalf_min(mn);
    const float cp = -mx * L2E, cn = mn * L2E;
#pragma unroll
    for (int i = 0; i < 14; ++i) { ep[i] = __builtin_amdgcn_exp2f(__builtin_fmaf(pp[i], L2E, cp)); en[i] = __builtin_amdgcn_exp2f(__builtin_fmaf(pp[i], -L2E, cn)); }
}

DI void wait_flags(const GAS u64* f, u64 tag, bool active) {
    for (unsigned spins = 0; spins < SPIN_CAP; ++spins) {
        u64 v = tag;
        if (active) v = __hip_atomic_load(f, __ATOMIC_RELAXED, __HIP_MEMORY_SCOPE_AGENT);
        if (__all(v == tag)) break;
        __builtin_amdgcn_s_sleep(1);
    }
    asm volatile("" ::: "memory");
}

#define SB() __builtin_amdgcn_sched_barrier(0)
struct Frag { bf16x8 x0, x1, fk, fv, fq; };

__global__ void __launch_bounds__(512, 2) hedgehog_mix_kernel(Params p) {
    const int tid = threadIdx.x, lane = tid & 63;
    const unsigned w = rfl(tid >> 6), cw = w & 3;
    const int r = lane & 31, h2 = lane >> 5, i16 = lane & 15, q4 = i16 >> 2, p4 = i16 & 3, blk16 = (lane >> 4) & 1;
    const unsigned xcd = blockIdx.x & 7, j = blockIdx.x >> 3;
    const unsigned tb = (xcd & 1) * 4 + (j & 3), bh = (xcd >> 1) * 8 + (j >> 2), br = bh >> 4, h = bh & 15;
    const unsigned og = xcd * 4 + j / 7, oc = j % 7;
    const bool has_out = j < 28;
    STAMP(0);
    const rsrc_t rws = mk_rsrc(p.ws, WS_END);
    const f32x16 zero16 = {};

    if (w >= 4) {
        const rsrc_t rx = mk_rsrc(p.x, S_ * XROW);
        const rsrc_t rw = mk_rsrc(br ? p.wqkvB : p.wqkvA, D_ * WROW);
        const unsigned vx0 = (lane >> 3) * XROW + 16 * ((lane & 7) ^ (lane >> 4));
        const unsigned vx1 = (lane >> 3) * XROW + 16 * ((lane & 7) ^ (4 + (lane >> 4)));
        const unsigned vw = (lane >> 2) * WROW + 16 * (lane & 3);
        const unsigned sx = (tb * 256 + cw * 64) * XROW;
        const unsigned sw = cw * 16 * WROW + h * 56 - 8 * (h & 1);
        auto issue = [&](unsigned s, unsigned slot) {
            const unsigned lb = L_RING + slot * SLOT;
            dma16(rw, vw, sw + s * 64 * WROW + 896, lb + SLOT_W + cw * 1024);
            dma16(rw, vw, sw + s * 64 * WROW + 1792, lb + SLOT_W + 4096 + cw * 1024);
            dma16(rw, vw, sw + s * 64 * WROW, lb + SLOT_W + 8192 + cw * 1024);
#pragma unroll
            for (int pc = 0; pc < 8; ++pc) dma16(rx, (pc & 1) ? vx1 : vx0, sx + pc * 8 * XROW + s * 128, lb + cw * 8192 + pc * 1024);
        };
        issue(0, 0); issue(1, 1); issue(2, 2);
        STAMP(1);
        WAIT_VM_LGKM_BAR(22);
        STAMP(2);
        WAIT_VM_LGKM_BAR(11);
        issue(3, 0); WAIT_VM_LGKM_BAR(11);
        issue(4, 1); WAIT_VM_LGKM_BAR(11);
        issue(5, 2); WAIT_VM_LGKM_BAR(11);
        issue(6, 0); WAIT_VM_LGKM_BAR(11);
        WAIT_VM_LGKM_BAR(0);
        STAMP(3);
        const u64 tag = *(const GAS u64*)((const char*)p.ws + WS_EPOCH) + 1;
        if (has_out) {
            const rsrc_t ro = mk_rsrc((cw >> 1) ? p.woB : p.woA, D_ * XROW);
            const unsigned vo = (lane >> 3) * XROW + 16 * ((lane & 7) ^ (4 * ((lane >> 4) & 1)));
#pragma unroll 4
            for (unsigned n = 0; n < 28; ++n) {
                const unsigned R = ((cw & 1) * 28 + n) * 8;
                dma16(ro, vo, R * XROW + oc * 128, L_WO + ((cw >> 1) * 448 + R) * 128);
            }
        }
        STAMP(4);
        LGKM_BAR();
        STAMP(5);
        {
            f32x4 lo = {0.f, 0.f, 0.f, 0.f}, hi = lo;
#pragma unroll
            for (int u = 0; u < 4; ++u) {
                lo += *(const LAS f32x4*)lds_at(L_PHI + u * 8192 + cw * 2048 + lane * 16);
                hi += *(const LAS f32x4*)lds_at(L_PHI + u * 8192 + cw * 2048 + 1024 + lane * 16);
            }
            const u32x4 o = {cvtpk(lo[0], lo[1]), cvtpk(lo[2], lo[3]), cvtpk(hi[0], hi[1]), cvtpk(hi[2], hi[3])};
            __builtin_amdgcn_raw_buffer_store_b128(o, rws, lane * 16, WS_KVP + ((bh * 8 + tb) * 4 + cw) * 1024, 16);
        }
        WAIT_VM(0);
        if (lane == 0) __hip_atomic_store((GAS u64*)((char*)p.ws + WS_F1 + ((bh * 4 + cw) * 8 + tb) * 128), tag, __ATOMIC_RELAXED, __HIP_MEMORY_SCOPE_AGENT);
        STAMP(6);
        wait_flags((const GAS u64*)((const char*)p.ws + WS_F1 + ((bh * 4 + cw) * 8 + (lane & 7)) * 128), tag, lane < 8);
        STAMP(7);
        {
            const int hr = (r >> 2) & 1, reg = (r & 3) + 4 * (r >> 3);
            const int col = reg < 14 ? 14 * hr + reg : 28;
            const unsigned vo = (h2 * 32 + col) * 16;
            u32x4 part[8];
#pragma unroll
            for (int u = 0; u < 8; ++u) part[u] = __builtin_amdgcn_raw_buffer_load_b128(rws, vo, WS_KVP + ((bh * 8 + u) * 4 + cw) * 1024, 16);
            SB();
            float acc[8];
#pragma unroll
            for (int i = 0; i < 8; ++i) acc[i] = 0.f;
#pragma unroll
            for (int u = 0; u < 8; ++u)
#pragma unroll
                for (int i = 0; i < 4; ++i) { acc[2 * i] += __uint_as_float(part[u][i] << 16); acc[2 * i + 1] += __uint_as_float(part[u][i] & 0xffff0000u); }
            u32x4 o = {cvtpk(acc[0], acc[1]), cvtpk(acc[2], acc[3]), cvtpk(acc[4], acc[5]), cvtpk(acc[6], acc[7])};
            if (reg == 15) o = (u32x4){ONE2, ONE2, ONE2, ONE2};
            *(LAS u32x4*)lds_at(L_KVA + cw * 1024 + lane * 16) = o;
        }
        STAMP(8);
        WAIT_VM_LGKM_BAR(0);
        return;
    }

    {
        const rsrc_t rg = mk_rsrc(p.gate, XROW);
        const rsrc_t rp = mk_rsrc(br ? p.wphiB : p.wphiA, 16 * DH * DH * 2);
        const unsigned g0 = __builtin_amdgcn_raw_buffer_load_b16(rg, tid * 2, 0, 0), g1 = __builtin_amdgcn_raw_buffer_load_b16(rg, tid * 2 + 512, 0, 0);
        const u32x4 wp = __builtin_amdgcn_raw_buffer_load_b128(rp, tid * 16, h * (DH * DH * 2), 0);
        *(LAS u32x2*)lds_at(L_GT + (tid & 63) * 64 + (tid >> 6) * 8) = (u32x2){g0, 0u};
        *(LAS u32x2*)lds_at(L_GT + (tid & 63) * 64 + (4 + (tid >> 6)) * 8) = (u32x2){g1, 0u};
        if (tid < 128) *(LAS u32x4*)lds_at(L_WPHI + tid * 16) = wp;
    }
    LGKM_BAR();
    STAMP(2);
    bf16x8 aphi[2];
    {
        const int hr = (r >> 2) & 1, reg = (r & 3) + 4 * (r >> 3), f = 14 * hr + (reg < 14 ? reg : 0);
#pragma unroll
        for (int s = 0; s < 2; ++s)
#pragma unroll
            for (int jj = 0; jj < 8; ++jj) {
                const int d = 16 * s + 8 * (jj >> 2) + 4 * h2 + (jj & 3);
                const bool ok = reg < 14 && d < DH;
                const unsigned short v = *(const LAS unsigned short*)lds_at(L_WPHI + 2 * ((d < DH ? d : 0) * DH + f));
                aphi[s][jj] = ok ? (short)v : (short)0;
            }
    }
    const int cq = 4 * blk16 + p4;
    const unsigned rowpart = (8 * h2 + q4) * 64;
    const unsigned laneW = L_RING + SLOT_W + rowpart + ((8 * (h & 1) + 8 * cq) & 63);
    const unsigned laneX = L_RING + cw * 8192 + r * 128 + 16 * (h2 ^ ((r >> 1) & 7));
    f32x16 kT[2], qT[2], vv[2];
#pragma unroll
    for (int b = 0; b < 2; ++b) { kT[b] = zero16; qT[b] = zero16; vv[b] = zero16; }
    {
        unsigned aX[4], aW, aQ;
        auto addr = [&](unsigned s) {
            const unsigned lb = (s % 3) * SLOT;
#pragma unroll
            for (int ks = 0; ks < 4; ++ks) { aX[ks] = (laneX ^ (32 * ks)) + lb; asm volatile("" : "+v"(aX[ks])); }
            aW = laneW + lb; aQ = cq == 7 ? L_GT + rowpart + 8 * s : aW + 8192;
            asm volatile("" : "+v"(aW), "+v"(aQ));
        };
        Frag F[2];
#define RD_X(f, ks) do { f.x0 = lds_b128(aX[ks]); } while (0)
#define RD_X1(f, ks) do { f.x1 = lds_b128(aX[ks] + 4096); } while (0)
#define RD_K(f, ks) do { f.fk = tr_frag(aW + ks * 1024, aW + ks * 1024 + 256); } while (0)
#define RD_V(f, ks) do { f.fv = tr_frag(aW + 4096 + ks * 1024, aW + 4096 + ks * 1024 + 256); } while (0)
#define RD_Q(f, ks) do { f.fq = tr_frag(aQ + ks * 1024, aQ + ks * 1024 + 256); } while (0)
        addr(0);
        RD_X(F[0], 0); RD_K(F[0], 0); RD_X1(F[0], 0); RD_V(F[0], 0); RD_Q(F[0], 0); SB();
#pragma unroll
        for (int n = 0; n < 28; ++n) {
            Frag& c = F[n & 1];
            Frag& x = F[(n + 1) & 1];
            const int nks = (n + 1) & 3;
            const bool more = n + 1 < 28;
            kT[0] = MFMA(c.fk, c.x0, kT[0]); SB();
            if (more && nks == 0) { LGKM_BAR(); addr((n + 1) >> 2); SB(); }
            if (more) { RD_X(x, nks); RD_K(x, nks); } SB();
            kT[1] = MFMA(c.fk, c.x1, kT[1]); SB();
            if (more) { RD_X1(x, nks); } SB();
            vv[0] = MFMA(c.x0, c.fv, vv[0]); SB();
            if (more) { RD_V(x, nks); } SB();
            vv[1] = MFMA(c.x1, c.fv, vv[1]); SB();
            if (more) { RD_Q(x, nks); } SB();
            qT[0] = MFMA(c.fq, c.x0, qT[0]); SB();
            qT[1] = MFMA(c.fq, c.x1, qT[1]); SB();
        }
    }
    STAMP(3);

    const unsigned phiW = L_PHI + cw * 8192;
    bf16x8 vp[2][2];
#pragma unroll
    for (int b = 0; b < 2; ++b)
#pragma unroll
        for (int s = 0; s < 2; ++s) {
            u32x4 t = __builtin_bit_cast(u32x4, pack8(vv[b], s));
            if (r == 28) t = (u32x4){ONE2, ONE2, ONE2, ONE2};
            vp[b][s] = __builtin_bit_cast(bf16x8, t);
        }
#pragma unroll
    for (int b = 0; b < 2; ++b) {
        f32x16 pk = MFMA(aphi[0], pack8(kT[b], 0), zero16);
        pk = MFMA(aphi[1], pack8(kT[b], 1), pk);
        float ep[14], en[14];
        exp_pair(pk, ep, en);
        f32x2 sm = {ep[0], en[0]};
#pragma unroll
        for (int i = 1; i < 14; ++i) sm += (f32x2){ep[i], en[i]};
        const float ip = __builtin_amdgcn_rcpf(xhalf_sum(sm[0])), in = __builtin_amdgcn_rcpf(xhalf_sum(sm[1]));
        const unsigned base = phiW + b * 4096 + r * 64;
#pragma unroll
        for (int g = 0; g < 4; ++g) {
            const unsigned a = base + 8 * ((2 * g + h2) ^ ((r >> 1) & 7));
            const u32x2 dp = {cvtpk(ep[4 * g] * ip, ep[4 * g + 1] * ip), g < 3 ? cvtpk(ep[(4 * g + 2) % 14] * ip, ep[(4 * g + 3) % 14] * ip) : 0u};
            const u32x2 dn = {cvtpk(en[4 * g] * in, en[4 * g + 1] * in), g < 3 ? cvtpk(en[(4 * g + 2) % 14] * in, en[(4 * g + 3) % 14] * in) : 0u};
            *(LAS u32x2*)lds_at(a) = dp;
            *(LAS u32x2*)lds_at(a + 2048) = dn;
        }
    }
    f32x16 kvp = zero16, kvn = zero16;
    {
        const unsigned t0 = 4 * h2 + q4;
        const unsigned a0 = phiW + t0 * 64 + 8 * (cq ^ ((t0 >> 1) & 7));
        const unsigned a1 = (a0 ^ 32) + 8 * 64;
#pragma unroll
        for (int b = 0; b < 2; ++b)
#pragma unroll
            for (int s = 0; s < 2; ++s) {
                const unsigned o = b * 4096 + s * 1024;
                kvp = MFMA(tr_frag(a0 + o, a1 + o), vp[b][s], kvp);
                kvn = MFMA(tr_frag(a0 + o + 2048, a1 + o + 2048), vp[b][s], kvn);
            }
    }
    asm volatile("s_waitcnt lgkmcnt(0)" ::: "memory");
#pragma unroll
    for (int f = 0; f < 4; ++f)
#pragma unroll
        for (int hh = 0; hh < 2; ++hh) {
            const f32x16& t = f < 2 ? kvp : kvn;
            const int o = 8 * (f & 1) + 4 * hh;
            *(LAS f32x4*)lds_at(phiW + f * 2048 + hh * 1024 + lane * 16) = (f32x4){t[o], t[o + 1], t[o + 2], t[o + 3]};
        }
    LGKM_BAR();
    STAMP(5);

    bf16x8 eq[2][2][2];
    float gs[2];
#pragma unroll
    for (int b = 0; b < 2; ++b) {
        f32x16 pq = MFMA(aphi[0], pack8(qT[b], 0), zero16);
        pq = MFMA(aphi[1], pack8(qT[b], 1), pq);
        float ep[14], en[14];
        exp_pair(pq, ep, en);
        const u32x4 p0 = {cvtpk(ep[0], ep[1]), cvtpk(ep[2], ep[3]), cvtpk(ep[4], ep[5]), cvtpk(ep[6], ep[7])};
        const u32x4 p1 = {cvtpk(ep[8], ep[9]), cvtpk(ep[10], ep[11]), cvtpk(ep[12], ep[13]), 0u};
        const u32x4 n0 = {cvtpk(en[0], en[1]), cvtpk(en[2], en[3]), cvtpk(en[4], en[5]), cvtpk(en[6], en[7])};
        const u32x4 n1 = {cvtpk(en[8], en[9]), cvtpk(en[10], en[11]), cvtpk(en[12], en[13]), 0u};
        eq[b][0][0] = __builtin_bit_cast(bf16x8, p0); eq[b][0][1] = __builtin_bit_cast(bf16x8, p1);
        eq[b][1][0] = __builtin_bit_cast(bf16x8, n0); eq[b][1][1] = __builtin_bit_cast(bf16x8, n1);
        const float lg = swap_hi(qT[b][12]);
        gs[b] = __builtin_amdgcn_rcpf(1.f + __builtin_amdgcn_exp2f(br ? lg * L2E : -lg * L2E));
    }
#pragma unroll
    for (int b = 0; b < 2; ++b) asm volatile("" : "+v"(eq[b][0][0]), "+v"(eq[b][0][1]), "+v"(eq[b][1][0]), "+v"(eq[b][1][1]), "+v"(gs[b]));
    const u64 tag = *(const GAS u64*)((const char*)p.ws + WS_EPOCH) + 1;
    STAMP(6);
    LGKM_BAR();
    STAMP(8);
    {
        bf16x8 akv[4];
#pragma unroll
        for (int f = 0; f < 4; ++f) akv[f] = lds_b128(L_KVA + f * 1024 + lane * 16);
#pragma unroll
        for (int b = 0; b < 2; ++b) {
            f32x16 np = MFMA(akv[0], eq[b][0][0], zero16); np = MFMA(akv[1], eq[b][0][1], np);
            f32x16 nn = MFMA(akv[2], eq[b][1][0], zero16); nn = MFMA(akv[3], eq[b][1][1], nn);
            const float ia = __builtin_amdgcn_rcpf(np[15]), ib = __builtin_amdgcn_rcpf(nn[15]);
            const float den = np[14] * ia + nn[14] * ib + 1e-6f;
            const float sc = gs[b] * __builtin_amdgcn_rcpf(den);
            const float ca = ia * sc, cb = ib * sc;
            float o[14];
#pragma unroll
            for (int i = 0; i < 14; ++i) o[i] = np[i] * ca + nn[i] * cb;
            const u32x4 o0 = {cvtpk(o[0], o[1]), cvtpk(o[2], o[3]), cvtpk(o[4], o[5]), cvtpk(o[6], o[7])};
            const u32x4 o1 = {cvtpk(o[8], o[9]), cvtpk(o[10], o[11]), cvtpk(o[12], o[13]), 0u};
            const unsigned so = WS_O + (((tb * 4 + cw) * 32 + bh) * 4 + b * 2) * 1024;
            __builtin_amdgcn_raw_buffer_store_b128(o0, rws, lane * 16, so, 16);
            __builtin_amdgcn_raw_buffer_store_b128(o1, rws, lane * 16, so + 1024, 16);
        }
    }
    STAMP(9);
    WAIT_VM(0);
    if (lane == 0) __hip_atomic_store((GAS u64*)((char*)p.ws + WS_F2 + ((tb * 4 + cw) * 32 + bh) * 128), tag, __ATOMIC_RELAXED, __HIP_MEMORY_SCOPE_AGENT);
    STAMP(10);
    if (!has_out) return;

    const unsigned tk = cw & 1, kh = cw >> 1;
    wait_flags((const GAS u64*)((const char*)p.ws + WS_F2 + (og * 32 + kh * 16 + (lane & 15)) * 128), tag, lane < 16);
    STAMP(11);
    if (blockIdx.x == 0 && tid == 0) __hip_atomic_store((GAS u64*)((char*)p.ws + WS_EPOCH), tag, __ATOMIC_RELAXED, __HIP_MEMORY_SCOPE_AGENT);
    f32x16 mine = zero16, other = zero16;
    {
        u32x4 of[16][2];
#pragma unroll
        for (int i = 0; i < 16; ++i)
#pragma unroll
            for (int s = 0; s < 2; ++s)
                of[i][s] = __builtin_amdgcn_raw_buffer_load_b128(rws, lane * 16, WS_O + (((og * 32 + kh * 16 + i) * 2 + tk) * 2 + s) * 1024, 16);
        SB();
        const unsigned colp = 32 * blk16 + 8 * p4;
        const unsigned r0 = kh * 448 + 14 * h2 + q4, r1 = kh * 448 + 14 * h2 + (q4 < 1 ? q4 : 1);
        unsigned a0 = L_WO + r0 * 128 + 64 * (kh ^ ((r0 >> 1) & 1)) + colp;
        unsigned a1 = L_WO + r1 * 128 + 64 * (kh ^ ((r1 >> 1) & 1)) + colp;
        unsigned a2 = a0 ^ 64, a3 = a1 ^ 64;
        asm volatile("" : "+v"(a0), "+v"(a1), "+v"(a2), "+v"(a3));
        bf16x8 A[2][4];
        auto rd = [&](bf16x8 (&f)[4], int i) {
            const unsigned o = i * 28 * 128;
            f[0] = tr_frag(a0 + o, a0 + o + 512); f[1] = tr_frag(a2 + o, a2 + o + 512);
            f[2] = tr_frag(a0 + o + 1024, a1 + o + 1536); f[3] = tr_frag(a2 + o + 1024, a3 + o + 1536);
        };
        rd(A[0], 0); SB();
#pragma unroll
        for (int i = 0; i < 16; ++i) {
            if (i + 1 < 16) rd(A[(i + 1) & 1], i + 1);
            SB();
            const bf16x8 b0 = __builtin_bit_cast(bf16x8, of[i][0]), b1 = __builtin_bit_cast(bf16x8, of[i][1]);
            mine = MFMA(A[i & 1][0], b0, mine); other = MFMA(A[i & 1][1], b0, other);
            mine = MFMA(A[i & 1][2], b1, mine); other = MFMA(A[i & 1][3], b1, other);
            SB();
        }
    }
    STAMP(12);
#pragma unroll
    for (int g = 0; g < 4; ++g)
        *(LAS f32x4*)lds_at(L_PHI + cw * 4096 + g * 1024 + lane * 16) = (f32x4){other[4 * g], other[4 * g + 1], other[4 * g + 2], other[4 * g + 3]};
    LGKM_BAR();
    {
        unsigned d[8];
#pragma unroll
        for (int g = 0; g < 4; ++g) {
            const f32x4 t = *(const LAS f32x4*)lds_at(L_PHI + (cw ^ 2) * 4096 + g * 1024 + lane * 16);
            d[2 * g] = cvtpk(mine[4 * g] + t[0], mine[4 * g + 1] + t[1]);
            d[2 * g + 1] = cvtpk(mine[4 * g + 2] + t[2], mine[4 * g + 3] + t[3]);
        }
        const rsrc_t rout = mk_rsrc(p.out, S_ * XROW);
        const unsigned vo = r * XROW + h2 * 16, so = (og * 64 + tk * 32) * XROW + oc * 128 + kh * 64;
#pragma unroll
        for (int k = 0; k < 4; k += 2) {
            auto sx0 = __builtin_amdgcn_permlane32_swap(d[2 * k], d[2 * k + 2], false, false);
            auto sx1 = __builtin_amdgcn_permlane32_swap(d[2 * k + 1], d[2 * k + 3], false, false);
            const u32x4 o = {sx0[0], sx1[0], sx0[1], sx1[1]};
            __builtin_amdgcn_raw_buffer_store_b128(o, rout, vo, so + 16 * k, 0);
        }
    }
    STAMP(13);
}

extern "C" void kernel_launch(void* const* d_in, const int* in_sizes, int n_in, void* d_out, int out_size, void* d_ws, size_t ws_size,
                              hipStream_t stream) {
    Params p;
    p.x = d_in[0]; p.wqkvA = d_in[1]; p.wphiA = d_in[2]; p.woA = d_in[3];
    p.wqkvB = d_in[5]; p.wphiB = d_in[6]; p.woB = d_in[7]; p.gate = d_in[8];
    p.out = d_out; p.ws = d_ws;
    (void)hipFuncSetAttribute((const void*)hedgehog_mix_kernel, hipFuncAttributeMaxDynamicSharedMemorySize, L_TOTAL);
    hedgehog_mix_kernel<<<256, 512, L_TOTAL, stream>>>(p);
}
```

```cpp
#include <hip/hip_runtime.h>
#include <stdint.h>

typedef short bf16x8 __attribute__((ext_vector_type(8)));
typedef short s16x4 __attribute__((ext_vector_type(4)));
typedef short v4i16_t __attribute__((ext_vector_type(4)));
typedef __bf16 bf2_t __attribute__((ext_vector_type(2)));
typedef float f32x2 __attribute__((ext_vector_type(2)));
typedef float f32x4 __attribute__((ext_vector_type(4)));
typedef float f32x16 __attribute__((ext_vector_type(16)));
typedef unsigned u32x2 __attribute__((ext_vector_type(2)));
typedef unsigned u32x4 __attribute__((ext_vector_type(4)));
typedef unsigned long long u64;
#define LAS __attribute__((address_space(3)))
#define GAS __attribute__((address_space(1)))
#define DI __device__ __forceinline__
#define MFMA(a, b, c) __builtin_amdgcn_mfma_f32_32x32x16_bf16((a), (b), (c), 0, 0, 0)

constexpr int S_ = 2048, D_ = 448, DH = 28;
constexpr unsigned XROW = D_ * 2;
constexpr unsigned WROW = 3 * D_ * 2;
constexpr unsigned WS_EPOCH = 0;
constexpr unsigned WS_F1 = 4096;
constexpr unsigned WS_F2 = WS_F1 + 32 * 4 * 8 * 128;
constexpr unsigned WS_KVP = WS_F2 + 32 * 32 * 128;
constexpr unsigned WS_O = WS_KVP + 32 * 8 * 4 * 1024;
constexpr unsigned WS_END = WS_O + 32 * 32 * 4 * 1024;
constexpr unsigned WS_STAMP = 8u << 20;
constexpr unsigned SLOT = 45056, SLOT_W = 32768;
constexpr unsigned L_GT = 0;
constexpr unsigned L_RING = 4096;
constexpr unsigned L_WPHI = L_RING + 3 * SLOT;
constexpr unsigned L_PHI = L_RING;
constexpr unsigned L_KVA = L_RING + SLOT_W;
constexpr unsigned L_WO = L_RING + SLOT;
constexpr unsigned L_TOTAL = 163840;
static_assert(L_WPHI + 2048 <= L_TOTAL, "lds");
static_assert(L_WO + 896 * 128 == L_TOTAL, "lds");

struct Params {
    const void* x; const void* wqkvA; const void* wqkvB; const void* wphiA; const void* wphiB;
    const void* woA; const void* woB; const void* gate; void* out; void* ws;
};

typedef __amdgpu_buffer_rsrc_t rsrc_t;
DI rsrc_t mk_rsrc(const void* p, unsigned bytes) { return __builtin_amdgcn_make_buffer_rsrc((void*)p, (short)0, (int)bytes, 0x00020000); }
DI unsigned rfl(unsigned v) { return (unsigned)__builtin_amdgcn_readfirstlane((int)v); }
DI void dma16(rsrc_t r, unsigned voff, unsigned soff, unsigned lds) {
    asm volatile("s_mov_b32 m0, %0\n\ts_nop 0\n\tbuffer_load_dwordx4 %1, %2, %3 offen lds" ::"s"(lds), "v"(voff), "s"(r), "s"(soff) : "memory");
}
#define WAIT_VM(n) asm volatile("s_waitcnt vmcnt(" #n ")" ::: "memory")
#define WAIT_VM_LGKM_BAR(n) asm volatile("s_waitcnt vmcnt(" #n ") lgkmcnt(0)\n\ts_barrier" ::: "memory")
#define LGKM_BAR() asm volatile("s_waitcnt lgkmcnt(0)\n\ts_barrier" ::: "memory")

extern __shared__ __attribute__((aligned(16))) char smem_dyn[];
DI LAS char* lds_at(unsigned off) { return (LAS char*)(uintptr_t)off; }
DI bf16x8 lds_b128(unsigned a) { return *(const LAS bf16x8*)lds_at(a); }
DI s16x4 lds_tr(unsigned a) { return __builtin_bit_cast(s16x4, __builtin_amdgcn_ds_read_tr16_b64_v4i16((LAS v4i16_t*)lds_at(a))); }
DI bf16x8 tr_frag(unsigned a0, unsigned a1) { const s16x4 lo = lds_tr(a0), hi = lds_tr(a1); return __builtin_shufflevector(lo, hi, 0, 1, 2, 3, 4, 5, 6, 7); }
DI unsigned cvtpk(float lo, float hi) { const f32x2 v = {lo, hi}; return __builtin_bit_cast(unsigned, __builtin_convertvector(v, bf2_t)); }
DI bf16x8 pack8(const f32x16& x, int s) {
    const u32x4 p = {cvtpk(x[8 * s], x[8 * s + 1]), cvtpk(x[8 * s + 2], x[8 * s + 3]), cvtpk(x[8 * s + 4], x[8 * s + 5]), cvtpk(x[8 * s + 6], x[8 * s + 7])};
    return __builtin_bit_cast(bf16x8, p);
}
DI float swap_lo(float v) { auto r = __builtin_amdgcn_permlane32_swap(__float_as_uint(v), __float_as_uint(v), false, false); return __uint_as_float(r[0]); }
DI float swap_hi(float v) { auto r = __builtin_amdgcn_permlane32_swap(__float_as_uint(v), __float_as_uint(v), false, false); return __uint_as_float(r[1]); }
DI float xhalf_max(float v) { auto r = __builtin_amdgcn_permlane32_swap(__float_as_uint(v), __float_as_uint(v), false, false); return __builtin_fmaxf(__uint_as_float(r[0]), __uint_as_float(r[1])); }
DI float xhalf_min(float v) { auto r = __builtin_amdgcn_permlane32_swap(__float_as_uint(v), __float_as_uint(v), false, false); return __builtin_fminf(__uint_as_float(r[0]), __uint_as_float(r[1])); }
DI float xhalf_sum(float v) { auto r = __builtin_amdgcn_permlane32_swap(__float_as_uint(v), __float_as_uint(v), false, false); return __uint_as_float(r[0]) + __uint_as_float(r[1]); }
constexpr float L2E = 1.4426950408889634f;
constexpr unsigned ONE2 = 0x3F803F80u;
constexpr unsigned SPIN_CAP = 1u << 16;

#define STAMP(i) do { } while (0)

DI void exp_pair(const f32x16& pp, float (&ep)[14], float (&en)[14]) {
    float mx = __builtin_fmaxf(pp[0], pp[1]), mn = __builtin_fminf(pp[0], pp[1]);
#pragma unroll
    for (int i = 2; i < 14; i += 2) { mx = __builtin_fmaxf(__builtin_fmaxf(mx, pp[i]), pp[i + 1]); mn = __builtin_fminf(__builtin_fminf(mn, pp[i]), pp[i + 1]); }
    mx = xhalf_max(mx); mn = xhalf_min(mn);
    const float cp = -mx * L2E, cn = mn * L2E;
#pragma unroll
    for (int i = 0; i < 14; ++i) { ep[i] = __builtin_amdgcn_exp2f(__builtin_fmaf(pp[i], L2E, cp)); en[i] = __builtin_amdgcn_exp2f(__builtin_fmaf(pp[i], -L2E, cn)); }
}

DI void wait_flags(const GAS u64* f, u64 tag, bool active) {
    for (unsigned spins = 0; spins < SPIN_CAP; ++spins) {
        u64 v = tag;
        if (active) v = __hip_atomic_load(f, __ATOMIC_RELAXED, __HIP_MEMORY_SCOPE_AGENT);
        if (__all(v == tag)) break;
        __builtin_amdgcn_s_sleep(1);
    }
    asm volatile("" ::: "memory");
}

#define DLWAIT() do { while (__builtin_amdgcn_s_memrealtime() - dl_t0 < 3000ull) __builtin_amdgcn_s_sleep(1); } while (0)
#define SB() __builtin_amdgcn_sched_barrier(0)
struct Frag { bf16x8 x0, x1, fk, fv, fq; };

__global__ void __launch_bounds__(512, 2) hedgehog_mix_kernel(Params p) {
    const int tid = threadIdx.x, lane = tid & 63;
    const unsigned w = rfl(tid >> 6), cw = w & 3;
    const int r = lane & 31, h2 = lane >> 5, i16 = lane & 15, q4 = i16 >> 2, p4 = i16 & 3, blk16 = (lane >> 4) & 1;
    const unsigned xcd = blockIdx.x & 7, j = blockIdx.x >> 3;
    const unsigned tb = (xcd & 1) * 4 + (j & 3), bh = (xcd >> 1) * 8 + (j >> 2), br = bh >> 4, h = bh & 15;
    const unsigned og = xcd * 4 + j / 7, oc = j % 7;
    const bool has_out = j < 28;
    const unsigned long long dl_t0 = __builtin_amdgcn_s_memrealtime();
    STAMP(0);
    if (blockIdx.x == 0 && tid == 0) { const u64 tg = *(const GAS u64*)((const char*)p.ws + WS_EPOCH) + 1;
        __hip_atomic_store((GAS u64*)((char*)p.ws + 256), (tg << 40) | (dl_t0 & 0xFFFFFFFFFFull), __ATOMIC_RELAXED, __HIP_MEMORY_SCOPE_AGENT); }
    const rsrc_t rws = mk_rsrc(p.ws, WS_END);
    const f32x16 zero16 = {};

    if (w >= 4) {
        const rsrc_t rx = mk_rsrc(p.x, S_ * XROW);
        const rsrc_t rw = mk_rsrc(br ? p.wqkvB : p.wqkvA, D_ * WROW);
        const unsigned vx0 = (lane >> 3) * XROW + 16 * ((lane & 7) ^ (lane >> 4));
        const unsigned vx1 = (lane >> 3) * XROW + 16 * ((lane & 7) ^ (4 + (lane >> 4)));
        const unsigned vw = (lane >> 2) * WROW + 16 * (lane & 3);
        const unsigned sx = (tb * 256 + cw * 64) * XROW;
        const unsigned sw = cw * 16 * WROW + h * 56 - 8 * (h & 1);
        auto issue = [&](unsigned s, unsigned slot) {
            const unsigned lb = L_RING + slot * SLOT;
            dma16(rw, vw, sw + s * 64 * WROW + 896, lb + SLOT_W + cw * 1024);
            dma16(rw, vw, sw + s * 64 * WROW + 1792, lb + SLOT_W + 4096 + cw * 1024);
            dma16(rw, vw, sw + s * 64 * WROW, lb + SLOT_W + 8192 + cw * 1024);
#pragma unroll
            for (int pc = 0; pc < 8; ++pc) dma16(rx, (pc & 1) ? vx1 : vx0, sx + pc * 8 * XROW + s * 128, lb + cw * 8192 + pc * 1024);
        };
        issue(0, 0); issue(1, 1); issue(2, 2);
        STAMP(1);
        WAIT_VM_LGKM_BAR(22);
        STAMP(2);
        WAIT_VM_LGKM_BAR(11);
        issue(3, 0); WAIT_VM_LGKM_BAR(11);
        issue(4, 1); WAIT_VM_LGKM_BAR(11);
        issue(5, 2); WAIT_VM_LGKM_BAR(11);
        issue(6, 0); WAIT_VM_LGKM_BAR(11);
        WAIT_VM_LGKM_BAR(0);
        STAMP(3);
        const u64 tag = *(const GAS u64*)((const char*)p.ws + WS_EPOCH) + 1;
        if (has_out) {
            const rsrc_t ro = mk_rsrc((cw >> 1) ? p.woB : p.woA, D_ * XROW);
            const unsigned vo = (lane >> 3) * XROW + 16 * ((lane & 7) ^ (4 * ((lane >> 4) & 1)));
#pragma unroll 4
            for (unsigned n = 0; n < 28; ++n) {
                const unsigned R = ((cw & 1) * 28 + n) * 8;
                dma16(ro, vo, R * XROW + oc * 128, L_WO + ((cw >> 1) * 448 + R) * 128);
            }
        }
        STAMP(4);
        LGKM_BAR();
        STAMP(5);
        {
            f32x4 lo = {0.f, 0.f, 0.f, 0.f}, hi = lo;
#pragma unroll
            for (int u = 0; u < 4; ++u) {
                lo += *(const LAS f32x4*)lds_at(L_PHI + u * 8192 + cw * 2048 + lane * 16);
                hi += *(const LAS f32x4*)lds_at(L_PHI + u * 8192 + cw * 2048 + 1024 + lane * 16);
            }
            const u32x4 o = {cvtpk(lo[0], lo[1]), cvtpk(lo[2], lo[3]), cvtpk(hi[0], hi[1]), cvtpk(hi[2], hi[3])};
            __builtin_amdgcn_raw_buffer_store_b128(o, rws, lane * 16, WS_KVP + ((bh * 8 + tb) * 4 + cw) * 1024, 16);
        }
        WAIT_VM(0);
        if (lane == 0) __hip_atomic_store((GAS u64*)((char*)p.ws + WS_F1 + ((bh * 4 + cw) * 8 + tb) * 128), tag, __ATOMIC_RELAXED, __HIP_MEMORY_SCOPE_AGENT);
        STAMP(6);
        wait_flags((const GAS u64*)((const char*)p.ws + WS_F1 + ((bh * 4 + cw) * 8 + (lane & 7)) * 128), tag, lane < 8);
        STAMP(7);
        {
            const int hr = (r >> 2) & 1, reg = (r & 3) + 4 * (r >> 3);
            const int col = reg < 14 ? 14 * hr + reg : 28;
            const unsigned vo = (h2 * 32 + col) * 16;
            u32x4 part[8];
#pragma unroll
            for (int u = 0; u < 8; ++u) part[u] = __builtin_amdgcn_raw_buffer_load_b128(rws, vo, WS_KVP + ((bh * 8 + u) * 4 + cw) * 1024, 16);
            SB();
            float acc[8];
#pragma unroll
            for (int i = 0; i < 8; ++i) acc[i] = 0.f;
#pragma unroll
            for (int u = 0; u < 8; ++u)
#pragma unroll
                for (int i = 0; i < 4; ++i) { acc[2 * i] += __uint_as_float(part[u][i] << 16); acc[2 * i + 1] += __uint_as_float(part[u][i] & 0xffff0000u); }
            u32x4 o = {cvtpk(acc[0], acc[1]), cvtpk(acc[2], acc[3]), cvtpk(acc[4], acc[5]), cvtpk(acc[6], acc[7])};
            if (reg == 15) o = (u32x4){ONE2, ONE2, ONE2, ONE2};
            *(LAS u32x4*)lds_at(L_KVA + cw * 1024 + lane * 16) = o;
        }
        STAMP(8);
        WAIT_VM_LGKM_BAR(0);
        return;
    }

    {
        const rsrc_t rg = mk_rsrc(p.gate, XROW);
        const rsrc_t rp = mk_rsrc(br ? p.wphiB : p.wphiA, 16 * DH * DH * 2);
        const unsigned g0 = __builtin_amdgcn_raw_buffer_load_b16(rg, tid * 2, 0, 0), g1 = __builtin_amdgcn_raw_buffer_load_b16(rg, tid * 2 + 512, 0, 0);
        const u32x4 wp = __builtin_amdgcn_raw_buffer_load_b128(rp, tid * 16, h * (DH * DH * 2), 0);
        *(LAS u32x2*)lds_at(L_GT + (tid & 63) * 64 + (tid >> 6) * 8) = (u32x2){g0, 0u};
        *(LAS u32x2*)lds_at(L_GT + (tid & 63) * 64 + (4 + (tid >> 6)) * 8) = (u32x2){g1, 0u};
        if (tid < 128) *(LAS u32x4*)lds_at(L_WPHI + tid * 16) = wp;
    }
    LGKM_BAR();
    STAMP(2);
    bf16x8 aphi[2];
    {
        const int hr = (r >> 2) & 1, reg = (r & 3) + 4 * (r >> 3), f = 14 * hr + (reg < 14 ? reg : 0);
#pragma unroll
        for (int s = 0; s < 2; ++s)
#pragma unroll
            for (int jj = 0; jj < 8; ++jj) {
                const int d = 16 * s + 8 * (jj >> 2) + 4 * h2 + (jj & 3);
                const bool ok = reg < 14 && d < DH;
                const unsigned short v = *(const LAS unsigned short*)lds_at(L_WPHI + 2 * ((d < DH ? d : 0) * DH + f));
                aphi[s][jj] = ok ? (short)v : (short)0;
            }
    }
    const int cq = 4 * blk16 + p4;
    const unsigned rowpart = (8 * h2 + q4) * 64;
    const unsigned laneW = L_RING + SLOT_W + rowpart + ((8 * (h & 1) + 8 * cq) & 63);
    const unsigned laneX = L_RING + cw * 8192 + r * 128 + 16 * (h2 ^ ((r >> 1) & 7));
    f32x16 kT[2], qT[2], vv[2];
#pragma unroll
    for (int b = 0; b < 2; ++b) { kT[b] = zero16; qT[b] = zero16; vv[b] = zero16; }
    {
        unsigned aX[4], aW, aQ;
        auto addr = [&](unsigned s) {
            const unsigned lb = (s % 3) * SLOT;
#pragma unroll
            for (int ks = 0; ks < 4; ++ks) { aX[ks] = (laneX ^ (32 * ks)) + lb; asm volatile("" : "+v"(aX[ks])); }
            aW = laneW + lb; aQ = cq == 7 ? L_GT + rowpart + 8 * s : aW + 8192;
            asm volatile("" : "+v"(aW), "+v"(aQ));
        };
        Frag F[2];
#define RD_X(f, ks) do { f.x0 = lds_b128(aX[ks]); } while (0)
#define RD_X1(f, ks) do { f.x1 = lds_b128(aX[ks] + 4096); } while (0)
#define RD_K(f, ks) do { f.fk = tr_frag(aW + ks * 1024, aW + ks * 1024 + 256); } while (0)
#define RD_V(f, ks) do { f.fv = tr_frag(aW + 4096 + ks * 1024, aW + 4096 + ks * 1024 + 256); } while (0)
#define RD_Q(f, ks) do { f.fq = tr_frag(aQ + ks * 1024, aQ + ks * 1024 + 256); } while (0)
        addr(0);
        RD_X(F[0], 0); RD_K(F[0], 0); RD_X1(F[0], 0); RD_V(F[0], 0); RD_Q(F[0], 0); SB();
#pragma unroll
        for (int n = 0; n < 28; ++n) {
            Frag& c = F[n & 1];
            Frag& x = F[(n + 1) & 1];
            const int nks = (n + 1) & 3;
            const bool more = n + 1 < 28;
            kT[0] = MFMA(c.fk, c.x0, kT[0]); SB();
            if (more && nks == 0) { LGKM_BAR(); addr((n + 1) >> 2); SB(); }
            if (more) { RD_X(x, nks); RD_K(x, nks); } SB();
            kT[1] = MFMA(c.fk, c.x1, kT[1]); SB();
            if (more) { RD_X1(x, nks); } SB();
            vv[0] = MFMA(c.x0, c.fv, vv[0]); SB();
            if (more) { RD_V(x, nks); } SB();
            vv[1] = MFMA(c.x1, c.fv, vv[1]); SB();
            if (more) { RD_Q(x, nks); } SB();
            qT[0] = MFMA(c.fq, c.x0, qT[0]); SB();
            qT[1] = MFMA(c.fq, c.x1, qT[1]); SB();
        }
    }
    STAMP(3);

    const unsigned phiW = L_PHI + cw * 8192;
    bf16x8 vp[2][2];
#pragma unroll
    for (int b = 0; b < 2; ++b)
#pragma unroll
        for (int s = 0; s < 2; ++s) {
            u32x4 t = __builtin_bit_cast(u32x4, pack8(vv[b], s));
            if (r == 28) t = (u32x4){ONE2, ONE2, ONE2, ONE2};
            vp[b][s] = __builtin_bit_cast(bf16x8, t);
        }
#pragma unroll
    for (int b = 0; b < 2; ++b) {
        f32x16 pk = MFMA(aphi[0], pack8(kT[b], 0), zero16);
        pk = MFMA(aphi[1], pack8(kT[b], 1), pk);
        float ep[14], en[14];
        exp_pair(pk, ep, en);
        f32x2 sm = {ep[0], en[0]};
#pragma unroll
        for (int i = 1; i < 14; ++i) sm += (f32x2){ep[i], en[i]};
        const float ip = __builtin_amdgcn_rcpf(xhalf_sum(sm[0])), in = __builtin_amdgcn_rcpf(xhalf_sum(sm[1]));
        const unsigned base = phiW + b * 4096 + r * 64;
#pragma unroll
        for (int g = 0; g < 4; ++g) {
            const unsigned a = base + 8 * ((2 * g + h2) ^ ((r >> 1) & 7));
            const u32x2 dp = {cvtpk(ep[4 * g] * ip, ep[4 * g + 1] * ip), g < 3 ? cvtpk(ep[(4 * g + 2) % 14] * ip, ep[(4 * g + 3) % 14] * ip) : 0u};
            const u32x2 dn = {cvtpk(en[4 * g] * in, en[4 * g + 1] * in), g < 3 ? cvtpk(en[(4 * g + 2) % 14] * in, en[(4 * g + 3) % 14] * in) : 0u};
            *(LAS u32x2*)lds_at(a) = dp;
            *(LAS u32x2*)lds_at(a + 2048) = dn;
        }
    }
    f32x16 kvp = zero16, kvn = zero16;
    {
        const unsigned t0 = 4 * h2 + q4;
        const unsigned a0 = phiW + t0 * 64 + 8 * (cq ^ ((t0 >> 1) & 7));
        const unsigned a1 = (a0 ^ 32) + 8 * 64;
#pragma unroll
        for (int b = 0; b < 2; ++b)
#pragma unroll
            for (int s = 0; s < 2; ++s) {
                const unsigned o = b * 4096 + s * 1024;
                kvp = MFMA(tr_frag(a0 + o, a1 + o), vp[b][s], kvp);
                kvn = MFMA(tr_frag(a0 + o + 2048, a1 + o + 2048), vp[b][s], kvn);
            }
    }
    asm volatile("s_waitcnt lgkmcnt(0)" ::: "memory");
#pragma unroll
    for (int f = 0; f < 4; ++f)
#pragma unroll
        for (int hh = 0; hh < 2; ++hh) {
            const f32x16& t = f < 2 ? kvp : kvn;
            const int o = 8 * (f & 1) + 4 * hh;
            *(LAS f32x4*)lds_at(phiW + f * 2048 + hh * 1024 + lane * 16) = (f32x4){t[o], t[o + 1], t[o + 2], t[o + 3]};
        }
    LGKM_BAR();
    STAMP(5);

    bf16x8 eq[2][2][2];
    float gs[2];
#pragma unroll
    for (int b = 0; b < 2; ++b) {
        f32x16 pq = MFMA(aphi[0], pack8(qT[b], 0), zero16);
        pq = MFMA(aphi[1], pack8(qT[b], 1), pq);
        float ep[14], en[14];
        exp_pair(pq, ep, en);
        const u32x4 p0 = {cvtpk(ep[0], ep[1]), cvtpk(ep[2], ep[3]), cvtpk(ep[4], ep[5]), cvtpk(ep[6], ep[7])};
        const u32x4 p1 = {cvtpk(ep[8], ep[9]), cvtpk(ep[10], ep[11]), cvtpk(ep[12], ep[13]), 0u};
        const u32x4 n0 = {cvtpk(en[0], en[1]), cvtpk(en[2], en[3]), cvtpk(en[4], en[5]), cvtpk(en[6], en[7])};
        const u32x4 n1 = {cvtpk(en[8], en[9]), cvtpk(en[10], en[11]), cvtpk(en[12], en[13]), 0u};
        eq[b][0][0] = __builtin_bit_cast(bf16x8, p0); eq[b][0][1] = __builtin_bit_cast(bf16x8, p1);
        eq[b][1][0] = __builtin_bit_cast(bf16x8, n0); eq[b][1][1] = __builtin_bit_cast(bf16x8, n1);
        const float lg = swap_hi(qT[b][12]);
        gs[b] = __builtin_amdgcn_rcpf(1.f + __builtin_amdgcn_exp2f(br ? lg * L2E : -lg * L2E));
    }
#pragma unroll
    for (int b = 0; b < 2; ++b) asm volatile("" : "+v"(eq[b][0][0]), "+v"(eq[b][0][1]), "+v"(eq[b][1][0]), "+v"(eq[b][1][1]), "+v"(gs[b]));
    const u64 tag = *(const GAS u64*)((const char*)p.ws + WS_EPOCH) + 1;
    STAMP(6);
    LGKM_BAR();
    STAMP(8);
    {
        bf16x8 akv[4];
#pragma unroll
        for (int f = 0; f < 4; ++f) akv[f] = lds_b128(L_KVA + f * 1024 + lane * 16);
#pragma unroll
        for (int b = 0; b < 2; ++b) {
            f32x16 np = MFMA(akv[0], eq[b][0][0], zero16); np = MFMA(akv[1], eq[b][0][1], np);
            f32x16 nn = MFMA(akv[2], eq[b][1][0], zero16); nn = MFMA(akv[3], eq[b][1][1], nn);
            const float ia = __builtin_amdgcn_rcpf(np[15]), ib = __builtin_amdgcn_rcpf(nn[15]);
            const float den = np[14] * ia + nn[14] * ib + 1e-6f;
            const float sc = gs[b] * __builtin_amdgcn_rcpf(den);
            const float ca = ia * sc, cb = ib * sc;
            float o[14];
#pragma unroll
            for (int i = 0; i < 14; ++i) o[i] = np[i] * ca + nn[i] * cb;
            const u32x4 o0 = {cvtpk(o[0], o[1]), cvtpk(o[2], o[3]), cvtpk(o[4], o[5]), cvtpk(o[6], o[7])};
            const u32x4 o1 = {cvtpk(o[8], o[9]), cvtpk(o[10], o[11]), cvtpk(o[12], o[13]), 0u};
            const unsigned so = WS_O + (((tb * 4 + cw) * 32 + bh) * 4 + b * 2) * 1024;
            __builtin_amdgcn_raw_buffer_store_b128(o0, rws, lane * 16, so, 16);
            __builtin_amdgcn_raw_buffer_store_b128(o1, rws, lane * 16, so + 1024, 16);
        }
    }
    STAMP(9);
    WAIT_VM(0);
    if (lane == 0) __hip_atomic_store((GAS u64*)((char*)p.ws + WS_F2 + ((tb * 4 + cw) * 32 + bh) * 128), tag, __ATOMIC_RELAXED, __HIP_MEMORY_SCOPE_AGENT);
    STAMP(10);
    if (!has_out) return;

    const unsigned tk = cw & 1, kh = cw >> 1;
    wait_flags((const GAS u64*)((const char*)p.ws + WS_F2 + (og * 32 + kh * 16 + (lane & 15)) * 128), tag, lane < 16);
    STAMP(11);
    if (blockIdx.x == 0 && tid == 0) __hip_atomic_store((GAS u64*)((char*)p.ws + WS_EPOCH), tag, __ATOMIC_RELAXED, __HIP_MEMORY_SCOPE_AGENT);
    f32x16 mine = zero16, other = zero16;
    {
        u32x4 of[16][2];
#pragma unroll
        for (int i = 0; i < 16; ++i)
#pragma unroll
            for (int s = 0; s < 2; ++s)
                of[i][s] = __builtin_amdgcn_raw_buffer_load_b128(rws, lane * 16, WS_O + (((og * 32 + kh * 16 + i) * 2 + tk) * 2 + s) * 1024, 16);
        SB();
        const unsigned colp = 32 * blk16 + 8 * p4;
        const unsigned r0 = kh * 448 + 14 * h2 + q4, r1 = kh * 448 + 14 * h2 + (q4 < 1 ? q4 : 1);
        unsigned a0 = L_WO + r0 * 128 + 64 * (kh ^ ((r0 >> 1) & 1)) + colp;
        unsigned a1 = L_WO + r1 * 128 + 64 * (kh ^ ((r1 >> 1) & 1)) + colp;
        unsigned a2 = a0 ^ 64, a3 = a1 ^ 64;
        asm volatile("" : "+v"(a0), "+v"(a1), "+v"(a2), "+v"(a3));
        bf16x8 A[2][4];
        auto rd = [&](bf16x8 (&f)[4], int i) {
            const unsigned o = i * 28 * 128;
            f[0] = tr_frag(a0 + o, a0 + o + 512); f[1] = tr_frag(a2 + o, a2 + o + 512);
            f[2] = tr_frag(a0 + o + 1024, a1 + o + 1536); f[3] = tr_frag(a2 + o + 1024, a3 + o + 1536);
        };
        rd(A[0], 0); SB();
#pragma unroll
        for (int i = 0; i < 16; ++i) {
            if (i + 1 < 16) rd(A[(i + 1) & 1], i + 1);
            SB();
            const bf16x8 b0 = __builtin_bit_cast(bf16x8, of[i][0]), b1 = __builtin_bit_cast(bf16x8, of[i][1]);
            mine = MFMA(A[i & 1][0], b0, mine); other = MFMA(A[i & 1][1], b0, other);
            mine = MFMA(A[i & 1][2], b1, mine); other = MFMA(A[i & 1][3], b1, other);
            SB();
        }
    }
    STAMP(12);
#pragma unroll
    for (int g = 0; g < 4; ++g)
        *(LAS f32x4*)lds_at(L_PHI + cw * 4096 + g * 1024 + lane * 16) = (f32x4){other[4 * g], other[4 * g + 1], other[4 * g + 2], other[4 * g + 3]};
    LGKM_BAR();
    {
        unsigned d[8];
#pragma unroll
        for (int g = 0; g < 4; ++g) {
            const f32x4 t = *(const LAS f32x4*)lds_at(L_PHI + (cw ^ 2) * 4096 + g * 1024 + lane * 16);
            d[2 * g] = cvtpk(mine[4 * g] + t[0], mine[4 * g + 1] + t[1]);
            d[2 * g + 1] = cvtpk(mine[4 * g + 2] + t[2], mine[4 * g + 3] + t[3]);
        }
        const rsrc_t rout = mk_rsrc(p.out, S_ * XROW);
        const unsigned vo = r * XROW + h2 * 16, so = (og * 64 + tk * 32) * XROW + oc * 128 + kh * 64;
#pragma unroll
        for (int k = 0; k < 4; k += 2) {
            auto sx0 = __builtin_amdgcn_permlane32_swap(d[2 * k], d[2 * k + 2], false, false);
            auto sx1 = __builtin_amdgcn_permlane32_swap(d[2 * k + 1], d[2 * k + 3], false, false);
            const u32x4 o = {sx0[0], sx1[0], sx0[1], sx1[1]};
            __builtin_amdgcn_raw_buffer_store_b128(o, rout, vo, so + 16 * k, 0);
        }
    }
    { const u64 gw = __hip_atomic_load((const GAS u64*)((const char*)p.ws + 256), __ATOMIC_RELAXED, __HIP_MEMORY_SCOPE_AGENT);
      const u64 g0 = (gw >> 40) == (tag & 0xFFFFFFull) ? (gw & 0xFFFFFFFFFFull) : (dl_t0 & 0xFFFFFFFFFFull);
      while (((__builtin_amdgcn_s_memrealtime() & 0xFFFFFFFFFFull) - g0) < 3000ull) __builtin_amdgcn_s_sleep(1); }
}

extern "C" void kernel_launch(void* const* d_in, const int* in_sizes, int n_in, void* d_out, int out_size, void* d_ws, size_t ws_size,
                              hipStream_t stream) {
    Params p;
    p.x = d_in[0]; p.wqkvA = d_in[1]; p.wphiA = d_in[2]; p.woA = d_in[3];
    p.wqkvB = d_in[5]; p.wphiB = d_in[6]; p.woB = d_in[7]; p.gate = d_in[8];
    p.out = d_out; p.ws = d_ws;
    (void)hipFuncSetAttribute((const void*)hedgehog_mix_kernel, hipFuncAttributeMaxDynamicSharedMemorySize, L_TOTAL);
    hedgehog_mix_kernel<<<256, 512, L_TOTAL, stream>>>(p);
}
```

```cpp
#define PREFETCH 1
#define PF_CODE 1
#include <hip/hip_runtime.h>
#include <stdint.h>

typedef short bf16x8 __attribute__((ext_vector_type(8)));
typedef short s16x4 __attribute__((ext_vector_type(4)));
typedef short v4i16_t __attribute__((ext_vector_type(4)));
typedef __bf16 bf2_t __attribute__((ext_vector_type(2)));
typedef float f32x2 __attribute__((ext_vector_type(2)));
typedef float f32x4 __attribute__((ext_vector_type(4)));
typedef float f32x16 __attribute__((ext_vector_type(16)));
typedef unsigned u32x2 __attribute__((ext_vector_type(2)));
typedef unsigned u32x4 __attribute__((ext_vector_type(4)));
typedef unsigned long long u64;
#define LAS __attribute__((address_space(3)))
#define GAS __attribute__((address_space(1)))
#define DI __device__ __forceinline__
#define MFMA(a, b, c) __builtin_amdgcn_mfma_f32_32x32x16_bf16((a), (b), (c), 0, 0, 0)

constexpr int S_ = 2048, D_ = 448, DH = 28;
constexpr unsigned XROW = D_ * 2;
constexpr unsigned WROW = 3 * D_ * 2;
constexpr unsigned WS_EPOCH = 0;
constexpr unsigned WS_F1 = 4096;
constexpr unsigned WS_F2 = WS_F1 + 32 * 4 * 8 * 128;
constexpr unsigned WS_KVP = WS_F2 + 32 * 32 * 128;
constexpr unsigned WS_O = WS_KVP + 32 * 8 * 4 * 1024;
constexpr unsigned WS_END = WS_O + 32 * 32 * 4 * 1024;
constexpr unsigned WS_STAMP = 8u << 20;
constexpr unsigned SLOT = 40960, SLOT_W = 32768;
constexpr unsigned L_B = 0, L_A = SLOT, L_C = 2 * SLOT;
constexpr unsigned L_WQ = 3 * SLOT;
constexpr unsigned L_GT = L_WQ + 448 * 64;
constexpr unsigned L_WPHI = L_GT + 4096;
constexpr unsigned L_SCR = L_WPHI + 2048;
constexpr unsigned L_XFREE = L_SCR + 512;
constexpr unsigned L_PHI = L_B;
constexpr unsigned L_KVA = L_B + SLOT_W;
constexpr unsigned L_WO = L_A;
constexpr unsigned L_TOTAL = 163840;
static_assert(L_WPHI + 2048 <= L_TOTAL, "lds");
static_assert(L_WO + 896 * 128 == L_WPHI, "lds");
__host__ __device__ constexpr unsigned slot_of(int s) { return s % 3 == 0 ? L_A : (s % 3 == 1 ? L_B : L_C); }

struct Params {
    const void* x; const void* wqkvA; const void* wqkvB; const void* wphiA; const void* wphiB;
    const void* woA; const void* woB; const void* gate; void* out; void* ws;
};

typedef __amdgpu_buffer_rsrc_t rsrc_t;
DI rsrc_t mk_rsrc(const void* p, unsigned bytes) { return __builtin_amdgcn_make_buffer_rsrc((void*)p, (short)0, (int)bytes, 0x00020000); }
DI unsigned rfl(unsigned v) { return (unsigned)__builtin_amdgcn_readfirstlane((int)v); }
DI void dma16(rsrc_t r, unsigned voff, unsigned soff, unsigned lds) {
    asm volatile("s_mov_b32 m0, %0\n\ts_nop 0\n\tbuffer_load_dwordx4 %1, %2, %3 offen lds" ::"s"(lds), "v"(voff), "s"(r), "s"(soff) : "memory");
}
DI void dma4(rsrc_t r, unsigned voff, unsigned soff, unsigned lds) {
    asm volatile("s_mov_b32 m0, %0\n\ts_nop 0\n\tbuffer_load_dword %1, %2, %3 offen lds" ::"s"(lds), "v"(voff), "s"(r), "s"(soff) : "memory");
}
#define PF_CODE_LINES 176
DI void gdma4(const void* a, unsigned lds) {
    asm volatile("s_mov_b32 m0, %0\n\ts_nop 0\n\tglobal_load_lds_dword %1, off" ::"s"(lds), "v"(a) : "memory");
}
#define PF_STAG 0
#define PF_WO_SEG 2
#define WAIT_VM(n) asm volatile("s_waitcnt vmcnt(" #n ")" ::: "memory")
#define WAIT_VM_LGKM_BAR(n) asm volatile("s_waitcnt vmcnt(" #n ") lgkmcnt(0)\n\ts_barrier" ::: "memory")
#define LGKM_BAR() asm volatile("s_waitcnt lgkmcnt(0)\n\ts_barrier" ::: "memory")

extern __shared__ __attribute__((aligned(16))) char smem_dyn[];
DI LAS char* lds_at(unsigned off) { return (LAS char*)(uintptr_t)off; }
DI bf16x8 lds_b128(unsigned a) { return *(const LAS bf16x8*)lds_at(a); }
DI s16x4 lds_tr(unsigned a) { return __builtin_bit_cast(s16x4, __builtin_amdgcn_ds_read_tr16_b64_v4i16((LAS v4i16_t*)lds_at(a))); }
DI bf16x8 tr_frag(unsigned a0, unsigned a1) { const s16x4 lo = lds_tr(a0), hi = lds_tr(a1); return __builtin_shufflevector(lo, hi, 0, 1, 2, 3, 4, 5, 6, 7); }
DI unsigned cvtpk(float lo, float hi) { const f32x2 v = {lo, hi}; return __builtin_bit_cast(unsigned, __builtin_convertvector(v, bf2_t)); }
DI bf16x8 pack8(const f32x16& x, int s) {
    const u32x4 p = {cvtpk(x[8 * s], x[8 * s + 1]), cvtpk(x[8 * s + 2], x[8 * s + 3]), cvtpk(x[8 * s + 4], x[8 * s + 5]), cvtpk(x[8 * s + 6], x[8 * s + 7])};
    return __builtin_bit_cast(bf16x8, p);
}
DI float addf(float a, float b) { float r; asm("v_add_f32 %0, %1, %2" : "=v"(r) : "v"(a), "v"(b)); return r; }
DI float swap_lo(float v) { auto r = __builtin_amdgcn_permlane32_swap(__float_as_uint(v), __float_as_uint(v), false, false); return __uint_as_float(r[0]); }
DI float swap_hi(float v) { auto r = __builtin_amdgcn_permlane32_swap(__float_as_uint(v), __float_as_uint(v), false, false); return __uint_as_float(r[1]); }
DI float xhalf_max(float v) { auto r = __builtin_amdgcn_permlane32_swap(__float_as_uint(v), __float_as_uint(v), false, false); return __builtin_fmaxf(__uint_as_float(r[0]), __uint_as_float(r[1])); }
DI float xhalf_min(float v) { auto r = __builtin_amdgcn_permlane32_swap(__float_as_uint(v), __float_as_uint(v), false, false); return __builtin_fminf(__uint_as_float(r[0]), __uint_as_float(r[1])); }
DI float xhalf_sum(float v) { auto r = __builtin_amdgcn_permlane32_swap(__float_as_uint(v), __float_as_uint(v), false, false); return __uint_as_float(r[0]) + __uint_as_float(r[1]); }
constexpr float L2E = 1.4426950408889634f;
constexpr unsigned ONE2 = 0x3F803F80u;
constexpr unsigned SPIN_CAP = 1u << 16;

#define STAMP_DECL do { } while (0)
#define STAMP(i) do { } while (0)
#define STAMPX(i, x) do { } while (0)
#define STAMP_FLUSH() do { } while (0)

DI void row_extrema(const f32x16& a, const f32x16& b, float (&cp)[2], float (&cn)[2]) {
    float mxa = __builtin_fmaxf(a[0], a[1]), mna = __builtin_fminf(a[0], a[1]), mxb = __builtin_fmaxf(b[0], b[1]), mnb = __builtin_fminf(b[0], b[1]);
#pragma unroll
    for (int i = 2; i < 14; i += 2) {
        mxa = __builtin_fmaxf(__builtin_fmaxf(mxa, a[i]), a[i + 1]); mxb = __builtin_fmaxf(__builtin_fmaxf(mxb, b[i]), b[i + 1]);
        mna = __builtin_fminf(__builtin_fminf(mna, a[i]), a[i + 1]); mnb = __builtin_fminf(__builtin_fminf(mnb, b[i]), b[i + 1]);
    }
    cp[0] = -xhalf_max(mxa) * L2E; cp[1] = -xhalf_max(mxb) * L2E;
    cn[0] = xhalf_min(mna) * L2E; cn[1] = xhalf_min(mnb) * L2E;
}
DI void exp_pair(const f32x16& pp, float cp, float cn, f32x2 (&ep)[7], f32x2 (&en)[7]) {
    const f32x2 l2 = {L2E, L2E}, cp2 = {cp, cp}, cn2 = {cn, cn};
#pragma unroll
    for (int i = 0; i < 7; ++i) {
        const f32x2 v = {pp[2 * i], pp[2 * i + 1]};
        const f32x2 a = __builtin_elementwise_fma(v, l2, cp2), b = __builtin_elementwise_fma(v, -l2, cn2);
        ep[i] = (f32x2){__builtin_amdgcn_exp2f(a[0]), __builtin_amdgcn_exp2f(a[1])};
        en[i] = (f32x2){__builtin_amdgcn_exp2f(b[0]), __builtin_amdgcn_exp2f(b[1])};
    }
}
DI unsigned cvtpk2(f32x2 v) { return __builtin_bit_cast(unsigned, __builtin_convertvector(v, bf2_t)); }

DI void wait_flags(const GAS u64* f, u64 tag) {
#pragma unroll 1
    for (unsigned spins = 0; spins < SPIN_CAP; ++spins) {
        const u64 v = __hip_atomic_load(f, __ATOMIC_RELAXED, __HIP_MEMORY_SCOPE_AGENT);
        if (__builtin_amdgcn_ballot_w64(v != tag) == 0) break;
        __builtin_amdgcn_s_sleep(1);
    }
    asm volatile("" ::: "memory");
}

#define SB() __builtin_amdgcn_sched_barrier(0)
#define PINV(x) asm volatile("" : "+v"(x))
struct Frag { bf16x8 x0, x1, fk, fv; };

DI void build_aphi(bf16x8 (&aphi)[2], int r, int h2) {
    const int hr = (r >> 2) & 1, reg = (r & 3) + 4 * (r >> 3), f = 14 * hr + (reg < 14 ? reg : 0);
    const unsigned a = L_WPHI + 2 * (4 * h2 * DH + f);
#pragma unroll
    for (int s = 0; s < 2; ++s)
#pragma unroll
        for (int jj = 0; jj < 8; ++jj) {
            const int d0 = 16 * s + 8 * (jj >> 2) + (jj & 3);
            const bool ok = reg < 14 && (d0 + 4 < DH || h2 == 0);
            const unsigned short v = *(const LAS unsigned short*)lds_at(a + 2 * d0 * DH);
            aphi[s][jj] = ok ? (short)v : (short)0;
        }
}

__global__ void __launch_bounds__(512, 2) hedgehog_mix_kernel(Params p) {
    asm volatile("" : "+s"(p.x), "+s"(p.wqkvA), "+s"(p.wqkvB), "+s"(p.wphiA), "+s"(p.wphiB), "+s"(p.woA), "+s"(p.woB), "+s"(p.gate), "+s"(p.out), "+s"(p.ws));
    const int tid = threadIdx.x, lane = tid & 63;
    const unsigned w = rfl(tid >> 6), cw = w & 3;
    const int r = lane & 31, h2 = lane >> 5, i16 = lane & 15, q4 = i16 >> 2, p4 = i16 & 3, blk16 = (lane >> 4) & 1;
    const unsigned xcd = blockIdx.x & 7, j = blockIdx.x >> 3;
    const unsigned tb = (xcd & 1) * 4 + (j & 3), bh = (xcd >> 1) * 8 + (j >> 2), br = bh >> 4, h = bh & 15;
    const unsigned og = xcd * 4 + j / 7, oc = j % 7;
    const bool has_out = j < 28;
    STAMP_DECL;
    STAMP(0);
    const rsrc_t rws = mk_rsrc(p.ws, WS_END);
    const f32x16 zero16 = {};
    const int cq = 4 * blk16 + p4;
    const unsigned rowpart = (8 * h2 + q4) * 64;
    const unsigned coloff = (8 * (h & 1) + 8 * cq) & 63;
    const unsigned laneX = cw * 8192 + r * 128 + 16 * (h2 ^ ((r >> 1) & 7));

    const char* const pfW = (const char*)(br ? p.wqkvB : p.wqkvA);
    const unsigned wcol = ((xcd >> 1) & 1) * 384;
    auto pf_addr = [&](unsigned l, int s) -> const char* {
        const unsigned m = (l - 32) & 15;
        return l < 32 ? (const char*)p.x + ((xcd & 1) * 1024 + j * 32 + l) * XROW + s * 128
                      : pfW + (64 * s + 2 * j + (m >> 3)) * WROW + 896 + ((m >> 2) & 1) * 896 + wcol + (m & 3) * 128;
    };
    if (w >= 4) {
        const rsrc_t rx = mk_rsrc(p.x, S_ * XROW);
        const rsrc_t rw = mk_rsrc(br ? p.wqkvB : p.wqkvA, D_ * WROW);
        const unsigned vx0 = (lane >> 3) * XROW + 16 * ((lane & 7) ^ (lane >> 4));
        const unsigned vx1 = (lane >> 3) * XROW + 16 * ((lane & 7) ^ (4 + (lane >> 4)));
        const unsigned vw = (lane >> 2) * WROW + 16 * (lane & 3);
        const unsigned sx = (tb * 256 + cw * 64) * XROW;
        const unsigned sw = cw * 16 * WROW + h * 56 - 8 * (h & 1);
        auto issue = [&](int s) {
            const unsigned lb = slot_of(s);
            dma16(rw, vw, sw + s * 64 * WROW + 896, lb + SLOT_W + cw * 1024);
            dma16(rw, vw, sw + s * 64 * WROW + 1792, lb + SLOT_W + 4096 + cw * 1024);
#pragma unroll
            for (int pc = 0; pc < 8; ++pc) dma16(rx, (pc & 1) ? vx1 : vx0, sx + pc * 8 * XROW + s * 128, lb + cw * 8192 + pc * 1024);
        };
        bf16x8 xs[5][4][2];
        auto grab = [&](int s) {
#pragma unroll
            for (int ks = 0; ks < 4; ++ks) {
                const unsigned a = (laneX ^ (32 * ks)) + slot_of(s);
                xs[s][ks][0] = lds_b128(a); xs[s][ks][1] = lds_b128(a + 4096);
            }
        };
        asm volatile("s_barrier" ::: "memory");
        STAMP(11);
        issue(0); issue(1);
        STAMP(1);
        WAIT_VM_LGKM_BAR(10);
        STAMP(2);
        grab(0); issue(2); WAIT_VM_LGKM_BAR(10);
        grab(1); issue(3); WAIT_VM_LGKM_BAR(10);
        grab(2); issue(4); WAIT_VM_LGKM_BAR(10);
        grab(3); issue(5); WAIT_VM_LGKM_BAR(10);
        grab(4); issue(6); WAIT_VM_LGKM_BAR(10);
        WAIT_VM_LGKM_BAR(0);
        STAMP(3);
#pragma unroll
        for (int s = 0; s < 7; ++s) dma16(rw, vw, sw + s * 64 * WROW, L_WQ + s * 4096 + cw * 1024);
        bf16x8 aphi[2];
        build_aphi(aphi, r, h2);
        PINV(aphi[0]); PINV(aphi[1]);
        u64 tag;
        asm volatile("s_waitcnt lgkmcnt(0)\n\ts_load_dwordx2 %0, %1, 0x0\n\ts_waitcnt lgkmcnt(0)" : "=&s"(tag) : "s"(p.ws) : "memory");
        tag += 1;
        STAMP(4);
        WAIT_VM_LGKM_BAR(0);
        STAMP(5);
        f32x16 qT[2] = {zero16, zero16};
        {
            const unsigned aq_real = L_WQ + rowpart + coloff, aq_pad = L_GT + rowpart;
            bf16x8 fq[2], xl[2][2];
            unsigned aQ = cq == 7 ? aq_pad + 8 * 5 : aq_real + 4096 * 5;
            fq[0] = tr_frag(aQ, aQ + 256);
            { const unsigned a = laneX + slot_of(5); xl[0][0] = lds_b128(a); xl[0][1] = lds_b128(a + 4096); }
            SB();
#pragma unroll
            for (int n = 0; n < 28; ++n) {
                const int s = n < 8 ? 5 + (n >> 2) : (n - 8) >> 2, ks = n & 3;
                const int n1 = n + 1, s1 = n1 < 8 ? 5 + (n1 >> 2) : (n1 - 8) >> 2, k1 = n1 & 3;
                if (n1 < 28) {
                    if (k1 == 0) { aQ = cq == 7 ? aq_pad + 8 * s1 : aq_real + 4096 * s1; PINV(aQ); }
                    fq[n1 & 1] = tr_frag(aQ + k1 * 1024, aQ + k1 * 1024 + 256);
                    if (n1 < 8) { const unsigned a = (laneX ^ (32 * k1)) + slot_of(s1); xl[k1 & 1][0] = lds_b128(a); xl[k1 & 1][1] = lds_b128(a + 4096); }
                }
                SB();
                qT[0] = MFMA(fq[n & 1], n >= 8 ? xs[n >= 8 ? s : 0][ks][0] : xl[ks & 1][0], qT[0]);
                SB();
                qT[1] = MFMA(fq[n & 1], n >= 8 ? xs[n >= 8 ? s : 0][ks][1] : xl[ks & 1][1], qT[1]);
                SB();
                if (n == 7) {
                    asm volatile("s_waitcnt lgkmcnt(0)" ::: "memory");
                    *(volatile LAS unsigned*)lds_at(L_XFREE + 4 * cw) = 1u;
                }
            }
        }
        STAMP(6);
        bf16x8 eq[2][2][2];
        float gs[2];
        {
            f32x16 pq[2];
            const bf16x8 q00 = pack8(qT[0], 0), q10 = pack8(qT[1], 0);
            pq[0] = MFMA(aphi[0], q00, zero16); pq[1] = MFMA(aphi[0], q10, zero16);
            const bf16x8 q01 = pack8(qT[0], 1), q11 = pack8(qT[1], 1);
            pq[0] = MFMA(aphi[1], q01, pq[0]); pq[1] = MFMA(aphi[1], q11, pq[1]);
#pragma unroll
            for (int b = 0; b < 2; ++b) {
                const float lg = swap_hi(qT[b][12]);
                gs[b] = __builtin_amdgcn_rcpf(1.f + __builtin_amdgcn_exp2f(br ? lg * L2E : -lg * L2E));
                PINV(gs[b]);
            }
            float cp[2], cn[2];
            row_extrema(pq[0], pq[1], cp, cn);
            PINV(cp[0]); PINV(cp[1]); PINV(cn[0]); PINV(cn[1]);
#pragma unroll
            for (int b = 0; b < 2; ++b) {
                const f32x2 l2 = {L2E, L2E}, cp2 = {cp[b], cp[b]}, cn2 = {cn[b], cn[b]};
                unsigned pe[8], ne[8];
#pragma unroll
                for (int i = 0; i < 7; ++i) {
                    const f32x2 v = {pq[b][2 * i], pq[b][2 * i + 1]};
                    const f32x2 a = __builtin_elementwise_fma(v, l2, cp2), c = __builtin_elementwise_fma(v, -l2, cn2);
                    pe[i] = cvtpk2((f32x2){__builtin_amdgcn_exp2f(a[0]), __builtin_amdgcn_exp2f(a[1])});
                    ne[i] = cvtpk2((f32x2){__builtin_amdgcn_exp2f(c[0]), __builtin_amdgcn_exp2f(c[1])});
                }
                pe[7] = 0; ne[7] = 0;
                eq[b][0][0] = __builtin_bit_cast(bf16x8, (u32x4){pe[0], pe[1], pe[2], pe[3]}); eq[b][0][1] = __builtin_bit_cast(bf16x8, (u32x4){pe[4], pe[5], pe[6], pe[7]});
                eq[b][1][0] = __builtin_bit_cast(bf16x8, (u32x4){ne[0], ne[1], ne[2], ne[3]}); eq[b][1][1] = __builtin_bit_cast(bf16x8, (u32x4){ne[4], ne[5], ne[6], ne[7]});
            }
        }
#pragma unroll
        for (int b = 0; b < 2; ++b) asm volatile("" : "+v"(eq[b][0][0]), "+v"(eq[b][0][1]), "+v"(eq[b][1][0]), "+v"(eq[b][1][1]), "+v"(gs[b]));
        STAMP(7);
        LGKM_BAR();
        __builtin_amdgcn_s_setprio(3);
        STAMP(8);
        {
            bf16x8 akv[4];
#pragma unroll
            for (int f = 0; f < 4; ++f) akv[f] = lds_b128(L_KVA + f * 1024 + lane * 16);
#pragma unroll
            for (int b = 0; b < 2; ++b) {
                f32x16 np = MFMA(akv[0], eq[b][0][0], zero16); np = MFMA(akv[1], eq[b][0][1], np);
                f32x16 nn = MFMA(akv[2], eq[b][1][0], zero16); nn = MFMA(akv[3], eq[b][1][1], nn);
                const float ia = __builtin_amdgcn_rcpf(np[15]), ib = __builtin_amdgcn_rcpf(nn[15]);
                const float den = np[14] * ia + nn[14] * ib + 1e-6f;
                const float sc = gs[b] * __builtin_amdgcn_rcpf(den);
                const float ca = ia * sc, cb = ib * sc;
                float o[14];
#pragma unroll
                for (int i = 0; i < 14; ++i) o[i] = np[i] * ca + nn[i] * cb;
                const u32x4 o0 = {cvtpk(o[0], o[1]), cvtpk(o[2], o[3]), cvtpk(o[4], o[5]), cvtpk(o[6], o[7])};
                const u32x4 o1 = {cvtpk(o[8], o[9]), cvtpk(o[10], o[11]), cvtpk(o[12], o[13]), 0u};
                const unsigned so = WS_O + (((tb * 4 + cw) * 32 + bh) * 4 + b * 2) * 1024;
                __builtin_amdgcn_raw_buffer_store_b128(o0, rws, lane * 16, so, 16);
                __builtin_amdgcn_raw_buffer_store_b128(o1, rws, lane * 16, so + 1024, 16);
            }
        }
        STAMP(9);
        if (has_out) asm volatile("s_barrier" ::: "memory");
        WAIT_VM(0);
        if (lane == 0) __hip_atomic_store((GAS u64*)((char*)p.ws + WS_F2 + ((tb * 4 + cw) * 32 + bh) * 128), tag, __ATOMIC_RELAXED, __HIP_MEMORY_SCOPE_AGENT);
        STAMP(10);
        STAMP_FLUSH();
        return;
    }

    if (cw == 3) {
        u64 pc; asm volatile("s_getpc_b64 %0" : "=s"(pc));
        const unsigned l0 = j * 6 + (lane < 6 ? lane : 0), l = l0 < PF_CODE_LINES ? l0 : 0;
        gdma4((const char*)(pc & ~127ull) + l * 128, L_SCR + 256);
    }
    auto pf_seg = [&](int s) { gdma4(pf_addr(lane < 48 ? lane : 0, s), L_SCR + 256); };
    auto pf_wq = [&]() { const unsigned l = lane < 56 ? lane : 0; gdma4(pfW + (j * 14 + (l >> 2)) * WROW + wcol + (l & 3) * 128, L_SCR + 256); };
    auto pf_wo = [&](int part) {
        const unsigned i0 = part * 64 + lane, i = i0 < 196 ? i0 : 0, R = j * 28 + i / 7;
        gdma4((const char*)(j < 16 ? p.woA : p.woB) + (R - (j < 16 ? 0 : 448)) * XROW + (i % 7) * 128, L_SCR + 256);
    };
    if (PF_STAG) { if (cw >= 1) __builtin_amdgcn_s_sleep(PF_STAG); if (cw >= 2) __builtin_amdgcn_s_sleep(PF_STAG); if (cw >= 3) __builtin_amdgcn_s_sleep(PF_STAG); }
    pf_seg(cw);
    u64 tag;
    {
        const rsrc_t rg = mk_rsrc(p.gate, XROW);
        const rsrc_t rp = mk_rsrc(br ? p.wphiB : p.wphiA, 16 * DH * DH * 2);
        unsigned g0, g1; u32x2 wp;
        asm volatile("s_load_dwordx2 %0, %1, 0x0" : "=&s"(tag) : "s"(p.ws) : "memory");
        asm volatile("buffer_load_ushort %0, %3, %4, 0 offen\n\tbuffer_load_ushort %1, %3, %4, 0 offen offset:512\n\tbuffer_load_dwordx2 %2, %5, %6, %7 offen"
                     : "=&v"(g0), "=&v"(g1), "=&v"(wp) : "v"(tid * 2), "s"(rg), "v"(tid * 8), "s"(rp), "s"(h * (DH * DH * 2)) : "memory");
        if (cw == 0) WAIT_VM(3);
        asm volatile("s_barrier" ::: "memory");
        if (cw < 3) pf_seg(4 + cw); else pf_wq();
        asm volatile("s_waitcnt vmcnt(1) lgkmcnt(0)" : "+v"(g0), "+v"(g1), "+v"(wp), "+s"(tag) :: "memory");
        *(LAS u32x2*)lds_at(L_GT + (tid & 63) * 64 + (tid >> 6) * 8) = (u32x2){g0 & 0xffffu, 0u};
        *(LAS u32x2*)lds_at(L_GT + (tid & 63) * 64 + (4 + (tid >> 6)) * 8) = (u32x2){g1 & 0xffffu, 0u};
        *(LAS u32x2*)lds_at(L_WPHI + tid * 8) = wp;
        if (tid < 4) *(volatile LAS unsigned*)lds_at(L_XFREE + 4 * tid) = 0u;
    }
    tag += 1;
    __builtin_amdgcn_s_setprio(2);
    LGKM_BAR();
    STAMP(2);
    bf16x8 aphi[2];
    build_aphi(aphi, r, h2);
    PINV(aphi[0]); PINV(aphi[1]);
    const unsigned laneW = SLOT_W + rowpart + coloff;
    f32x16 vinit;
#pragma unroll
    for (int i = 0; i < 16; ++i) vinit[i] = r == 28 ? 1.f : 0.f;
    PINV(vinit);
    f32x16 kT[2] = {zero16, zero16}, vv[2] = {vinit, vinit};
    {
        unsigned aX[4], aW, aV;
        auto addr = [&](int s) {
#pragma unroll
            for (int ks = 0; ks < 4; ++ks) { aX[ks] = (laneX ^ (32 * ks)) + slot_of(s); PINV(aX[ks]); }
            aW = laneW + slot_of(s); PINV(aW);
            aV = cq == 7 ? L_GT + rowpart + 56 : aW + 4096; PINV(aV);
        };
        Frag F[2];
#define RD_X(f, ks) do { f.x0 = lds_b128(aX[ks]); } while (0)
#define RD_X1(f, ks) do { f.x1 = lds_b128(aX[ks] + 4096); } while (0)
#define RD_K(f, ks) do { f.fk = tr_frag(aW + ks * 1024, aW + ks * 1024 + 256); } while (0)
#define RD_V(f, ks) do { f.fv = tr_frag(aV + ks * 1024, aV + ks * 1024 + 256); } while (0)
        addr(0);
        RD_X(F[0], 0); RD_K(F[0], 0); RD_X1(F[0], 0); RD_V(F[0], 0); SB();
#pragma unroll
        for (int n = 0; n < 28; ++n) {
            Frag& c = F[n & 1];
            Frag& x = F[(n + 1) & 1];
            const int nks = (n + 1) & 3;
            const bool more = n + 1 < 28;
            kT[0] = MFMA(c.fk, c.x0, kT[0]); SB();
            if (more && nks == 0) { LGKM_BAR(); addr((n + 1) >> 2); SB(); }
            if (n == 1 + 4 * PF_WO_SEG) { pf_wo(cw); SB(); }
            if (more) { RD_X(x, nks); RD_K(x, nks); } SB();
            kT[1] = MFMA(c.fk, c.x1, kT[1]); SB();
            if (more) { RD_X1(x, nks); } SB();
            vv[0] = MFMA(c.x0, c.fv, vv[0]); SB();
            if (more) { RD_V(x, nks); } SB();
            vv[1] = MFMA(c.x1, c.fv, vv[1]); SB();
        }
    }
    STAMP(3);

    const unsigned phiW = L_PHI + cw * 8192;
    f32x16 pk[2];
    {
        const bf16x8 k00 = pack8(kT[0], 0), k10 = pack8(kT[1], 0);
        pk[0] = MFMA(aphi[0], k00, zero16); pk[1] = MFMA(aphi[0], k10, zero16);
        const bf16x8 k01 = pack8(kT[0], 1), k11 = pack8(kT[1], 1);
        pk[0] = MFMA(aphi[1], k01, pk[0]); pk[1] = MFMA(aphi[1], k11, pk[1]);
    }
    bf16x8 vp[2][2];
#pragma unroll
    for (int b = 0; b < 2; ++b)
#pragma unroll
        for (int s = 0; s < 2; ++s) { vp[b][s] = pack8(vv[b], s); PINV(vp[b][s]); }
    SB();
    float cp[2], cn[2];
    row_extrema(pk[0], pk[1], cp, cn);
    const unsigned pt0 = 4 * h2 + q4;
    const unsigned pa0 = phiW + pt0 * 64 + 8 * (cq ^ ((pt0 >> 1) & 7));
    const unsigned pa1 = (pa0 ^ 32) + 8 * 64;
    f32x16 kvp = zero16, kvn = zero16;
#pragma unroll
    for (int b = 0; b < 2; ++b) {
        f32x2 ep[7], en[7];
        exp_pair(pk[b], cp[b], cn[b], ep, en);
        f32x2 sp = ep[0], sn = en[0];
#pragma unroll
        for (int i = 1; i < 7; ++i) { sp += ep[i]; sn += en[i]; }
        const float ip = __builtin_amdgcn_rcpf(xhalf_sum(sp[0] + sp[1])), in = __builtin_amdgcn_rcpf(xhalf_sum(sn[0] + sn[1]));
        const f32x2 ip2 = {ip, ip}, in2 = {in, in};
        const unsigned base = phiW + b * 4096 + r * 64;
#pragma unroll
        for (int g = 0; g < 4; ++g) {
            const unsigned a = base + 8 * ((2 * g + h2) ^ ((r >> 1) & 7));
            const u32x2 dp = {cvtpk2(ep[2 * g] * ip2), g < 3 ? cvtpk2(ep[(2 * g + 1) % 7] * ip2) : 0u};
            const u32x2 dn = {cvtpk2(en[2 * g] * in2), g < 3 ? cvtpk2(en[(2 * g + 1) % 7] * in2) : 0u};
            *(LAS u32x2*)lds_at(a) = dp;
            *(LAS u32x2*)lds_at(a + 2048) = dn;
        }
#pragma unroll
        for (int s = 0; s < 2; ++s) {
            const unsigned o = b * 4096 + s * 1024;
            kvp = MFMA(tr_frag(pa0 + o, pa1 + o), vp[b][s], kvp);
            kvn = MFMA(tr_frag(pa0 + o + 2048, pa1 + o + 2048), vp[b][s], kvn);
        }
    }
    asm volatile("s_waitcnt lgkmcnt(0)" ::: "memory");
#pragma unroll
    for (int f = 0; f < 4; ++f)
#pragma unroll
        for (int hh = 0; hh < 2; ++hh) {
            const f32x16& t = f < 2 ? kvp : kvn;
            const int o = 8 * (f & 1) + 4 * hh;
            *(LAS f32x4*)lds_at(phiW + f * 2048 + hh * 1024 + lane * 16) = (f32x4){t[o], t[o + 1], t[o + 2], t[o + 3]};
        }
    STAMP(4);
    LGKM_BAR();
    STAMP(5);
    {
        f32x4 t[4][2];
#pragma unroll
        for (int u = 0; u < 4; ++u) {
            t[u][0] = *(const LAS f32x4*)lds_at(L_PHI + u * 8192 + cw * 2048 + lane * 16);
            t[u][1] = *(const LAS f32x4*)lds_at(L_PHI + u * 8192 + cw * 2048 + 1024 + lane * 16);
        }
        SB();
        float a[8];
#pragma unroll
        for (int i = 0; i < 8; ++i) {
            float x01 = addf(t[0][i >> 2][i & 3], t[1][i >> 2][i & 3]), x23 = addf(t[2][i >> 2][i & 3], t[3][i >> 2][i & 3]);
            a[i] = addf(x01, x23);
        }
        u32x4 o = {cvtpk(a[0], a[1]), cvtpk(a[2], a[3]), cvtpk(a[4], a[5]), cvtpk(a[6], a[7])};
        STAMPX(1, o);
        __builtin_amdgcn_raw_buffer_store_b128(o, rws, lane * 16, WS_KVP + ((bh * 8 + tb) * 4 + cw) * 1024, 16);
    }
    {
        const rsrc_t roA = mk_rsrc(p.woA, D_ * XROW), roB = mk_rsrc(p.woB, D_ * XROW);
        const unsigned vwo = (lane >> 3) * XROW + 16 * ((lane & 7) ^ (4 * ((lane >> 4) & 1)));
#pragma unroll
        for (int m = 0; m < 2; ++m) {
            dma16(roA, vwo, (256 + 16 * cw + 8 * m) * XROW + oc * 128, L_WO + (256 + 16 * cw + 8 * m) * 128);
            dma16(roB, vwo, (576 - 448 + 16 * cw + 8 * m) * XROW + oc * 128, L_WO + (576 + 16 * cw + 8 * m) * 128);
        }
#pragma unroll 1
        for (unsigned spins = 0; spins < SPIN_CAP; ++spins) { if (*(volatile LAS unsigned*)lds_at(L_XFREE + 4 * cw) != 0u) break; }
#pragma unroll
        for (int m = 0; m < 6; ++m) dma16(roA, vwo, (64 * cw + 8 * m) * XROW + oc * 128, L_WO + (64 * cw + 8 * m) * 128);
        WAIT_VM(10);
        if (lane == 0) __hip_atomic_store((GAS u64*)((char*)p.ws + WS_F1 + ((bh * 4 + cw) * 8 + tb) * 128), tag, __ATOMIC_RELAXED, __HIP_MEMORY_SCOPE_AGENT);
        STAMP(9);
#pragma unroll
        for (int m = 6; m < 8; ++m) dma16(roA, vwo, (64 * cw + 8 * m) * XROW + oc * 128, L_WO + (64 * cw + 8 * m) * 128);
        const rsrc_t roC = cw < 2 ? roA : roB;
        const unsigned R = 320 + 64 * cw, Rg = cw < 2 ? R : R - 448;
#pragma unroll
        for (int m = 0; m < 8; ++m) dma16(roC, vwo, (Rg + 8 * m) * XROW + oc * 128, L_WO + (R + 8 * m) * 128);
    }
    STAMP(6);
    wait_flags((const GAS u64*)((const char*)p.ws + WS_F1 + ((bh * 4 + cw) * 8 + (lane & 7)) * 128), tag);
    STAMP(7);
    {
        const int hr = (r >> 2) & 1, reg = (r & 3) + 4 * (r >> 3);
        const int col = reg < 14 ? 14 * hr + reg : 28;
        const unsigned vo = (h2 * 32 + col) * 16;
        u32x4 part[8];
#pragma unroll
        for (int u = 0; u < 8; ++u) part[u] = __builtin_amdgcn_raw_buffer_load_b128(rws, vo, WS_KVP + ((bh * 8 + u) * 4 + cw) * 1024, 16);
        SB();
        float acc[8];
#pragma unroll
        for (int i = 0; i < 8; ++i) acc[i] = 0.f;
#pragma unroll
        for (int u = 0; u < 8; ++u)
#pragma unroll
            for (int i = 0; i < 4; ++i) { acc[2 * i] += __uint_as_float(part[u][i] << 16); acc[2 * i + 1] += __uint_as_float(part[u][i] & 0xffff0000u); }
        u32x4 o = {cvtpk(acc[0], acc[1]), cvtpk(acc[2], acc[3]), cvtpk(acc[4], acc[5]), cvtpk(acc[6], acc[7])};
        if (reg == 15) o = (u32x4){ONE2, ONE2, ONE2, ONE2};
        *(LAS u32x4*)lds_at(L_KVA + cw * 1024 + lane * 16) = o;
    }
    LGKM_BAR();
    STAMP(8);
    if (!has_out) { STAMP_FLUSH(); return; }
    {
        const rsrc_t ro = mk_rsrc(p.woB, D_ * XROW);
        const unsigned vo = (lane >> 3) * XROW + 16 * ((lane & 7) ^ (4 * ((lane >> 4) & 1)));
#pragma unroll
        for (unsigned n = 0; n < 8; ++n) {
            const unsigned R = 640 + 64 * cw + 8 * n;
            dma16(ro, vo, (R - 448) * XROW + oc * 128, L_WO + R * 128);
        }
    }
    WAIT_VM_LGKM_BAR(0);
    STAMP(10);

    const unsigned tk = cw & 1, kh = cw >> 1;
    constexpr u64 HORD = 0xDB9642FECA875310ull;
    const GAS u64* f2 = (const GAS u64*)((const char*)p.ws + WS_F2 + (og * 32 + kh * 16) * 128);
    f32x16 mine = zero16, other = zero16;
    {
        const unsigned colp = 32 * blk16 + 8 * p4;
        const unsigned r0 = kh * 448 + 14 * h2 + q4, r1 = kh * 448 + 14 * h2 + (q4 < 1 ? q4 : 1);
        unsigned a0 = L_WO + r0 * 128 + 64 * (kh ^ ((r0 >> 1) & 1)) + colp;
        unsigned a1 = L_WO + r1 * 128 + 64 * (kh ^ ((r1 >> 1) & 1)) + colp;
        unsigned a2 = a0 ^ 64, a3 = a1 ^ 64;
        asm volatile("" : "+v"(a0), "+v"(a1), "+v"(a2), "+v"(a3));
        u32x4 of[16][2];
        auto ld = [&](int i) {
            const unsigned hd = (unsigned)(HORD >> (4 * i)) & 15u;
#pragma unroll
            for (int s = 0; s < 2; ++s)
                of[i][s] = __builtin_amdgcn_raw_buffer_load_b128(rws, lane * 16, WS_O + (((og * 32 + kh * 16 + hd) * 2 + tk) * 2 + s) * 1024, 16);
        };
        bf16x8 A[2][4];
        auto rd = [&](bf16x8 (&f)[4], int i) {
            const unsigned o = ((unsigned)(HORD >> (4 * i)) & 15u) * 28 * 128;
            f[0] = tr_frag(a0 + o, a0 + o + 512); f[1] = tr_frag(a2 + o, a2 + o + 512);
            f[2] = tr_frag(a0 + o + 1024, a1 + o + 1536); f[3] = tr_frag(a2 + o + 1024, a3 + o + 1536);
        };
        auto step = [&](int i) {
            if (i + 1 < 16) rd(A[(i + 1) & 1], i + 1);
            SB();
            const bf16x8 b0 = __builtin_bit_cast(bf16x8, of[i][0]), b1 = __builtin_bit_cast(bf16x8, of[i][1]);
            mine = MFMA(A[i & 1][0], b0, mine); other = MFMA(A[i & 1][1], b0, other);
            mine = MFMA(A[i & 1][2], b1, mine); other = MFMA(A[i & 1][3], b1, other);
            SB();
        };
        wait_flags(f2 + 16 * ((HORD >> (4 * (lane % 10))) & 15), tag);
        STAMP(14);
#pragma unroll
        for (int i = 0; i < 10; ++i) ld(i);
        SB();
        rd(A[0], 0); SB();
#pragma unroll
        for (int i = 0; i < 10; ++i) step(i);
        STAMP(15);
        wait_flags(f2 + 16 * ((HORD >> (4 * (10 + lane % 6))) & 15), tag);
        STAMP(11);
        if (blockIdx.x == 0 && tid == 0) __hip_atomic_store((GAS u64*)((char*)p.ws + WS_EPOCH), tag, __ATOMIC_RELAXED, __HIP_MEMORY_SCOPE_AGENT);
#pragma unroll
        for (int i = 10; i < 16; ++i) ld(i);
        SB();
#pragma unroll
        for (int i = 10; i < 16; ++i) step(i);
    }
    STAMP(12);
#pragma unroll
    for (int g = 0; g < 4; ++g)
        *(LAS f32x4*)lds_at(L_PHI + cw * 4096 + g * 1024 + lane * 16) = (f32x4){other[4 * g], other[4 * g + 1], other[4 * g + 2], other[4 * g + 3]};
    LGKM_BAR();
    {
        unsigned d[8];
#pragma unroll
        for (int g = 0; g < 4; ++g) {
            const f32x4 t = *(const LAS f32x4*)lds_at(L_PHI + (cw ^ 2) * 4096 + g * 1024 + lane * 16);
            d[2 * g] = cvtpk(mine[4 * g] + t[0], mine[4 * g + 1] + t[1]);
            d[2 * g + 1] = cvtpk(mine[4 * g + 2] + t[2], mine[4 * g + 3] + t[3]);
        }
        const rsrc_t rout = mk_rsrc(p.out, S_ * XROW);
        const unsigned vo = r * XROW + h2 * 16, so = (og * 64 + tk * 32) * XROW + oc * 128 + kh * 64;
#pragma unroll
        for (int k = 0; k < 4; k += 2) {
            auto sx0 = __builtin_amdgcn_permlane32_swap(d[2 * k], d[2 * k + 2], false, false);
            auto sx1 = __builtin_amdgcn_permlane32_swap(d[2 * k + 1], d[2 * k + 3], false, false);
            const u32x4 o = {sx0[0], sx1[0], sx0[1], sx1[1]};
            __builtin_amdgcn_raw_buffer_store_b128(o, rout, vo, so + 16 * k, 0);
        }
    }
    STAMP(13);
    STAMP_FLUSH();
}

extern "C" void kernel_launch(void* const* d_in, const int* in_sizes, int n_in, void* d_out, int out_size, void* d_ws, size_t ws_size,
                              hipStream_t stream) {
    Params p;
    p.x = d_in[0]; p.wqkvA = d_in[1]; p.wphiA = d_in[2]; p.woA = d_in[3];
    p.wqkvB = d_in[5]; p.wphiB = d_in[6]; p.woB = d_in[7]; p.gate = d_in[8];
    p.out = d_out; p.ws = d_ws;
    (void)hipFuncSetAttribute((const void*)hedgehog_mix_kernel, hipFuncAttributeMaxDynamicSharedMemorySize, L_TOTAL);
    hedgehog_mix_kernel<<<256, 512, L_TOTAL, stream>>>(p);
}
```

```cpp
#define PREFETCH 3
#define PF_WO_SEG 3
#define PF_CODE 1
#include <hip/hip_runtime.h>
#include <stdint.h>

typedef short bf16x8 __attribute__((ext_vector_type(8)));
typedef short s16x4 __attribute__((ext_vector_type(4)));
typedef short v4i16_t __attribute__((ext_vector_type(4)));
typedef __bf16 bf2_t __attribute__((ext_vector_type(2)));
typedef float f32x2 __attribute__((ext_vector_type(2)));
typedef float f32x4 __attribute__((ext_vector_type(4)));
typedef float f32x16 __attribute__((ext_vector_type(16)));
typedef unsigned u32x2 __attribute__((ext_vector_type(2)));
typedef unsigned u32x4 __attribute__((ext_vector_type(4)));
typedef unsigned long long u64;
#define LAS __attribute__((address_space(3)))
#define GAS __attribute__((address_space(1)))
#define DI __device__ __forceinline__
#define MFMA(a, b, c) __builtin_amdgcn_mfma_f32_32x32x16_bf16((a), (b), (c), 0, 0, 0)

constexpr int S_ = 2048, D_ = 448, DH = 28;
constexpr unsigned XROW = D_ * 2;
constexpr unsigned WROW = 3 * D_ * 2;
constexpr unsigned WS_EPOCH = 0;
constexpr unsigned WS_F1 = 4096;
constexpr unsigned WS_F2 = WS_F1 + 32 * 4 * 8 * 128;
constexpr unsigned WS_KVP = WS_F2 + 32 * 32 * 128;
constexpr unsigned WS_O = WS_KVP + 32 * 8 * 4 * 1024;
constexpr unsigned WS_END = WS_O + 32 * 32 * 4 * 1024;
constexpr unsigned WS_STAMP = 8u << 20;
constexpr unsigned SLOT = 40960, SLOT_W = 32768;
constexpr unsigned L_B = 0, L_A = SLOT, L_C = 2 * SLOT;
constexpr unsigned L_WQ = 3 * SLOT;
constexpr unsigned L_GT = L_WQ + 448 * 64;
constexpr unsigned L_WPHI = L_GT + 4096;
constexpr unsigned L_SCR = L_WPHI + 2048;
constexpr unsigned L_XFREE = L_SCR + 512;
constexpr unsigned L_WOCNT = L_XFREE + 16;
constexpr unsigned L_PHI = L_B;
constexpr unsigned L_KVA = L_B + SLOT_W;
constexpr unsigned L_WO = L_A;
constexpr unsigned L_TOTAL = 163840;
static_assert(L_WPHI + 2048 <= L_TOTAL, "lds");
static_assert(L_WO + 896 * 128 == L_WPHI, "lds");
__host__ __device__ constexpr unsigned slot_of(int s) { return s % 3 == 0 ? L_A : (s % 3 == 1 ? L_B : L_C); }

struct Params {
    const void* x; const void* wqkvA; const void* wqkvB; const void* wphiA; const void* wphiB;
    const void* woA; const void* woB; const void* gate; void* out; void* ws;
};

typedef __amdgpu_buffer_rsrc_t rsrc_t;
DI rsrc_t mk_rsrc(const void* p, unsigned bytes) { return __builtin_amdgcn_make_buffer_rsrc((void*)p, (short)0, (int)bytes, 0x00020000); }
DI unsigned rfl(unsigned v) { return (unsigned)__builtin_amdgcn_readfirstlane((int)v); }
DI void dma16(rsrc_t r, unsigned voff, unsigned soff, unsigned lds) {
    asm volatile("s_mov_b32 m0, %0\n\ts_nop 0\n\tbuffer_load_dwordx4 %1, %2, %3 offen lds" ::"s"(lds), "v"(voff), "s"(r), "s"(soff) : "memory");
}
DI void dma4(rsrc_t r, unsigned voff, unsigned soff, unsigned lds) {
    asm volatile("s_mov_b32 m0, %0\n\ts_nop 0\n\tbuffer_load_dword %1, %2, %3 offen lds" ::"s"(lds), "v"(voff), "s"(r), "s"(soff) : "memory");
}
#define PF_CODE_LINES 176
DI void gdma4(const void* a, unsigned lds) {
    asm volatile("s_mov_b32 m0, %0\n\ts_nop 0\n\tglobal_load_lds_dword %1, off" ::"s"(lds), "v"(a) : "memory");
}
#define PF_STAG 0
#define PF_WQ_SEG 1
#define WAIT_VM(n) asm volatile("s_waitcnt vmcnt(" #n ")" ::: "memory")
#define WAIT_VM_LGKM_BAR(n) asm volatile("s_waitcnt vmcnt(" #n ") lgkmcnt(0)\n\ts_barrier" ::: "memory")
#define LGKM_BAR() asm volatile("s_waitcnt lgkmcnt(0)\n\ts_barrier" ::: "memory")

extern __shared__ __attribute__((aligned(16))) char smem_dyn[];
DI LAS char* lds_at(unsigned off) { return (LAS char*)(uintptr_t)off; }
DI bf16x8 lds_b128(unsigned a) { return *(const LAS bf16x8*)lds_at(a); }
DI s16x4 lds_tr(unsigned a) { return __builtin_bit_cast(s16x4, __builtin_amdgcn_ds_read_tr16_b64_v4i16((LAS v4i16_t*)lds_at(a))); }
DI bf16x8 tr_frag(unsigned a0, unsigned a1) { const s16x4 lo = lds_tr(a0), hi = lds_tr(a1); return __builtin_shufflevector(lo, hi, 0, 1, 2, 3, 4, 5, 6, 7); }
DI unsigned cvtpk(float lo, float hi) { const f32x2 v = {lo, hi}; return __builtin_bit_cast(unsigned, __builtin_convertvector(v, bf2_t)); }
DI bf16x8 pack8(const f32x16& x, int s) {
    const u32x4 p = {cvtpk(x[8 * s], x[8 * s + 1]), cvtpk(x[8 * s + 2], x[8 * s + 3]), cvtpk(x[8 * s + 4], x[8 * s + 5]), cvtpk(x[8 * s + 6], x[8 * s + 7])};
    return __builtin_bit_cast(bf16x8, p);
}
DI float addf(float a, float b) { float r; asm("v_add_f32 %0, %1, %2" : "=v"(r) : "v"(a), "v"(b)); return r; }
DI float swap_lo(float v) { auto r = __builtin_amdgcn_permlane32_swap(__float_as_uint(v), __float_as_uint(v), false, false); return __uint_as_float(r[0]); }
DI float swap_hi(float v) { auto r = __builtin_amdgcn_permlane32_swap(__float_as_uint(v), __float_as_uint(v), false, false); return __uint_as_float(r[1]); }
DI float xhalf_max(float v) { auto r = __builtin_amdgcn_permlane32_swap(__float_as_uint(v), __float_as_uint(v), false, false); return __builtin_fmaxf(__uint_as_float(r[0]), __uint_as_float(r[1])); }
DI float xhalf_min(float v) { auto r = __builtin_amdgcn_permlane32_swap(__float_as_uint(v), __float_as_uint(v), false, false); return __builtin_fminf(__uint_as_float(r[0]), __uint_as_float(r[1])); }
DI float xhalf_sum(float v) { auto r = __builtin_amdgcn_permlane32_swap(__float_as_uint(v), __float_as_uint(v), false, false); return __uint_as_float(r[0]) + __uint_as_float(r[1]); }
constexpr float L2E = 1.4426950408889634f;
constexpr unsigned ONE2 = 0x3F803F80u;
constexpr unsigned SPIN_CAP = 1u << 16;

#define STAMP_DECL do { } while (0)
#define STAMP(i) do { } while (0)
#define STAMPX(i, x) do { } while (0)
#define STAMP_FLUSH() do { } while (0)

DI void row_extrema(const f32x16& a, const f32x16& b, float (&cp)[2], float (&cn)[2]) {
    float mxa = __builtin_fmaxf(a[0], a[1]), mna = __builtin_fminf(a[0], a[1]), mxb = __builtin_fmaxf(b[0], b[1]), mnb = __builtin_fminf(b[0], b[1]);
#pragma unroll
    for (int i = 2; i < 14; i += 2) {
        mxa = __builtin_fmaxf(__builtin_fmaxf(mxa, a[i]), a[i + 1]); mxb = __builtin_fmaxf(__builtin_fmaxf(mxb, b[i]), b[i + 1]);
        mna = __builtin_fminf(__builtin_fminf(mna, a[i]), a[i + 1]); mnb = __builtin_fminf(__builtin_fminf(mnb, b[i]), b[i + 1]);
    }
    cp[0] = -xhalf_max(mxa) * L2E; cp[1] = -xhalf_max(mxb) * L2E;
    cn[0] = xhalf_min(mna) * L2E; cn[1] = xhalf_min(mnb) * L2E;
}
DI void exp_pair(const f32x16& pp, float cp, float cn, f32x2 (&ep)[7], f32x2 (&en)[7]) {
    const f32x2 l2 = {L2E, L2E}, cp2 = {cp, cp}, cn2 = {cn, cn};
#pragma unroll
    for (int i = 0; i < 7; ++i) {
        const f32x2 v = {pp[2 * i], pp[2 * i + 1]};
        const f32x2 a = __builtin_elementwise_fma(v, l2, cp2), b = __builtin_elementwise_fma(v, -l2, cn2);
        ep[i] = (f32x2){__builtin_amdgcn_exp2f(a[0]), __builtin_amdgcn_exp2f(a[1])};
        en[i] = (f32x2){__builtin_amdgcn_exp2f(b[0]), __builtin_amdgcn_exp2f(b[1])};
    }
}
DI unsigned cvtpk2(f32x2 v) { return __builtin_bit_cast(unsigned, __builtin_convertvector(v, bf2_t)); }

DI void wait_flags(const GAS u64* f, u64 tag) {
#pragma unroll 1
    for (unsigned spins = 0; spins < SPIN_CAP; ++spins) {
        const u64 v = __hip_atomic_load(f, __ATOMIC_RELAXED, __HIP_MEMORY_SCOPE_AGENT);
        if (__builtin_amdgcn_ballot_w64(v != tag) == 0) break;
        __builtin_amdgcn_s_sleep(1);
    }
    asm volatile("" ::: "memory");
}

#define SB() __builtin_amdgcn_sched_barrier(0)
#define PINV(x) asm volatile("" : "+v"(x))
struct Frag { bf16x8 x0, x1, fk, fv; };

DI void build_aphi(bf16x8 (&aphi)[2], int r, int h2) {
    const int hr = (r >> 2) & 1, reg = (r & 3) + 4 * (r >> 3), f = 14 * hr + (reg < 14 ? reg : 0);
    const unsigned a = L_WPHI + 2 * (4 * h2 * DH + f);
#pragma unroll
    for (int s = 0; s < 2; ++s)
#pragma unroll
        for (int jj = 0; jj < 8; ++jj) {
            const int d0 = 16 * s + 8 * (jj >> 2) + (jj & 3);
            const bool ok = reg < 14 && (d0 + 4 < DH || h2 == 0);
            const unsigned short v = *(const LAS unsigned short*)lds_at(a + 2 * d0 * DH);
            aphi[s][jj] = ok ? (short)v : (short)0;
        }
}

__global__ void __launch_bounds__(512, 2) hedgehog_mix_kernel(Params p) {
    asm volatile("" : "+s"(p.x), "+s"(p.wqkvA), "+s"(p.wqkvB), "+s"(p.wphiA), "+s"(p.wphiB), "+s"(p.woA), "+s"(p.woB), "+s"(p.gate), "+s"(p.out), "+s"(p.ws));
    const int tid = threadIdx.x, lane = tid & 63;
    const unsigned w = rfl(tid >> 6), cw = w & 3;
    const int r = lane & 31, h2 = lane >> 5, i16 = lane & 15, q4 = i16 >> 2, p4 = i16 & 3, blk16 = (lane >> 4) & 1;
    const unsigned xcd = blockIdx.x & 7, j = blockIdx.x >> 3;
    const unsigned tb = (xcd & 1) * 4 + (j & 3), bh = (xcd >> 1) * 8 + (j >> 2), br = bh >> 4, h = bh & 15;
    const unsigned og = xcd * 4 + j / 7, oc = j % 7;
    const bool has_out = j < 28;
    STAMP_DECL;
    STAMP(0);
    const rsrc_t rws = mk_rsrc(p.ws, WS_END);
    const f32x16 zero16 = {};
    const int cq = 4 * blk16 + p4;
    const unsigned rowpart = (8 * h2 + q4) * 64;
    const unsigned coloff = (8 * (h & 1) + 8 * cq) & 63;
    const unsigned laneX = cw * 8192 + r * 128 + 16 * (h2 ^ ((r >> 1) & 7));

    const char* const pfW = (const char*)(br ? p.wqkvB : p.wqkvA);
    const unsigned wcol = ((xcd >> 1) & 1) * 384;
    auto pf_addr = [&](unsigned l, int s) -> const char* {
        const unsigned m = (l - 32) & 15;
        return l < 32 ? (const char*)p.x + ((xcd & 1) * 1024 + j * 32 + l) * XROW + s * 128
                      : pfW + (64 * s + 2 * j + (m >> 3)) * WROW + 896 + ((m >> 2) & 1) * 896 + wcol + (m & 3) * 128;
    };
    if (w >= 4) {
        const rsrc_t rx = mk_rsrc(p.x, S_ * XROW);
        const rsrc_t rw = mk_rsrc(br ? p.wqkvB : p.wqkvA, D_ * WROW);
        const unsigned vx0 = (lane >> 3) * XROW + 16 * ((lane & 7) ^ (lane >> 4));
        const unsigned vx1 = (lane >> 3) * XROW + 16 * ((lane & 7) ^ (4 + (lane >> 4)));
        const unsigned vw = (lane >> 2) * WROW + 16 * (lane & 3);
        const unsigned sx = (tb * 256 + cw * 64) * XROW;
        const unsigned sw = cw * 16 * WROW + h * 56 - 8 * (h & 1);
        auto issue = [&](int s) {
            const unsigned lb = slot_of(s);
            dma16(rw, vw, sw + s * 64 * WROW + 896, lb + SLOT_W + cw * 1024);
            dma16(rw, vw, sw + s * 64 * WROW + 1792, lb + SLOT_W + 4096 + cw * 1024);
#pragma unroll
            for (int pc = 0; pc < 8; ++pc) dma16(rx, (pc & 1) ? vx1 : vx0, sx + pc * 8 * XROW + s * 128, lb + cw * 8192 + pc * 1024);
        };
        bf16x8 xs[5][4][2];
        auto grab = [&](int s) {
#pragma unroll
            for (int ks = 0; ks < 4; ++ks) {
                const unsigned a = (laneX ^ (32 * ks)) + slot_of(s);
                xs[s][ks][0] = lds_b128(a); xs[s][ks][1] = lds_b128(a + 4096);
            }
        };
        STAMP(11);
        issue(0); issue(1);
        STAMP(1);
        WAIT_VM_LGKM_BAR(10);
        STAMP(2);
        grab(0); issue(2); WAIT_VM_LGKM_BAR(10);
        grab(1); issue(3); WAIT_VM_LGKM_BAR(10);
        grab(2); issue(4); WAIT_VM_LGKM_BAR(10);
        grab(3); issue(5); WAIT_VM_LGKM_BAR(10);
        grab(4); issue(6); WAIT_VM_LGKM_BAR(10);
        WAIT_VM_LGKM_BAR(0);
        STAMP(3);
#pragma unroll
        for (int s = 0; s < 7; ++s) dma16(rw, vw, sw + s * 64 * WROW, L_WQ + s * 4096 + cw * 1024);
        bf16x8 aphi[2];
        build_aphi(aphi, r, h2);
        PINV(aphi[0]); PINV(aphi[1]);
        u64 tag;
        asm volatile("s_waitcnt lgkmcnt(0)\n\ts_load_dwordx2 %0, %1, 0x0\n\ts_waitcnt lgkmcnt(0)" : "=&s"(tag) : "s"(p.ws) : "memory");
        tag += 1;
        STAMP(4);
        WAIT_VM_LGKM_BAR(0);
        STAMP(5);
        f32x16 qT[2] = {zero16, zero16};
        {
            const unsigned aq_real = L_WQ + rowpart + coloff, aq_pad = L_GT + rowpart;
            bf16x8 fq[3], xl[2][2];
            auto seg_of = [](int n) { return n < 8 ? 5 + (n >> 2) : (n - 8) >> 2; };
            unsigned aQ = 0;
            auto rdq = [&](int n) {
                if ((n & 3) == 0) { aQ = cq == 7 ? aq_pad + 8 * seg_of(n) : aq_real + 4096 * seg_of(n); PINV(aQ); }
                fq[n % 3] = tr_frag(aQ + (n & 3) * 1024, aQ + (n & 3) * 1024 + 256);
            };
            auto rdx = [&](int n) { const unsigned a = (laneX ^ (32 * (n & 3))) + slot_of(seg_of(n)); xl[n & 1][0] = lds_b128(a); xl[n & 1][1] = lds_b128(a + 4096); };
            rdq(0); rdx(0); rdq(1);
            SB();
#pragma unroll
            for (int n = 0; n < 28; ++n) {
                const int s = seg_of(n), ks = n & 3;
                if (n + 1 < 8) rdx(n + 1);
                if (n + 2 < 28) rdq(n + 2);
                SB();
                qT[0] = MFMA(fq[n % 3], n >= 8 ? xs[n >= 8 ? s : 0][ks][0] : xl[n & 1][0], qT[0]);
                qT[1] = MFMA(fq[n % 3], n >= 8 ? xs[n >= 8 ? s : 0][ks][1] : xl[n & 1][1], qT[1]);
                SB();
                if (n == 7) {
                    asm volatile("s_waitcnt lgkmcnt(0)" ::: "memory");
                    *(volatile LAS unsigned*)lds_at(L_XFREE + 4 * cw) = 1u;
                }
            }
        }
        STAMP(6);
        bf16x8 eq[2][2][2];
        float gs[2];
        {
            f32x16 pq[2];
            const bf16x8 q00 = pack8(qT[0], 0), q10 = pack8(qT[1], 0);
            pq[0] = MFMA(aphi[0], q00, zero16); pq[1] = MFMA(aphi[0], q10, zero16);
            const bf16x8 q01 = pack8(qT[0], 1), q11 = pack8(qT[1], 1);
            pq[0] = MFMA(aphi[1], q01, pq[0]); pq[1] = MFMA(aphi[1], q11, pq[1]);
#pragma unroll
            for (int b = 0; b < 2; ++b) {
                const float lg = swap_hi(qT[b][12]);
                gs[b] = __builtin_amdgcn_rcpf(1.f + __builtin_amdgcn_exp2f(br ? lg * L2E : -lg * L2E));
                PINV(gs[b]);
            }
            float cp[2], cn[2];
            row_extrema(pq[0], pq[1], cp, cn);
            PINV(cp[0]); PINV(cp[1]); PINV(cn[0]); PINV(cn[1]);
#pragma unroll
            for (int b = 0; b < 2; ++b) {
                const f32x2 l2 = {L2E, L2E}, cp2 = {cp[b], cp[b]}, cn2 = {cn[b], cn[b]};
                unsigned pe[8], ne[8];
#pragma unroll
                for (int i = 0; i < 7; ++i) {
                    const f32x2 v = {pq[b][2 * i], pq[b][2 * i + 1]};
                    const f32x2 a = __builtin_elementwise_fma(v, l2, cp2), c = __builtin_elementwise_fma(v, -l2, cn2);
                    pe[i] = cvtpk2((f32x2){__builtin_amdgcn_exp2f(a[0]), __builtin_amdgcn_exp2f(a[1])});
                    ne[i] = cvtpk2((f32x2){__builtin_amdgcn_exp2f(c[0]), __builtin_amdgcn_exp2f(c[1])});
                }
                pe[7] = 0; ne[7] = 0;
                eq[b][0][0] = __builtin_bit_cast(bf16x8, (u32x4){pe[0], pe[1], pe[2], pe[3]}); eq[b][0][1] = __builtin_bit_cast(bf16x8, (u32x4){pe[4], pe[5], pe[6], pe[7]});
                eq[b][1][0] = __builtin_bit_cast(bf16x8, (u32x4){ne[0], ne[1], ne[2], ne[3]}); eq[b][1][1] = __builtin_bit_cast(bf16x8, (u32x4){ne[4], ne[5], ne[6], ne[7]});
            }
        }
#pragma unroll
        for (int b = 0; b < 2; ++b) asm volatile("" : "+v"(eq[b][0][0]), "+v"(eq[b][0][1]), "+v"(eq[b][1][0]), "+v"(eq[b][1][1]), "+v"(gs[b]));
        STAMP(7);
        LGKM_BAR();
        __builtin_amdgcn_s_setprio(3);
        STAMP(8);
        {
            bf16x8 akv[4];
#pragma unroll
            for (int f = 0; f < 4; ++f) akv[f] = lds_b128(L_KVA + f * 1024 + lane * 16);
#pragma unroll
            for (int b = 0; b < 2; ++b) {
                f32x16 np = MFMA(akv[0], eq[b][0][0], zero16); np = MFMA(akv[1], eq[b][0][1], np);
                f32x16 nn = MFMA(akv[2], eq[b][1][0], zero16); nn = MFMA(akv[3], eq[b][1][1], nn);
                const float ia = __builtin_amdgcn_rcpf(np[15]), ib = __builtin_amdgcn_rcpf(nn[15]);
                const float den = np[14] * ia + nn[14] * ib + 1e-6f;
                const float sc = gs[b] * __builtin_amdgcn_rcpf(den);
                const float ca = ia * sc, cb = ib * sc;
                float o[14];
#pragma unroll
                for (int i = 0; i < 14; ++i) o[i] = np[i] * ca + nn[i] * cb;
                const u32x4 o0 = {cvtpk(o[0], o[1]), cvtpk(o[2], o[3]), cvtpk(o[4], o[5]), cvtpk(o[6], o[7])};
                const u32x4 o1 = {cvtpk(o[8], o[9]), cvtpk(o[10], o[11]), cvtpk(o[12], o[13]), 0u};
                const unsigned so = WS_O + (((tb * 4 + cw) * 32 + bh) * 4 + b * 2) * 1024;
                __builtin_amdgcn_raw_buffer_store_b128(o0, rws, lane * 16, so, 16);
                __builtin_amdgcn_raw_buffer_store_b128(o1, rws, lane * 16, so + 1024, 16);
            }
        }
        STAMP(9);
        WAIT_VM(0);
        if (lane == 0) __hip_atomic_store((GAS u64*)((char*)p.ws + WS_F2 + ((tb * 4 + cw) * 32 + bh) * 128), tag, __ATOMIC_RELAXED, __HIP_MEMORY_SCOPE_AGENT);
        STAMP(10);
        STAMP_FLUSH();
        return;
    }

    if (cw == 3) {
        u64 pc; asm volatile("s_getpc_b64 %0" : "=s"(pc));
        const unsigned l0 = j * 6 + (lane < 6 ? lane : 0), l = l0 < PF_CODE_LINES ? l0 : 0;
        gdma4((const char*)(pc & ~127ull) + l * 128, L_SCR + 256);
    }
    auto pf_seg = [&](int s) { gdma4(pf_addr(lane < 48 ? lane : 0, s), L_SCR + 256); };
    auto pf_wq = [&]() { const unsigned l = lane < 56 ? lane : 0; gdma4(pfW + (j * 14 + (l >> 2)) * WROW + wcol + (l & 3) * 128, L_SCR + 256); };
    auto pf_wo = [&](int part) {
        const unsigned i0 = part * 64 + lane, i = i0 < 196 ? i0 : 0, R = j * 28 + i / 7;
        gdma4((const char*)(j < 16 ? p.woA : p.woB) + (R - (j < 16 ? 0 : 448)) * XROW + (i % 7) * 128, L_SCR + 256);
    };
    u64 tag;
    {
        const rsrc_t rg = mk_rsrc(p.gate, XROW);
        const rsrc_t rp = mk_rsrc(br ? p.wphiB : p.wphiA, 16 * DH * DH * 2);
        unsigned g0, g1; u32x2 wp;
        asm volatile("s_load_dwordx2 %0, %1, 0x0" : "=&s"(tag) : "s"(p.ws) : "memory");
        asm volatile("buffer_load_ushort %0, %3, %4, 0 offen\n\tbuffer_load_ushort %1, %3, %4, 0 offen offset:512\n\tbuffer_load_dwordx2 %2, %5, %6, %7 offen"
                     : "=&v"(g0), "=&v"(g1), "=&v"(wp) : "v"(tid * 2), "s"(rg), "v"(tid * 8), "s"(rp), "s"(h * (DH * DH * 2)) : "memory");
        asm volatile("s_waitcnt vmcnt(0) lgkmcnt(0)" : "+v"(g0), "+v"(g1), "+v"(wp), "+s"(tag) :: "memory");
        *(LAS u32x2*)lds_at(L_GT + (tid & 63) * 64 + (tid >> 6) * 8) = (u32x2){g0 & 0xffffu, 0u};
        *(LAS u32x2*)lds_at(L_GT + (tid & 63) * 64 + (4 + (tid >> 6)) * 8) = (u32x2){g1 & 0xffffu, 0u};
        *(LAS u32x2*)lds_at(L_WPHI + tid * 8) = wp;
        if (tid < 5) *(volatile LAS unsigned*)lds_at(L_XFREE + 4 * tid) = 0u;
    }
    tag += 1;
    __builtin_amdgcn_s_setprio(2);
    LGKM_BAR();
    STAMP(2);
    bf16x8 aphi[2];
    build_aphi(aphi, r, h2);
    PINV(aphi[0]); PINV(aphi[1]);
    const unsigned laneW = SLOT_W + rowpart + coloff;
    f32x16 vinit;
#pragma unroll
    for (int i = 0; i < 16; ++i) vinit[i] = r == 28 ? 1.f : 0.f;
    PINV(vinit);
    f32x16 kT[2] = {zero16, zero16}, vv[2] = {vinit, vinit};
    {
        unsigned aX[4], aW, aV;
        auto addr = [&](int s) {
#pragma unroll
            for (int ks = 0; ks < 4; ++ks) { aX[ks] = (laneX ^ (32 * ks)) + slot_of(s); PINV(aX[ks]); }
            aW = laneW + slot_of(s); PINV(aW);
            aV = cq == 7 ? L_GT + rowpart + 56 : aW + 4096; PINV(aV);
        };
        Frag F[2];
#define RD_X(f, ks) do { f.x0 = lds_b128(aX[ks]); } while (0)
#define RD_X1(f, ks) do { f.x1 = lds_b128(aX[ks] + 4096); } while (0)
#define RD_K(f, ks) do { f.fk = tr_frag(aW + ks * 1024, aW + ks * 1024 + 256); } while (0)
#define RD_V(f, ks) do { f.fv = tr_frag(aV + ks * 1024, aV + ks * 1024 + 256); } while (0)
        addr(0);
        RD_X(F[0], 0); RD_K(F[0], 0); RD_X1(F[0], 0); RD_V(F[0], 0); SB();
#pragma unroll
        for (int n = 0; n < 28; ++n) {
            Frag& c = F[n & 1];
            Frag& x = F[(n + 1) & 1];
            const int nks = (n + 1) & 3;
            const bool more = n + 1 < 28;
            kT[0] = MFMA(c.fk, c.x0, kT[0]); SB();
            if (more && nks == 0) { LGKM_BAR(); addr((n + 1) >> 2); SB(); }
            if (n == 1 + 4 * PF_WQ_SEG) { if (cw == 3) pf_wq(); SB(); }
            if (n == 1 + 4 * PF_WO_SEG) { pf_wo(cw); SB(); }
            if (more) { RD_X(x, nks); RD_K(x, nks); } SB();
            kT[1] = MFMA(c.fk, c.x1, kT[1]); SB();
            if (more) { RD_X1(x, nks); } SB();
            vv[0] = MFMA(c.x0, c.fv, vv[0]); SB();
            if (more) { RD_V(x, nks); } SB();
            vv[1] = MFMA(c.x1, c.fv, vv[1]); SB();
        }
    }
    STAMP(3);

    const unsigned phiW = L_PHI + cw * 8192;
    f32x16 pk[2];
    {
        const bf16x8 k00 = pack8(kT[0], 0), k10 = pack8(kT[1], 0);
        pk[0] = MFMA(aphi[0], k00, zero16); pk[1] = MFMA(aphi[0], k10, zero16);
        const bf16x8 k01 = pack8(kT[0], 1), k11 = pack8(kT[1], 1);
        pk[0] = MFMA(aphi[1], k01, pk[0]); pk[1] = MFMA(aphi[1], k11, pk[1]);
    }
    bf16x8 vp[2][2];
#pragma unroll
    for (int b = 0; b < 2; ++b)
#pragma unroll
        for (int s = 0; s < 2; ++s) { vp[b][s] = pack8(vv[b], s); PINV(vp[b][s]); }
    SB();
    float cp[2], cn[2];
    row_extrema(pk[0], pk[1], cp, cn);
    const unsigned pt0 = 4 * h2 + q4;
    const unsigned pa0 = phiW + pt0 * 64 + 8 * (cq ^ ((pt0 >> 1) & 7));
    const unsigned pa1 = (pa0 ^ 32) + 8 * 64;
    f32x16 kvp = zero16, kvn = zero16;
#pragma unroll
    for (int b = 0; b < 2; ++b) {
        f32x2 ep[7], en[7];
        exp_pair(pk[b], cp[b], cn[b], ep, en);
        f32x2 sp = ep[0], sn = en[0];
#pragma unroll
        for (int i = 1; i < 7; ++i) { sp += ep[i]; sn += en[i]; }
        const float ip = __builtin_amdgcn_rcpf(xhalf_sum(sp[0] + sp[1])), in = __builtin_amdgcn_rcpf(xhalf_sum(sn[0] + sn[1]));
        const f32x2 ip2 = {ip, ip}, in2 = {in, in};
        const unsigned base = phiW + b * 4096 + r * 64;
#pragma unroll
        for (int g = 0; g < 4; ++g) {
            const unsigned a = base + 8 * ((2 * g + h2) ^ ((r >> 1) & 7));
            const u32x2 dp = {cvtpk2(ep[2 * g] * ip2), g < 3 ? cvtpk2(ep[(2 * g + 1) % 7] * ip2) : 0u};
            const u32x2 dn = {cvtpk2(en[2 * g] * in2), g < 3 ? cvtpk2(en[(2 * g + 1) % 7] * in2) : 0u};
            *(LAS u32x2*)lds_at(a) = dp;
            *(LAS u32x2*)lds_at(a + 2048) = dn;
        }
#pragma unroll
        for (int s = 0; s < 2; ++s) {
            const unsigned o = b * 4096 + s * 1024;
            kvp = MFMA(tr_frag(pa0 + o, pa1 + o), vp[b][s], kvp);
            kvn = MFMA(tr_frag(pa0 + o + 2048, pa1 + o + 2048), vp[b][s], kvn);
        }
    }
    asm volatile("s_waitcnt lgkmcnt(0)" ::: "memory");
#pragma unroll
    for (int f = 0; f < 4; ++f)
#pragma unroll
        for (int hh = 0; hh < 2; ++hh) {
            const f32x16& t = f < 2 ? kvp : kvn;
            const int o = 8 * (f & 1) + 4 * hh;
            *(LAS f32x4*)lds_at(phiW + f * 2048 + hh * 1024 + lane * 16) = (f32x4){t[o], t[o + 1], t[o + 2], t[o + 3]};
        }
    STAMP(4);
    LGKM_BAR();
    STAMP(5);
    {
        f32x4 t[4][2];
#pragma unroll
        for (int u = 0; u < 4; ++u) {
            t[u][0] = *(const LAS f32x4*)lds_at(L_PHI + u * 8192 + cw * 2048 + lane * 16);
            t[u][1] = *(const LAS f32x4*)lds_at(L_PHI + u * 8192 + cw * 2048 + 1024 + lane * 16);
        }
        SB();
        float a[8];
#pragma unroll
        for (int i = 0; i < 8; ++i) {
            float x01 = addf(t[0][i >> 2][i & 3], t[1][i >> 2][i & 3]), x23 = addf(t[2][i >> 2][i & 3], t[3][i >> 2][i & 3]);
            a[i] = addf(x01, x23);
        }
        u32x4 o = {cvtpk(a[0], a[1]), cvtpk(a[2], a[3]), cvtpk(a[4], a[5]), cvtpk(a[6], a[7])};
        STAMPX(1, o);
        __builtin_amdgcn_raw_buffer_store_b128(o, rws, lane * 16, WS_KVP + ((bh * 8 + tb) * 4 + cw) * 1024, 16);
    }
    {
        const rsrc_t roA = mk_rsrc(p.woA, D_ * XROW), roB = mk_rsrc(p.woB, D_ * XROW);
        const unsigned vwo = (lane >> 3) * XROW + 16 * ((lane & 7) ^ (4 * ((lane >> 4) & 1)));
#pragma unroll
        for (int m = 0; m < 2; ++m) {
            dma16(roA, vwo, (256 + 16 * cw + 8 * m) * XROW + oc * 128, L_WO + (256 + 16 * cw + 8 * m) * 128);
            dma16(roB, vwo, (576 - 448 + 16 * cw + 8 * m) * XROW + oc * 128, L_WO + (576 + 16 * cw + 8 * m) * 128);
        }
#pragma unroll 1
        for (unsigned spins = 0; spins < SPIN_CAP; ++spins) { if (*(volatile LAS unsigned*)lds_at(L_XFREE + 4 * cw) != 0u) break; }
#pragma unroll
        for (int m = 0; m < 6; ++m) dma16(roA, vwo, (64 * cw + 8 * m) * XROW + oc * 128, L_WO + (64 * cw + 8 * m) * 128);
        WAIT_VM(10);
        if (lane == 0) __hip_atomic_store((GAS u64*)((char*)p.ws + WS_F1 + ((bh * 4 + cw) * 8 + tb) * 128), tag, __ATOMIC_RELAXED, __HIP_MEMORY_SCOPE_AGENT);
        STAMP(9);
#pragma unroll
        for (int m = 6; m < 8; ++m) dma16(roA, vwo, (64 * cw + 8 * m) * XROW + oc * 128, L_WO + (64 * cw + 8 * m) * 128);
        const rsrc_t roC = cw < 2 ? roA : roB;
        const unsigned R = 320 + 64 * cw, Rg = cw < 2 ? R : R - 448;
#pragma unroll
        for (int m = 0; m < 8; ++m) dma16(roC, vwo, (Rg + 8 * m) * XROW + oc * 128, L_WO + (R + 8 * m) * 128);
    }
    bf16x8 sel;
    {
        const int jj = (r & 3) + 4 * (r >> 3);
        const bool on = r < 16 && ((r >> 2) & 1) == h2;
        const unsigned one = on ? (0x3F80u << (16 * (jj & 1))) : 0u;
        sel = __builtin_bit_cast(bf16x8, (u32x4){(jj >> 1) == 0 ? one : 0u, (jj >> 1) == 1 ? one : 0u, (jj >> 1) == 2 ? one : 0u, (jj >> 1) == 3 ? one : 0u});
        PINV(sel);
    }
    STAMP(6);
    wait_flags((const GAS u64*)((const char*)p.ws + WS_F1 + ((bh * 4 + cw) * 8 + (lane & 7)) * 128), tag);
    STAMP(7);
    {
        const int hr = (r >> 2) & 1, reg = (r & 3) + 4 * (r >> 3);
        const int col = reg < 14 ? 14 * hr + reg : 28;
        const unsigned vo = (h2 * 32 + col) * 16;
        u32x4 part[8];
#pragma unroll
        for (int u = 0; u < 8; ++u) part[u] = __builtin_amdgcn_raw_buffer_load_b128(rws, vo, WS_KVP + ((bh * 8 + u) * 4 + cw) * 1024, 16);
        SB();
        f32x16 acc = zero16;
#pragma unroll
        for (int u = 0; u < 8; ++u) acc = MFMA(sel, __builtin_bit_cast(bf16x8, part[u]), acc);
        u32x4 o = __builtin_bit_cast(u32x4, pack8(acc, 0));
        if (reg == 15) o = (u32x4){ONE2, ONE2, ONE2, ONE2};
        *(LAS u32x4*)lds_at(L_KVA + cw * 1024 + lane * 16) = o;
    }
    LGKM_BAR();
    STAMP(8);
    if (!has_out) { STAMP_FLUSH(); return; }
    {
        const rsrc_t ro = mk_rsrc(p.woB, D_ * XROW);
        const unsigned vo = (lane >> 3) * XROW + 16 * ((lane & 7) ^ (4 * ((lane >> 4) & 1)));
#pragma unroll
        for (unsigned n = 0; n < 8; ++n) {
            const unsigned R = 640 + 64 * cw + 8 * n;
            dma16(ro, vo, (R - 448) * XROW + oc * 128, L_WO + R * 128);
        }
    }
    WAIT_VM(0);
    if (lane == 0) __hip_atomic_fetch_add((LAS unsigned*)lds_at(L_WOCNT), 1u, __ATOMIC_RELAXED, __HIP_MEMORY_SCOPE_WORKGROUP);
#pragma unroll 1
    for (unsigned spins = 0; spins < SPIN_CAP; ++spins) { if (*(volatile LAS unsigned*)lds_at(L_WOCNT) >= 4u) break; }
    asm volatile("" ::: "memory");
    STAMP(10);

    const unsigned tk = cw & 1, kh = cw >> 1;
    constexpr u64 HORD = 0xDB9642FECA875310ull;
    const GAS u64* f2 = (const GAS u64*)((const char*)p.ws + WS_F2 + (og * 32 + kh * 16) * 128);
    f32x16 mine = zero16, other = zero16;
    {
        const unsigned colp = 32 * blk16 + 8 * p4;
        const unsigned r0 = kh * 448 + 14 * h2 + q4, r1 = kh * 448 + 14 * h2 + (q4 < 1 ? q4 : 1);
        unsigned a0 = L_WO + r0 * 128 + 64 * (kh ^ ((r0 >> 1) & 1)) + colp;
        unsigned a1 = L_WO + r1 * 128 + 64 * (kh ^ ((r1 >> 1) & 1)) + colp;
        unsigned a2 = a0 ^ 64, a3 = a1 ^ 64;
        asm volatile("" : "+v"(a0), "+v"(a1), "+v"(a2), "+v"(a3));
        u32x4 of[16][2];
        auto ld = [&](int i) {
            const unsigned hd = (unsigned)(HORD >> (4 * i)) & 15u;
#pragma unroll
            for (int s = 0; s < 2; ++s)
                of[i][s] = __builtin_amdgcn_raw_buffer_load_b128(rws, lane * 16, WS_O + (((og * 32 + kh * 16 + hd) * 2 + tk) * 2 + s) * 1024, 16);
        };
        bf16x8 A[2][4];
        auto rd = [&](bf16x8 (&f)[4], int i) {
            const unsigned o = ((unsigned)(HORD >> (4 * i)) & 15u) * 28 * 128;
            f[0] = tr_frag(a0 + o, a0 + o + 512); f[1] = tr_frag(a2 + o, a2 + o + 512);
            f[2] = tr_frag(a0 + o + 1024, a1 + o + 1536); f[3] = tr_frag(a2 + o + 1024, a3 + o + 1536);
        };
        auto step = [&](int i) {
            if (i + 1 < 16) rd(A[(i + 1) & 1], i + 1);
            SB();
            const bf16x8 b0 = __builtin_bit_cast(bf16x8, of[i][0]), b1 = __builtin_bit_cast(bf16x8, of[i][1]);
            mine = MFMA(A[i & 1][0], b0, mine); other = MFMA(A[i & 1][1], b0, other);
            mine = MFMA(A[i & 1][2], b1, mine); other = MFMA(A[i & 1][3], b1, other);
            SB();
        };
        wait_flags(f2 + 16 * ((HORD >> (4 * (lane % 10))) & 15), tag);
        STAMP(14);
#pragma unroll
        for (int i = 0; i < 10; ++i) ld(i);
        SB();
        rd(A[0], 0); SB();
#pragma unroll
        for (int i = 0; i < 10; ++i) step(i);
        STAMP(15);
        wait_flags(f2 + 16 * ((HORD >> (4 * (10 + lane % 6))) & 15), tag);
        STAMP(11);
        if (blockIdx.x == 0 && tid == 0) __hip_atomic_store((GAS u64*)((char*)p.ws + WS_EPOCH), tag, __ATOMIC_RELAXED, __HIP_MEMORY_SCOPE_AGENT);
#pragma unroll
        for (int i = 10; i < 16; ++i) ld(i);
        SB();
#pragma unroll
        for (int i = 10; i < 16; ++i) step(i);
    }
    STAMP(12);
#pragma unroll
    for (int g = 0; g < 4; ++g)
        *(LAS f32x4*)lds_at(L_PHI + cw * 4096 + g * 1024 + lane * 16) = (f32x4){other[4 * g], other[4 * g + 1], other[4 * g + 2], other[4 * g + 3]};
    LGKM_BAR();
    {
        unsigned d[8];
#pragma unroll
        for (int g = 0; g < 4; ++g) {
            const f32x4 t = *(const LAS f32x4*)lds_at(L_PHI + (cw ^ 2) * 4096 + g * 1024 + lane * 16);
            d[2 * g] = cvtpk(mine[4 * g] + t[0], mine[4 * g + 1] + t[1]);
            d[2 * g + 1] = cvtpk(mine[4 * g + 2] + t[2], mine[4 * g + 3] + t[3]);
        }
        const rsrc_t rout = mk_rsrc(p.out, S_ * XROW);
        const unsigned vo = r * XROW + h2 * 16, so = (og * 64 + tk * 32) * XROW + oc * 128 + kh * 64;
#pragma unroll
        for (int k = 0; k < 4; k += 2) {
            auto sx0 = __builtin_amdgcn_permlane32_swap(d[2 * k], d[2 * k + 2], false, false);
            auto sx1 = __builtin_amdgcn_permlane32_swap(d[2 * k + 1], d[2 * k + 3], false, false);
            const u32x4 o = {sx0[0], sx1[0], sx0[1], sx1[1]};
            __builtin_amdgcn_raw_buffer_store_b128(o, rout, vo, so + 16 * k, 0);
        }
    }
    STAMP(13);
    STAMP_FLUSH();
}

extern "C" void kernel_launch(void* const* d_in, const int* in_sizes, int n_in, void* d_out, int out_size, void* d_ws, size_t ws_size,
                              hipStream_t stream) {
    Params p;
    p.x = d_in[0]; p.wqkvA = d_in[1]; p.wphiA = d_in[2]; p.woA = d_in[3];
    p.wqkvB = d_in[5]; p.wphiB = d_in[6]; p.woB = d_in[7]; p.gate = d_in[8];
    p.out = d_out; p.ws = d_ws;
    (void)hipFuncSetAttribute((const void*)hedgehog_mix_kernel, hipFuncAttributeMaxDynamicSharedMemorySize, L_TOTAL);
    hedgehog_mix_kernel<<<256, 512, L_TOTAL, stream>>>(p);
}
```

```cpp
#define PREFETCH 0
#define PF_CODE 1
#define POLL_DEPTH 1
#define FS 8
#include <hip/hip_runtime.h>
#include <stdint.h>

typedef short bf16x8 __attribute__((ext_vector_type(8)));
typedef short s16x4 __attribute__((ext_vector_type(4)));
typedef short v4i16_t __attribute__((ext_vector_type(4)));
typedef __bf16 bf2_t __attribute__((ext_vector_type(2)));
typedef float f32x2 __attribute__((ext_vector_type(2)));
typedef float f32x4 __attribute__((ext_vector_type(4)));
typedef float f32x16 __attribute__((ext_vector_type(16)));
typedef unsigned u32x2 __attribute__((ext_vector_type(2)));
typedef unsigned u32x4 __attribute__((ext_vector_type(4)));
typedef unsigned long long u64;
#define LAS __attribute__((address_space(3)))
#define GAS __attribute__((address_space(1)))
#define DI __device__ __forceinline__
#define MFMA(a, b, c) __builtin_amdgcn_mfma_f32_32x32x16_bf16((a), (b), (c), 0, 0, 0)

constexpr int S_ = 2048, D_ = 448, DH = 28;
constexpr unsigned XROW = D_ * 2;
constexpr unsigned WROW = 3 * D_ * 2;
constexpr unsigned WS_EPOCH = 0;
constexpr unsigned F1_GRP = FS * 8 < 128 ? 128 : FS * 8, F2_GRP = FS * 16;
constexpr unsigned WS_F1 = 4096;
constexpr unsigned WS_F2 = WS_F1 + 32 * 4 * 1024;
constexpr unsigned WS_KVP = WS_F2 + 64 * 2048;
constexpr unsigned WS_O = WS_KVP + 32 * 8 * 4 * 1024;
constexpr unsigned WS_END = WS_O + 32 * 32 * 4 * 1024;
constexpr unsigned WS_STAMP = 8u << 20;
constexpr unsigned SLOT = 40960, SLOT_W = 32768;
constexpr unsigned L_B = 0, L_A = SLOT, L_C = 2 * SLOT;
constexpr unsigned L_WQ = 3 * SLOT;
constexpr unsigned L_GT = L_WQ + 448 * 64;
constexpr unsigned L_WPHI = L_GT + 4096;
constexpr unsigned L_SCR = L_WPHI + 2048;
constexpr unsigned L_XFREE = L_SCR + 512;
constexpr unsigned L_WOCNT = L_XFREE + 16;
constexpr unsigned L_PHI = L_B;
constexpr unsigned L_KVA = L_B + SLOT_W;
constexpr unsigned L_WO = L_A;
constexpr unsigned L_TOTAL = 163840;
static_assert(L_WPHI + 2048 <= L_TOTAL, "lds");
static_assert(L_WO + 896 * 128 == L_WPHI, "lds");
__host__ __device__ constexpr unsigned slot_of(int s) { return s % 3 == 0 ? L_A : (s % 3 == 1 ? L_B : L_C); }

struct Params {
    const void* x; const void* wqkvA; const void* wqkvB; const void* wphiA; const void* wphiB;
    const void* woA; const void* woB; const void* gate; void* out; void* ws;
};

typedef __amdgpu_buffer_rsrc_t rsrc_t;
DI rsrc_t mk_rsrc(const void* p, unsigned bytes) { return __builtin_amdgcn_make_buffer_rsrc((void*)p, (short)0, (int)bytes, 0x00020000); }
DI unsigned rfl(unsigned v) { return (unsigned)__builtin_amdgcn_readfirstlane((int)v); }
DI void dma16(rsrc_t r, unsigned voff, unsigned soff, unsigned lds) {
    asm volatile("s_mov_b32 m0, %0\n\ts_nop 0\n\tbuffer_load_dwordx4 %1, %2, %3 offen lds" ::"s"(lds), "v"(voff), "s"(r), "s"(soff) : "memory");
}
DI void dma_seg(rsrc_t rw, rsrc_t rx, unsigned vw, unsigned vx0, unsigned vx1, unsigned sw, unsigned sx, unsigned mw, unsigned mx) {
#define XP(v) "s_addk_i32 m0, 0x400\n\ts_addk_i32 %1, 0x1c00\n\tbuffer_load_dwordx4 " v ", %6, %1 offen lds\n\t"
    asm volatile("s_mov_b32 m0, %7\n\ts_nop 0\n\tbuffer_load_dwordx4 %2, %5, %0 offen lds\n\t"
                 "s_addk_i32 m0, 0x1000\n\ts_addk_i32 %0, 0x380\n\tbuffer_load_dwordx4 %2, %5, %0 offen lds\n\t"
                 "s_mov_b32 m0, %8\n\ts_nop 0\n\tbuffer_load_dwordx4 %3, %6, %1 offen lds\n\t"
                 XP("%4") XP("%3") XP("%4") XP("%3") XP("%4") XP("%3") XP("%4")
                 : "+s"(sw), "+s"(sx) : "v"(vw), "v"(vx0), "v"(vx1), "s"(rw), "s"(rx), "s"(mw), "s"(mx) : "scc", "memory");
#undef XP
}
template <int N> DI void dma_rows(rsrc_t r, unsigned v, unsigned so, unsigned m) {
    static_assert(N == 2 || N == 6 || N == 8, "");
#define RP "s_addk_i32 m0, 0x400\n\ts_addk_i32 %0, 0x1c00\n\tbuffer_load_dwordx4 %1, %2, %0 offen lds\n\t"
#define R0 "s_mov_b32 m0, %3\n\ts_nop 0\n\tbuffer_load_dwordx4 %1, %2, %0 offen lds\n\t"
    if (N == 2) asm volatile(R0 RP : "+s"(so) : "v"(v), "s"(r), "s"(m) : "scc", "memory");
    if (N == 6) asm volatile(R0 RP RP RP RP RP : "+s"(so) : "v"(v), "s"(r), "s"(m) : "scc", "memory");
    if (N == 8) asm volatile(R0 RP RP RP RP RP RP RP : "+s"(so) : "v"(v), "s"(r), "s"(m) : "scc", "memory");
#undef RP
#undef R0
}
DI void dma4(rsrc_t r, unsigned voff, unsigned soff, unsigned lds) {
    asm volatile("s_mov_b32 m0, %0\n\ts_nop 0\n\tbuffer_load_dword %1, %2, %3 offen lds" ::"s"(lds), "v"(voff), "s"(r), "s"(soff) : "memory");
}
#define PF_CODE_LINES 168
DI void gdma4(const void* a, unsigned lds) {
    asm volatile("s_mov_b32 m0, %0\n\ts_nop 0\n\tglobal_load_lds_dword %1, off" ::"s"(lds), "v"(a) : "memory");
}
#define PF_STAG 0
#define PF_WQ_SEG 1
#define PF_WO_SEG 2
#define WAIT_VM(n) asm volatile("s_waitcnt vmcnt(" #n ")" ::: "memory")
#define WAIT_VM_LGKM_BAR(n) asm volatile("s_waitcnt vmcnt(" #n ") lgkmcnt(0)\n\ts_barrier" ::: "memory")
#define LGKM_BAR() asm volatile("s_waitcnt lgkmcnt(0)\n\ts_barrier" ::: "memory")

extern __shared__ __attribute__((aligned(16))) char smem_dyn[];
DI LAS char* lds_at(unsigned off) { return (LAS char*)(uintptr_t)off; }
DI bf16x8 lds_b128(unsigned a) { return *(const LAS bf16x8*)lds_at(a); }
DI s16x4 lds_tr(unsigned a) { return __builtin_bit_cast(s16x4, __builtin_amdgcn_ds_read_tr16_b64_v4i16((LAS v4i16_t*)lds_at(a))); }
DI bf16x8 tr_frag(unsigned a0, unsigned a1) { const s16x4 lo = lds_tr(a0), hi = lds_tr(a1); return __builtin_shufflevector(lo, hi, 0, 1, 2, 3, 4, 5, 6, 7); }
DI unsigned cvtpk(float lo, float hi) { const f32x2 v = {lo, hi}; return __builtin_bit_cast(unsigned, __builtin_convertvector(v, bf2_t)); }
DI bf16x8 pack8(const f32x16& x, int s) {
    const u32x4 p = {cvtpk(x[8 * s], x[8 * s + 1]), cvtpk(x[8 * s + 2], x[8 * s + 3]), cvtpk(x[8 * s + 4], x[8 * s + 5]), cvtpk(x[8 * s + 6], x[8 * s + 7])};
    return __builtin_bit_cast(bf16x8, p);
}
DI float addf(float a, float b) { float r; asm("v_add_f32 %0, %1, %2" : "=v"(r) : "v"(a), "v"(b)); return r; }
DI float swap_lo(float v) { auto r = __builtin_amdgcn_permlane32_swap(__float_as_uint(v), __float_as_uint(v), false, false); return __uint_as_float(r[0]); }
DI float swap_hi(float v) { auto r = __builtin_amdgcn_permlane32_swap(__float_as_uint(v), __float_as_uint(v), false, false); return __uint_as_float(r[1]); }
DI float xhalf_max(float v) { auto r = __builtin_amdgcn_permlane32_swap(__float_as_uint(v), __float_as_uint(v), false, false); return __builtin_fmaxf(__uint_as_float(r[0]), __uint_as_float(r[1])); }
DI float xhalf_min(float v) { auto r = __builtin_amdgcn_permlane32_swap(__float_as_uint(v), __float_as_uint(v), false, false); return __builtin_fminf(__uint_as_float(r[0]), __uint_as_float(r[1])); }
DI float xhalf_sum(float v) { auto r = __builtin_amdgcn_permlane32_swap(__float_as_uint(v), __float_as_uint(v), false, false); return __uint_as_float(r[0]) + __uint_as_float(r[1]); }
constexpr float L2E = 1.4426950408889634f;
constexpr unsigned ONE2 = 0x3F803F80u;
constexpr unsigned SPIN_CAP = 1u << 16;

#define STAMP_DECL do { } while (0)
#define STAMP(i) do { } while (0)
#define STAMPX(i, x) do { } while (0)
#define STAMP_FLUSH() do { } while (0)

DI void row_extrema(const f32x16& a, const f32x16& b, float (&cp)[2], float (&cn)[2]) {
    float mxa = __builtin_fmaxf(a[0], a[1]), mna = __builtin_fminf(a[0], a[1]), mxb = __builtin_fmaxf(b[0], b[1]), mnb = __builtin_fminf(b[0], b[1]);
#pragma unroll
    for (int i = 2; i < 14; i += 2) {
        mxa = __builtin_fmaxf(__builtin_fmaxf(mxa, a[i]), a[i + 1]); mxb = __builtin_fmaxf(__builtin_fmaxf(mxb, b[i]), b[i + 1]);
        mna = __builtin_fminf(__builtin_fminf(mna, a[i]), a[i + 1]); mnb = __builtin_fminf(__builtin_fminf(mnb, b[i]), b[i + 1]);
    }
    cp[0] = -xhalf_max(mxa) * L2E; cp[1] = -xhalf_max(mxb) * L2E;
    cn[0] = xhalf_min(mna) * L2E; cn[1] = xhalf_min(mnb) * L2E;
}
DI void exp_pair(const f32x16& pp, float cp, float cn, f32x2 (&ep)[7], f32x2 (&en)[7]) {
    const f32x2 l2 = {L2E, L2E}, cp2 = {cp, cp}, cn2 = {cn, cn};
#pragma unroll
    for (int i = 0; i < 7; ++i) {
        const f32x2 v = {pp[2 * i], pp[2 * i + 1]};
        const f32x2 a = __builtin_elementwise_fma(v, l2, cp2), b = __builtin_elementwise_fma(v, -l2, cn2);
        ep[i] = (f32x2){__builtin_amdgcn_exp2f(a[0]), __builtin_amdgcn_exp2f(a[1])};
        en[i] = (f32x2){__builtin_amdgcn_exp2f(b[0]), __builtin_amdgcn_exp2f(b[1])};
    }
}
DI unsigned cvtpk2(f32x2 v) { return __builtin_bit_cast(unsigned, __builtin_convertvector(v, bf2_t)); }

DI void wait_flags(const GAS u64* f, u64 tag) {
#pragma unroll 1
    for (unsigned spins = 0; spins < SPIN_CAP; ++spins) {
        const u64 v = __hip_atomic_load(f, __ATOMIC_RELAXED, __HIP_MEMORY_SCOPE_AGENT);
        if (__builtin_amdgcn_ballot_w64(v != tag) == 0) break;
        __builtin_amdgcn_s_sleep(1);
    }
    asm volatile("" ::: "memory");
}

#define SB() __builtin_amdgcn_sched_barrier(0)
#define PINV(x) asm volatile("" : "+v"(x))
struct Frag { bf16x8 x0, x1, fk, fv; };

DI void build_aphi(bf16x8 (&aphi)[2], int r, int h2) {
    const int hr = (r >> 2) & 1, reg = (r & 3) + 4 * (r >> 3), f = 14 * hr + (reg < 14 ? reg : 0);
    const unsigned a = L_WPHI + 2 * (4 * h2 * DH + f);
#pragma unroll
    for (int s = 0; s < 2; ++s)
#pragma unroll
        for (int jj = 0; jj < 8; ++jj) {
            const int d0 = 16 * s + 8 * (jj >> 2) + (jj & 3);
            const bool ok = reg < 14 && (d0 + 4 < DH || h2 == 0);
            const unsigned short v = *(const LAS unsigned short*)lds_at(a + 2 * d0 * DH);
            aphi[s][jj] = ok ? (short)v : (short)0;
        }
}

__global__ void __launch_bounds__(512, 2) hedgehog_mix_kernel(Params p) {
    const int tid = threadIdx.x, lane = tid & 63;
    const unsigned w = rfl(tid >> 6), cw = w & 3;
    if (w == 4) {
        u64 pc; asm volatile("s_getpc_b64 %0" : "=s"(pc));
        const unsigned l0 = (blockIdx.x >> 3) * 6 + (lane < 6 ? lane : 5), l = l0 < PF_CODE_LINES ? l0 : 0;
        gdma4((const char*)(pc & ~127ull) + l * 128, L_SCR + 256);
    }
    const int r = lane & 31, h2 = lane >> 5, i16 = lane & 15, q4 = i16 >> 2, p4 = i16 & 3, blk16 = (lane >> 4) & 1;
    const unsigned xcd = blockIdx.x & 7, j = blockIdx.x >> 3;
    const unsigned tb = (xcd & 1) * 4 + (j & 3), bh = (xcd >> 1) * 8 + (j >> 2), br = bh >> 4, h = bh & 15;
    const unsigned og = xcd * 4 + j / 7, oc = j % 7;
    const bool has_out = j < 28;
#define PIN_ARGS() asm volatile("" : "+s"(p.x), "+s"(p.wqkvA), "+s"(p.wqkvB), "+s"(p.wphiA), "+s"(p.wphiB), "+s"(p.woA), "+s"(p.woB), "+s"(p.gate), "+s"(p.out), "+s"(p.ws))
    PIN_ARGS();
    STAMP_DECL;
    STAMP(0);
    const rsrc_t rws = mk_rsrc(p.ws, WS_END);
    const f32x16 zero16 = {};
    const int cq = 4 * blk16 + p4;
    const unsigned rowpart = (8 * h2 + q4) * 64;
    const unsigned coloff = (8 * (h & 1) + 8 * cq) & 63;
    const unsigned laneX = cw * 8192 + r * 128 + 16 * (h2 ^ ((r >> 1) & 7));

    if (__builtin_expect(w >= 4, 1)) {
        const rsrc_t rx = mk_rsrc(p.x, S_ * XROW);
        const rsrc_t rw = mk_rsrc(br ? p.wqkvB : p.wqkvA, D_ * WROW);
        const unsigned vx0 = (lane >> 3) * XROW + 16 * ((lane & 7) ^ (lane >> 4));
        const unsigned vx1 = (lane >> 3) * XROW + 16 * ((lane & 7) ^ (4 + (lane >> 4)));
        const unsigned vw = (lane >> 2) * WROW + 16 * (lane & 3);
        const unsigned sx = (tb * 256 + cw * 64) * XROW;
        const unsigned sw = cw * 16 * WROW + h * 56 - 8 * (h & 1);
        auto issue = [&](int s) {
            dma_seg(rw, rx, vw, vx0, vx1, sw + s * 64 * WROW + 896, sx + s * 128, slot_of(s) + SLOT_W + cw * 1024, slot_of(s) + cw * 8192);
        };
        bf16x8 xs[5][4][2];
        auto grab = [&](int s) {
#pragma unroll
            for (int ks = 0; ks < 4; ++ks) {
                const unsigned a = (laneX ^ (32 * ks)) + slot_of(s);
                xs[s][ks][0] = lds_b128(a); xs[s][ks][1] = lds_b128(a + 4096);
            }
        };
        STAMP(11);
        issue(0); issue(1);
        STAMP(1);
        WAIT_VM_LGKM_BAR(10);
        STAMP(2);
        grab(0); issue(2); WAIT_VM_LGKM_BAR(10);
        grab(1); issue(3); WAIT_VM_LGKM_BAR(10);
        grab(2); issue(4); WAIT_VM_LGKM_BAR(10);
        grab(3); issue(5); WAIT_VM_LGKM_BAR(10);
        grab(4); issue(6); WAIT_VM_LGKM_BAR(10);
        WAIT_VM_LGKM_BAR(0);
        STAMP(3);
#pragma unroll
        for (int s = 0; s < 7; ++s) dma16(rw, vw, sw + s * 64 * WROW, L_WQ + s * 4096 + cw * 1024);
        bf16x8 aphi[2];
        build_aphi(aphi, r, h2);
        PINV(aphi[0]); PINV(aphi[1]);
        u64 tag;
        asm volatile("s_waitcnt lgkmcnt(0)\n\ts_load_dwordx2 %0, %1, 0x0\n\ts_waitcnt lgkmcnt(0)" : "=&s"(tag) : "s"(p.ws) : "memory");
        tag += 1;
        STAMP(4);
        WAIT_VM_LGKM_BAR(0);
        STAMP(5);
        f32x16 qT[2] = {zero16, zero16};
        {
            const unsigned aq_real = L_WQ + rowpart + coloff, aq_pad = L_GT + rowpart;
            bf16x8 fq[3], xl[2][2];
            auto seg_of = [](int n) { return n < 8 ? 5 + (n >> 2) : (n - 8) >> 2; };
            unsigned aQ = 0;
            auto rdq = [&](int n) {
                if ((n & 3) == 0) { aQ = cq == 7 ? aq_pad + 8 * seg_of(n) : aq_real + 4096 * seg_of(n); PINV(aQ); }
                fq[n % 3] = tr_frag(aQ + (n & 3) * 1024, aQ + (n & 3) * 1024 + 256);
            };
            auto rdx = [&](int n) { const unsigned a = (laneX ^ (32 * (n & 3))) + slot_of(seg_of(n)); xl[n & 1][0] = lds_b128(a); xl[n & 1][1] = lds_b128(a + 4096); };
            rdq(0); rdx(0); rdq(1);
            SB();
#pragma unroll
            for (int n = 0; n < 28; ++n) {
                const int s = seg_of(n), ks = n & 3;
                if (n + 1 < 8) rdx(n + 1);
                if (n + 2 < 28) rdq(n + 2);
                SB();
                qT[0] = MFMA(fq[n % 3], n >= 8 ? xs[n >= 8 ? s : 0][ks][0] : xl[n & 1][0], qT[0]);
                qT[1] = MFMA(fq[n % 3], n >= 8 ? xs[n >= 8 ? s : 0][ks][1] : xl[n & 1][1], qT[1]);
                SB();
                if (n == 7) {
                    asm volatile("s_waitcnt lgkmcnt(0)" ::: "memory");
                    *(volatile LAS unsigned*)lds_at(L_XFREE + 4 * cw) = 1u;
                }
            }
        }
        STAMP(6);
        bf16x8 eq[2][2][2];
        float gs[2];
        {
            f32x16 pq[2];
            const bf16x8 q00 = pack8(qT[0], 0), q10 = pack8(qT[1], 0);
            pq[0] = MFMA(aphi[0], q00, zero16); pq[1] = MFMA(aphi[0], q10, zero16);
            const bf16x8 q01 = pack8(qT[0], 1), q11 = pack8(qT[1], 1);
            pq[0] = MFMA(aphi[1], q01, pq[0]); pq[1] = MFMA(aphi[1], q11, pq[1]);
#pragma unroll
            for (int b = 0; b < 2; ++b) {
                const float lg = swap_hi(qT[b][12]);
                gs[b] = __builtin_amdgcn_rcpf(1.f + __builtin_amdgcn_exp2f(br ? lg * L2E : -lg * L2E));
                PINV(gs[b]);
            }
            float cp[2], cn[2];
            row_extrema(pq[0], pq[1], cp, cn);
            PINV(cp[0]); PINV(cp[1]); PINV(cn[0]); PINV(cn[1]);
#pragma unroll
            for (int b = 0; b < 2; ++b) {
                const f32x2 l2 = {L2E, L2E}, cp2 = {cp[b], cp[b]}, cn2 = {cn[b], cn[b]};
                unsigned pe[8], ne[8];
#pragma unroll
                for (int i = 0; i < 7; ++i) {
                    const f32x2 v = {pq[b][2 * i], pq[b][2 * i + 1]};
                    const f32x2 a = __builtin_elementwise_fma(v, l2, cp2), c = __builtin_elementwise_fma(v, -l2, cn2);
                    pe[i] = cvtpk2((f32x2){__builtin_amdgcn_exp2f(a[0]), __builtin_amdgcn_exp2f(a[1])});
                    ne[i] = cvtpk2((f32x2){__builtin_amdgcn_exp2f(c[0]), __builtin_amdgcn_exp2f(c[1])});
                }
                pe[7] = 0; ne[7] = 0;
                eq[b][0][0] = __builtin_bit_cast(bf16x8, (u32x4){pe[0], pe[1], pe[2], pe[3]}); eq[b][0][1] = __builtin_bit_cast(bf16x8, (u32x4){pe[4], pe[5], pe[6], pe[7]});
                eq[b][1][0] = __builtin_bit_cast(bf16x8, (u32x4){ne[0], ne[1], ne[2], ne[3]}); eq[b][1][1] = __builtin_bit_cast(bf16x8, (u32x4){ne[4], ne[5], ne[6], ne[7]});
            }
        }
#pragma unroll
        for (int b = 0; b < 2; ++b) asm volatile("" : "+v"(eq[b][0][0]), "+v"(eq[b][0][1]), "+v"(eq[b][1][0]), "+v"(eq[b][1][1]), "+v"(gs[b]));
        STAMP(7);
        LGKM_BAR();
        __builtin_amdgcn_s_setprio(3);
        STAMP(8);
        {
            bf16x8 akv[4];
#pragma unroll
            for (int f = 0; f < 4; ++f) akv[f] = lds_b128(L_KVA + f * 1024 + lane * 16);
#pragma unroll
            for (int b = 0; b < 2; ++b) {
                f32x16 np = MFMA(akv[0], eq[b][0][0], zero16); np = MFMA(akv[1], eq[b][0][1], np);
                f32x16 nn = MFMA(akv[2], eq[b][1][0], zero16); nn = MFMA(akv[3], eq[b][1][1], nn);
                const float ia = __builtin_amdgcn_rcpf(np[15]), ib = __builtin_amdgcn_rcpf(nn[15]);
                const float den = np[14] * ia + nn[14] * ib + 1e-6f;
                const float sc = gs[b] * __builtin_amdgcn_rcpf(den);
                const float ca = ia * sc, cb = ib * sc;
                float o[14];
#pragma unroll
                for (int i = 0; i < 14; ++i) o[i] = np[i] * ca + nn[i] * cb;
                const u32x4 o0 = {cvtpk(o[0], o[1]), cvtpk(o[2], o[3]), cvtpk(o[4], o[5]), cvtpk(o[6], o[7])};
                const u32x4 o1 = {cvtpk(o[8], o[9]), cvtpk(o[10], o[11]), cvtpk(o[12], o[13]), 0u};
                const unsigned so = WS_O + (((tb * 4 + cw) * 32 + bh) * 4 + b * 2) * 1024;
                __builtin_amdgcn_raw_buffer_store_b128(o0, rws, lane * 16, so, 16);
                __builtin_amdgcn_raw_buffer_store_b128(o1, rws, lane * 16, so + 1024, 16);
            }
        }
        STAMP(9);
        WAIT_VM(0);
        if (lane == 0) __hip_atomic_store((GAS u64*)((char*)p.ws + WS_F2 + ((tb * 4 + cw) * 2 + br) * F2_GRP + h * FS), tag, __ATOMIC_RELAXED, __HIP_MEMORY_SCOPE_AGENT);
        STAMP(10);
        STAMP_FLUSH();
        return;
    }

    u64 tag;
    {
        const rsrc_t rg = mk_rsrc(p.gate, XROW);
        const rsrc_t rp = mk_rsrc(br ? p.wphiB : p.wphiA, 16 * DH * DH * 2);
        unsigned g0, g1; u32x2 wp;
        asm volatile("s_load_dwordx2 %0, %1, 0x0" : "=&s"(tag) : "s"(p.ws) : "memory");
        asm volatile("buffer_load_ushort %0, %3, %4, 0 offen\n\tbuffer_load_ushort %1, %3, %4, 0 offen offset:512\n\tbuffer_load_dwordx2 %2, %5, %6, %7 offen"
                     : "=&v"(g0), "=&v"(g1), "=&v"(wp) : "v"(tid * 2), "s"(rg), "v"(tid * 8), "s"(rp), "s"(h * (DH * DH * 2)) : "memory");
        asm volatile("s_waitcnt vmcnt(0) lgkmcnt(0)" : "+v"(g0), "+v"(g1), "+v"(wp), "+s"(tag) :: "memory");
        *(LAS u32x2*)lds_at(L_GT + (tid & 63) * 64 + (tid >> 6) * 8) = (u32x2){g0 & 0xffffu, 0u};
        *(LAS u32x2*)lds_at(L_GT + (tid & 63) * 64 + (4 + (tid >> 6)) * 8) = (u32x2){g1 & 0xffffu, 0u};
        *(LAS u32x2*)lds_at(L_WPHI + tid * 8) = wp;
        if (tid < 5) *(volatile LAS unsigned*)lds_at(L_XFREE + 4 * tid) = 0u;
    }
    tag += 1;
    __builtin_amdgcn_s_setprio(2);
    LGKM_BAR();
    STAMP(2);
    bf16x8 aphi[2];
    build_aphi(aphi, r, h2);
    PINV(aphi[0]); PINV(aphi[1]);
    const unsigned laneW = SLOT_W + rowpart + coloff;
    f32x16 vinit;
#pragma unroll
    for (int i = 0; i < 16; ++i) vinit[i] = r == 28 ? 1.f : 0.f;
    PINV(vinit);
    f32x16 kT[2] = {zero16, zero16}, vv[2] = {vinit, vinit};
    {
        unsigned aX[4], aW, aV;
        auto addr = [&](int s) {
#pragma unroll
            for (int ks = 0; ks < 4; ++ks) { aX[ks] = (laneX ^ (32 * ks)) + slot_of(s); PINV(aX[ks]); }
            aW = laneW + slot_of(s); PINV(aW);
            aV = cq == 7 ? L_GT + rowpart + 56 : aW + 4096; PINV(aV);
        };
        Frag F[2];
#define RD_X(f, ks) do { f.x0 = lds_b128(aX[ks]); } while (0)
#define RD_X1(f, ks) do { f.x1 = lds_b128(aX[ks] + 4096); } while (0)
#define RD_K(f, ks) do { f.fk = tr_frag(aW + ks * 1024, aW + ks * 1024 + 256); } while (0)
#define RD_V(f, ks) do { f.fv = tr_frag(aV + ks * 1024, aV + ks * 1024 + 256); } while (0)
        addr(0);
        RD_X(F[0], 0); RD_K(F[0], 0); RD_X1(F[0], 0); RD_V(F[0], 0); SB();
#pragma unroll
        for (int n = 0; n < 28; ++n) {
            Frag& c = F[n & 1];
            Frag& x = F[(n + 1) & 1];
            const int nks = (n + 1) & 3;
            const bool more = n + 1 < 28;
            kT[0] = MFMA(c.fk, c.x0, kT[0]); SB();
            if (more && nks == 0) { LGKM_BAR(); addr((n + 1) >> 2); SB(); }
            if (more) { RD_X(x, nks); RD_K(x, nks); } SB();
            kT[1] = MFMA(c.fk, c.x1, kT[1]); SB();
            if (more) { RD_X1(x, nks); } SB();
            vv[0] = MFMA(c.x0, c.fv, vv[0]); SB();
            if (more) { RD_V(x, nks); } SB();
            vv[1] = MFMA(c.x1, c.fv, vv[1]); SB();
        }
    }
    STAMP(3);

    const unsigned phiW = L_PHI + cw * 8192;
    f32x16 pk[2];
    {
        const bf16x8 k00 = pack8(kT[0], 0), k10 = pack8(kT[1], 0);
        pk[0] = MFMA(aphi[0], k00, zero16); pk[1] = MFMA(aphi[0], k10, zero16);
        const bf16x8 k01 = pack8(kT[0], 1), k11 = pack8(kT[1], 1);
        pk[0] = MFMA(aphi[1], k01, pk[0]); pk[1] = MFMA(aphi[1], k11, pk[1]);
    }
    bf16x8 vp[2][2];
#pragma unroll
    for (int b = 0; b < 2; ++b)
#pragma unroll
        for (int s = 0; s < 2; ++s) { vp[b][s] = pack8(vv[b], s); PINV(vp[b][s]); }
    SB();
    float cp[2], cn[2];
    row_extrema(pk[0], pk[1], cp, cn);
    const unsigned pt0 = 4 * h2 + q4;
    const unsigned pa0 = phiW + pt0 * 64 + 8 * (cq ^ ((pt0 >> 1) & 7));
    const unsigned pa1 = (pa0 ^ 32) + 8 * 64;
    f32x16 kvp = zero16, kvn = zero16;
#pragma unroll
    for (int b = 0; b < 2; ++b) {
        f32x2 ep[7], en[7];
        exp_pair(pk[b], cp[b], cn[b], ep, en);
        f32x2 sp = ep[0], sn = en[0];
#pragma unroll
        for (int i = 1; i < 7; ++i) { sp += ep[i]; sn += en[i]; }
        const float ip = __builtin_amdgcn_rcpf(xhalf_sum(sp[0] + sp[1])), in = __builtin_amdgcn_rcpf(xhalf_sum(sn[0] + sn[1]));
        const f32x2 ip2 = {ip, ip}, in2 = {in, in};
        const unsigned base = phiW + b * 4096 + r * 64;
#pragma unroll
        for (int g = 0; g < 4; ++g) {
            const unsigned a = base + 8 * ((2 * g + h2) ^ ((r >> 1) & 7));
            const u32x2 dp = {cvtpk2(ep[2 * g] * ip2), g < 3 ? cvtpk2(ep[(2 * g + 1) % 7] * ip2) : 0u};
            const u32x2 dn = {cvtpk2(en[2 * g] * in2), g < 3 ? cvtpk2(en[(2 * g + 1) % 7] * in2) : 0u};
            *(LAS u32x2*)lds_at(a) = dp;
            *(LAS u32x2*)lds_at(a + 2048) = dn;
        }
#pragma unroll
        for (int s = 0; s < 2; ++s) {
            const unsigned o = b * 4096 + s * 1024;
            kvp = MFMA(tr_frag(pa0 + o, pa1 + o), vp[b][s], kvp);
            kvn = MFMA(tr_frag(pa0 + o + 2048, pa1 + o + 2048), vp[b][s], kvn);
        }
    }
    asm volatile("s_waitcnt lgkmcnt(0)" ::: "memory");
#pragma unroll
    for (int f = 0; f < 4; ++f)
#pragma unroll
        for (int hh = 0; hh < 2; ++hh) {
            const f32x16& t = f < 2 ? kvp : kvn;
            const int o = 8 * (f & 1) + 4 * hh;
            *(LAS f32x4*)lds_at(phiW + f * 2048 + hh * 1024 + lane * 16) = (f32x4){t[o], t[o + 1], t[o + 2], t[o + 3]};
        }
    STAMP(4);
    LGKM_BAR();
    STAMP(5);
    {
        f32x4 t[4][2];
#pragma unroll
        for (int u = 0; u < 4; ++u) {
            t[u][0] = *(const LAS f32x4*)lds_at(L_PHI + u * 8192 + cw * 2048 + lane * 16);
            t[u][1] = *(const LAS f32x4*)lds_at(L_PHI + u * 8192 + cw * 2048 + 1024 + lane * 16);
        }
        SB();
        float a[8];
#pragma unroll
        for (int i = 0; i < 8; ++i) {
            float x01 = addf(t[0][i >> 2][i & 3], t[1][i >> 2][i & 3]), x23 = addf(t[2][i >> 2][i & 3], t[3][i >> 2][i & 3]);
            a[i] = addf(x01, x23);
        }
        u32x4 o = {cvtpk(a[0], a[1]), cvtpk(a[2], a[3]), cvtpk(a[4], a[5]), cvtpk(a[6], a[7])};
        STAMPX(1, o);
        __builtin_amdgcn_raw_buffer_store_b128(o, rws, lane * 16, WS_KVP + ((bh * 8 + tb) * 4 + cw) * 1024, 16);
    }
    {
        const rsrc_t roA = mk_rsrc(p.woA, D_ * XROW), roB = mk_rsrc(p.woB, D_ * XROW);
        const unsigned vwo = (lane >> 3) * XROW + 16 * ((lane & 7) ^ (4 * ((lane >> 4) & 1)));
        dma_rows<2>(roA, vwo, (256 + 16 * cw) * XROW + oc * 128, L_WO + (256 + 16 * cw) * 128);
        dma_rows<2>(roB, vwo, (576 - 448 + 16 * cw) * XROW + oc * 128, L_WO + (576 + 16 * cw) * 128);
#pragma unroll 1
        for (unsigned spins = 0; spins < SPIN_CAP; ++spins) { if (*(volatile LAS unsigned*)lds_at(L_XFREE + 4 * cw) != 0u) break; }
        dma_rows<6>(roA, vwo, 64 * cw * XROW + oc * 128, L_WO + 64 * cw * 128);
        WAIT_VM(10);
        if (lane == 0) __hip_atomic_store((GAS u64*)((char*)p.ws + WS_F1 + (bh * 4 + cw) * F1_GRP + tb * FS), tag, __ATOMIC_RELAXED, __HIP_MEMORY_SCOPE_AGENT);
        STAMP(9);
        dma_rows<2>(roA, vwo, (64 * cw + 48) * XROW + oc * 128, L_WO + (64 * cw + 48) * 128);
        const rsrc_t roC = cw < 2 ? roA : roB;
        const unsigned R = 320 + 64 * cw, Rg = cw < 2 ? R : R - 448;
        dma_rows<8>(roC, vwo, Rg * XROW + oc * 128, L_WO + R * 128);
    }
    bf16x8 sel;
    {
        const int jj = (r & 3) + 4 * (r >> 3);
        const bool on = r < 16 && ((r >> 2) & 1) == h2;
        const unsigned one = on ? (0x3F80u << (16 * (jj & 1))) : 0u;
        sel = __builtin_bit_cast(bf16x8, (u32x4){(jj >> 1) == 0 ? one : 0u, (jj >> 1) == 1 ? one : 0u, (jj >> 1) == 2 ? one : 0u, (jj >> 1) == 3 ? one : 0u});
        PINV(sel);
    }
    STAMP(6);
    wait_flags((const GAS u64*)((const char*)p.ws + WS_F1 + (bh * 4 + cw) * F1_GRP + (lane & 7) * FS), tag);
    STAMP(7);
    {
        const int hr = (r >> 2) & 1, reg = (r & 3) + 4 * (r >> 3);
        const int col = reg < 14 ? 14 * hr + reg : 28;
        const unsigned vo = (h2 * 32 + col) * 16;
        u32x4 part[8];
#pragma unroll
        for (int u = 0; u < 8; ++u) part[u] = __builtin_amdgcn_raw_buffer_load_b128(rws, vo, WS_KVP + ((bh * 8 + u) * 4 + cw) * 1024, 16);
        SB();
        f32x16 acc = zero16;
#pragma unroll
        for (int u = 0; u < 8; ++u) acc = MFMA(sel, __builtin_bit_cast(bf16x8, part[u]), acc);
        u32x4 o = __builtin_bit_cast(u32x4, pack8(acc, 0));
        if (reg == 15) o = (u32x4){ONE2, ONE2, ONE2, ONE2};
        *(LAS u32x4*)lds_at(L_KVA + cw * 1024 + lane * 16) = o;
    }
    LGKM_BAR();
    STAMP(8);
    if (!has_out) { STAMP_FLUSH(); return; }
    {
        const rsrc_t ro = mk_rsrc(p.woB, D_ * XROW);
        const unsigned vo = (lane >> 3) * XROW + 16 * ((lane & 7) ^ (4 * ((lane >> 4) & 1)));
        dma_rows<8>(ro, vo, (640 - 448 + 64 * cw) * XROW + oc * 128, L_WO + (640 + 64 * cw) * 128);
    }
    WAIT_VM(0);
    if (lane == 0) __hip_atomic_fetch_add((LAS unsigned*)lds_at(L_WOCNT), 1u, __ATOMIC_RELAXED, __HIP_MEMORY_SCOPE_WORKGROUP);
#pragma unroll 1
    for (unsigned spins = 0; spins < SPIN_CAP; ++spins) { if (*(volatile LAS unsigned*)lds_at(L_WOCNT) >= 4u) break; }
    asm volatile("" ::: "memory");
    STAMP(10);

    const unsigned tk = cw & 1, kh = cw >> 1;
    constexpr u64 HORD = 0xDB9642FECA875310ull;
    const GAS u64* f2 = (const GAS u64*)((const char*)p.ws + WS_F2 + (og * 2 + kh) * F2_GRP);
    f32x16 mine = zero16, other = zero16;
    {
        const unsigned colp = 32 * blk16 + 8 * p4;
        const unsigned r0 = kh * 448 + 14 * h2 + q4, r1 = kh * 448 + 14 * h2 + (q4 < 1 ? q4 : 1);
        unsigned a0 = L_WO + r0 * 128 + 64 * (kh ^ ((r0 >> 1) & 1)) + colp;
        unsigned a1 = L_WO + r1 * 128 + 64 * (kh ^ ((r1 >> 1) & 1)) + colp;
        unsigned a2 = a0 ^ 64, a3 = a1 ^ 64;
        asm volatile("" : "+v"(a0), "+v"(a1), "+v"(a2), "+v"(a3));
        u32x4 of[16][2];
        auto ld = [&](int i) {
            const unsigned hd = (unsigned)(HORD >> (4 * i)) & 15u;
#pragma unroll
            for (int s = 0; s < 2; ++s)
                of[i][s] = __builtin_amdgcn_raw_buffer_load_b128(rws, lane * 16, WS_O + (((og * 32 + kh * 16 + hd) * 2 + tk) * 2 + s) * 1024, 16);
        };
        bf16x8 A[2][4];
        auto rd = [&](bf16x8 (&f)[4], int i) {
            const unsigned o = ((unsigned)(HORD >> (4 * i)) & 15u) * 28 * 128;
            f[0] = tr_frag(a0 + o, a0 + o + 512); f[1] = tr_frag(a2 + o, a2 + o + 512);
            f[2] = tr_frag(a0 + o + 1024, a1 + o + 1536); f[3] = tr_frag(a2 + o + 1024, a3 + o + 1536);
        };
        auto step = [&](int i) {
            if (i + 1 < 16) rd(A[(i + 1) & 1], i + 1);
            SB();
            const bf16x8 b0 = __builtin_bit_cast(bf16x8, of[i][0]), b1 = __builtin_bit_cast(bf16x8, of[i][1]);
            mine = MFMA(A[i & 1][0], b0, mine); other = MFMA(A[i & 1][1], b0, other);
            mine = MFMA(A[i & 1][2], b1, mine); other = MFMA(A[i & 1][3], b1, other);
            SB();
        };
        wait_flags(f2 + (FS / 8) * ((HORD >> (4 * (lane % 10))) & 15), tag);
        STAMP(14);
#pragma unroll
        for (int i = 0; i < 10; ++i) ld(i);
        SB();
        rd(A[0], 0); SB();
#pragma unroll
        for (int i = 0; i < 10; ++i) step(i);
        STAMP(15);
        wait_flags(f2 + (FS / 8) * ((HORD >> (4 * (10 + lane % 6))) & 15), tag);
        STAMP(11);
        if (blockIdx.x == 0 && tid == 0) __hip_atomic_store((GAS u64*)((char*)p.ws + WS_EPOCH), tag, __ATOMIC_RELAXED, __HIP_MEMORY_SCOPE_AGENT);
#pragma unroll
        for (int i = 10; i < 16; ++i) ld(i);
        SB();
#pragma unroll
        for (int i = 10; i < 16; ++i) step(i);
    }
    STAMP(12);
#pragma unroll
    for (int g = 0; g < 4; ++g)
        *(LAS f32x4*)lds_at(L_PHI + cw * 4096 + g * 1024 + lane * 16) = (f32x4){other[4 * g], other[4 * g + 1], other[4 * g + 2], other[4 * g + 3]};
    LGKM_BAR();
    {
        unsigned d[8];
#pragma unroll
        for (int g = 0; g < 4; ++g) {
            const f32x4 t = *(const LAS f32x4*)lds_at(L_PHI + (cw ^ 2) * 4096 + g * 1024 + lane * 16);
            d[2 * g] = cvtpk(mine[4 * g] + t[0], mine[4 * g + 1] + t[1]);
            d[2 * g + 1] = cvtpk(mine[4 * g + 2] + t[2], mine[4 * g + 3] + t[3]);
        }
        const rsrc_t rout = mk_rsrc(p.out, S_ * XROW);
        const unsigned vo = r * XROW + h2 * 16, so = (og * 64 + tk * 32) * XROW + oc * 128 + kh * 64;
#pragma unroll
        for (int k = 0; k < 4; k += 2) {
            auto sx0 = __builtin_amdgcn_permlane32_swap(d[2 * k], d[2 * k + 2], false, false);
            auto sx1 = __builtin_amdgcn_permlane32_swap(d[2 * k + 1], d[2 * k + 3], false, false);
            const u32x4 o = {sx0[0], sx1[0], sx0[1], sx1[1]};
            __builtin_amdgcn_raw_buffer_store_b128(o, rout, vo, so + 16 * k, 0);
        }
    }
    STAMP(13);
    STAMP_FLUSH();
}

extern "C" void kernel_launch(void* const* d_in, const int* in_sizes, int n_in, void* d_out, int out_size, void* d_ws, size_t ws_size,
                              hipStream_t stream) {
    Params p;
    p.x = d_in[0]; p.wqkvA = d_in[1]; p.wphiA = d_in[2]; p.woA = d_in[3];
    p.wqkvB = d_in[5]; p.wphiB = d_in[6]; p.woB = d_in[7]; p.gate = d_in[8];
    p.out = d_out; p.ws = d_ws;
    (void)hipFuncSetAttribute((const void*)hedgehog_mix_kernel, hipFuncAttributeMaxDynamicSharedMemorySize, L_TOTAL);
    hedgehog_mix_kernel<<<256, 512, L_TOTAL, stream>>>(p);
}
```

```cpp
#define PREFETCH 0
#define PF_CODE 1
#define POLL_DEPTH 1
#define FS 8
#define ROT 1
#include <hip/hip_runtime.h>
#include <stdint.h>

typedef short bf16x8 __attribute__((ext_vector_type(8)));
typedef short s16x4 __attribute__((ext_vector_type(4)));
typedef short v4i16_t __attribute__((ext_vector_type(4)));
typedef __bf16 bf2_t __attribute__((ext_vector_type(2)));
typedef float f32x2 __attribute__((ext_vector_type(2)));
typedef float f32x4 __attribute__((ext_vector_type(4)));
typedef float f32x16 __attribute__((ext_vector_type(16)));
typedef unsigned u32x2 __attribute__((ext_vector_type(2)));
typedef unsigned u32x4 __attribute__((ext_vector_type(4)));
typedef unsigned long long u64;
#define LAS __attribute__((address_space(3)))
#define GAS __attribute__((address_space(1)))
#define DI __device__ __forceinline__
#define MFMA(a, b, c) __builtin_amdgcn_mfma_f32_32x32x16_bf16((a), (b), (c), 0, 0, 0)

constexpr int S_ = 2048, D_ = 448, DH = 28;
constexpr unsigned XROW = D_ * 2;
constexpr unsigned WROW = 3 * D_ * 2;
constexpr unsigned WS_EPOCH = 0;
constexpr unsigned F1_GRP = FS * 8 < 128 ? 128 : FS * 8, F2_GRP = FS * 16;
constexpr unsigned WS_F1 = 4096;
constexpr unsigned WS_F2 = WS_F1 + 32 * 4 * 1024;
constexpr unsigned WS_KVP = WS_F2 + 64 * 2048;
constexpr unsigned WS_O = WS_KVP + 32 * 8 * 4 * 1024;
constexpr unsigned WS_END = WS_O + 32 * 32 * 4 * 1024;
constexpr unsigned WS_STAMP = 8u << 20;
constexpr unsigned SLOT = 40960, SLOT_W = 32768;
constexpr unsigned L_B = 0, L_A = SLOT, L_C = 2 * SLOT;
constexpr unsigned L_WQ = 3 * SLOT;
constexpr unsigned L_GT = L_WQ + 448 * 64;
constexpr unsigned L_WPHI = L_GT + 4096;
constexpr unsigned L_SCR = L_WPHI + 2048;
constexpr unsigned L_XFREE = L_SCR + 512;
constexpr unsigned L_WOCNT = L_XFREE + 16;
constexpr unsigned L_PHI = L_B;
constexpr unsigned L_KVA = L_B + SLOT_W;
constexpr unsigned L_WO = L_A;
constexpr unsigned L_TOTAL = 163840;
static_assert(L_WPHI + 2048 <= L_TOTAL, "lds");
static_assert(L_WO + 896 * 128 == L_WPHI, "lds");
__host__ __device__ constexpr unsigned slot_of(int s) { return s % 3 == 0 ? L_A : (s % 3 == 1 ? L_B : L_C); }

struct Params {
    const void* x; const void* wqkvA; const void* wqkvB; const void* wphiA; const void* wphiB;
    const void* woA; const void* woB; const void* gate; void* out; void* ws;
};

typedef __amdgpu_buffer_rsrc_t rsrc_t;
DI rsrc_t mk_rsrc(const void* p, unsigned bytes) { return __builtin_amdgcn_make_buffer_rsrc((void*)p, (short)0, (int)bytes, 0x00020000); }
DI unsigned rfl(unsigned v) { return (unsigned)__builtin_amdgcn_readfirstlane((int)v); }
DI void dma16(rsrc_t r, unsigned voff, unsigned soff, unsigned lds) {
    asm volatile("s_mov_b32 m0, %0\n\ts_nop 0\n\tbuffer_load_dwordx4 %1, %2, %3 offen lds" ::"s"(lds), "v"(voff), "s"(r), "s"(soff) : "memory");
}
DI void dma_seg(rsrc_t rw, rsrc_t rx, unsigned vw, unsigned vx0, unsigned vx1, unsigned sw, unsigned sx, unsigned mw, unsigned mx) {
#define XP(v) "s_addk_i32 m0, 0x400\n\ts_addk_i32 %1, 0x1c00\n\tbuffer_load_dwordx4 " v ", %6, %1 offen lds\n\t"
    asm volatile("s_mov_b32 m0, %7\n\ts_nop 0\n\tbuffer_load_dwordx4 %2, %5, %0 offen lds\n\t"
                 "s_addk_i32 m0, 0x1000\n\ts_addk_i32 %0, 0x380\n\tbuffer_load_dwordx4 %2, %5, %0 offen lds\n\t"
                 "s_mov_b32 m0, %8\n\ts_nop 0\n\tbuffer_load_dwordx4 %3, %6, %1 offen lds\n\t"
                 XP("%4") XP("%3") XP("%4") XP("%3") XP("%4") XP("%3") XP("%4")
                 : "+s"(sw), "+s"(sx) : "v"(vw), "v"(vx0), "v"(vx1), "s"(rw), "s"(rx), "s"(mw), "s"(mx) : "scc", "memory");
#undef XP
}
template <int N> DI void dma_rows(rsrc_t r, unsigned v, unsigned so, unsigned m) {
    static_assert(N == 2 || N == 6 || N == 8, "");
#define RP "s_addk_i32 m0, 0x400\n\ts_addk_i32 %0, 0x1c00\n\tbuffer_load_dwordx4 %1, %2, %0 offen lds\n\t"
#define R0 "s_mov_b32 m0, %3\n\ts_nop 0\n\tbuffer_load_dwordx4 %1, %2, %0 offen lds\n\t"
    if (N == 2) asm volatile(R0 RP : "+s"(so) : "v"(v), "s"(r), "s"(m) : "scc", "memory");
    if (N == 6) asm volatile(R0 RP RP RP RP RP : "+s"(so) : "v"(v), "s"(r), "s"(m) : "scc", "memory");
    if (N == 8) asm volatile(R0 RP RP RP RP RP RP RP : "+s"(so) : "v"(v), "s"(r), "s"(m) : "scc", "memory");
#undef RP
#undef R0
}
DI void dma4(rsrc_t r, unsigned voff, unsigned soff, unsigned lds) {
    asm volatile("s_mov_b32 m0, %0\n\ts_nop 0\n\tbuffer_load_dword %1, %2, %3 offen lds" ::"s"(lds), "v"(voff), "s"(r), "s"(soff) : "memory");
}
#define PF_CODE_LINES 168
DI void gdma4(const void* a, unsigned lds) {
    asm volatile("s_mov_b32 m0, %0\n\ts_nop 0\n\tglobal_load_lds_dword %1, off" ::"s"(lds), "v"(a) : "memory");
}
#define PF_STAG 0
#define PF_WQ_SEG 1
#define PF_WO_SEG 2
#define WAIT_VM(n) asm volatile("s_waitcnt vmcnt(" #n ")" ::: "memory")
#define WAIT_VM_LGKM_BAR(n) asm volatile("s_waitcnt vmcnt(" #n ") lgkmcnt(0)\n\ts_barrier" ::: "memory")
#define LGKM_BAR() asm volatile("s_waitcnt lgkmcnt(0)\n\ts_barrier" ::: "memory")

extern __shared__ __attribute__((aligned(16))) char smem_dyn[];
DI LAS char* lds_at(unsigned off) { return (LAS char*)(uintptr_t)off; }
DI bf16x8 lds_b128(unsigned a) { return *(const LAS bf16x8*)lds_at(a); }
DI s16x4 lds_tr(unsigned a) { return __builtin_bit_cast(s16x4, __builtin_amdgcn_ds_read_tr16_b64_v4i16((LAS v4i16_t*)lds_at(a))); }
DI bf16x8 tr_frag(unsigned a0, unsigned a1) { const s16x4 lo = lds_tr(a0), hi = lds_tr(a1); return __builtin_shufflevector(lo, hi, 0, 1, 2, 3, 4, 5, 6, 7); }
DI unsigned cvtpk(float lo, float hi) { const f32x2 v = {lo, hi}; return __builtin_bit_cast(unsigned, __builtin_convertvector(v, bf2_t)); }
DI bf16x8 pack8(const f32x16& x, int s) {
    const u32x4 p = {cvtpk(x[8 * s], x[8 * s + 1]), cvtpk(x[8 * s + 2], x[8 * s + 3]), cvtpk(x[8 * s + 4], x[8 * s + 5]), cvtpk(x[8 * s + 6], x[8 * s + 7])};
    return __builtin_bit_cast(bf16x8, p);
}
DI float addf(float a, float b) { float r; asm("v_add_f32 %0, %1, %2" : "=v"(r) : "v"(a), "v"(b)); return r; }
DI float swap_lo(float v) { auto r = __builtin_amdgcn_permlane32_swap(__float_as_uint(v), __float_as_uint(v), false, false); return __uint_as_float(r[0]); }
DI float swap_hi(float v) { auto r = __builtin_amdgcn_permlane32_swap(__float_as_uint(v), __float_as_uint(v), false, false); return __uint_as_float(r[1]); }
DI float xhalf_max(float v) { auto r = __builtin_amdgcn_permlane32_swap(__float_as_uint(v), __float_as_uint(v), false, false); return __builtin_fmaxf(__uint_as_float(r[0]), __uint_as_float(r[1])); }
DI float xhalf_min(float v) { auto r = __builtin_amdgcn_permlane32_swap(__float_as_uint(v), __float_as_uint(v), false, false); return __builtin_fminf(__uint_as_float(r[0]), __uint_as_float(r[1])); }
DI float xhalf_sum(float v) { auto r = __builtin_amdgcn_permlane32_swap(__float_as_uint(v), __float_as_uint(v), false, false); return __uint_as_float(r[0]) + __uint_as_float(r[1]); }
constexpr float L2E = 1.4426950408889634f;
constexpr unsigned ONE2 = 0x3F803F80u;
constexpr unsigned SPIN_CAP = 1u << 16;

#define STAMP_DECL do { } while (0)
#define STAMP(i) do { } while (0)
#define STAMPX(i, x) do { } while (0)
#define STAMP_FLUSH() do { } while (0)

DI void row_extrema(const f32x16& a, const f32x16& b, float (&cp)[2], float (&cn)[2]) {
    float mxa = __builtin_fmaxf(a[0], a[1]), mna = __builtin_fminf(a[0], a[1]), mxb = __builtin_fmaxf(b[0], b[1]), mnb = __builtin_fminf(b[0], b[1]);
#pragma unroll
    for (int i = 2; i < 14; i += 2) {
        mxa = __builtin_fmaxf(__builtin_fmaxf(mxa, a[i]), a[i + 1]); mxb = __builtin_fmaxf(__builtin_fmaxf(mxb, b[i]), b[i + 1]);
        mna = __builtin_fminf(__builtin_fminf(mna, a[i]), a[i + 1]); mnb = __builtin_fminf(__builtin_fminf(mnb, b[i]), b[i + 1]);
    }
    cp[0] = -xhalf_max(mxa) * L2E; cp[1] = -xhalf_max(mxb) * L2E;
    cn[0] = xhalf_min(mna) * L2E; cn[1] = xhalf_min(mnb) * L2E;
}
DI void exp_pair(const f32x16& pp, float cp, float cn, f32x2 (&ep)[7], f32x2 (&en)[7]) {
    const f32x2 l2 = {L2E, L2E}, cp2 = {cp, cp}, cn2 = {cn, cn};
#pragma unroll
    for (int i = 0; i < 7; ++i) {
        const f32x2 v = {pp[2 * i], pp[2 * i + 1]};
        const f32x2 a = __builtin_elementwise_fma(v, l2, cp2), b = __builtin_elementwise_fma(v, -l2, cn2);
        ep[i] = (f32x2){__builtin_amdgcn_exp2f(a[0]), __builtin_amdgcn_exp2f(a[1])};
        en[i] = (f32x2){__builtin_amdgcn_exp2f(b[0]), __builtin_amdgcn_exp2f(b[1])};
    }
}
DI unsigned cvtpk2(f32x2 v) { return __builtin_bit_cast(unsigned, __builtin_convertvector(v, bf2_t)); }

DI void wait_flags(const GAS u64* f, u64 tag) {
#pragma unroll 1
    for (unsigned spins = 0; spins < SPIN_CAP; ++spins) {
        const u64 v = __hip_atomic_load(f, __ATOMIC_RELAXED, __HIP_MEMORY_SCOPE_AGENT);
        if (__builtin_amdgcn_ballot_w64(v != tag) == 0) break;
        __builtin_amdgcn_s_sleep(1);
    }
    asm volatile("" ::: "memory");
}

#define SB() __builtin_amdgcn_sched_barrier(0)
#define PINV(x) asm volatile("" : "+v"(x))
struct Frag { bf16x8 x0, x1, fk, fv; };

DI void build_aphi(bf16x8 (&aphi)[2], int r, int h2) {
    const int hr = (r >> 2) & 1, reg = (r & 3) + 4 * (r >> 3), f = 14 * hr + (reg < 14 ? reg : 0);
    const unsigned a = L_WPHI + 2 * (4 * h2 * DH + f);
#pragma unroll
    for (int s = 0; s < 2; ++s)
#pragma unroll
        for (int jj = 0; jj < 8; ++jj) {
            const int d0 = 16 * s + 8 * (jj >> 2) + (jj & 3);
            const bool ok = reg < 14 && (d0 + 4 < DH || h2 == 0);
            const unsigned short v = *(const LAS unsigned short*)lds_at(a + 2 * d0 * DH);
            aphi[s][jj] = ok ? (short)v : (short)0;
        }
}

__global__ void __launch_bounds__(512, 2) hedgehog_mix_kernel(Params p) {
    const int tid = threadIdx.x, lane = tid & 63;
    const unsigned w = rfl(tid >> 6), cw = w & 3;
    if (w == 4) {
        u64 pc; asm volatile("s_getpc_b64 %0" : "=s"(pc));
        const unsigned l0 = (blockIdx.x >> 3) * 6 + (lane < 6 ? lane : 5), l = l0 < PF_CODE_LINES ? l0 : 0;
        gdma4((const char*)(pc & ~127ull) + l * 128, L_SCR + 256);
    }
    const int r = lane & 31, h2 = lane >> 5, i16 = lane & 15, q4 = i16 >> 2, p4 = i16 & 3, blk16 = (lane >> 4) & 1;
    const unsigned xcd = blockIdx.x & 7, j = blockIdx.x >> 3;
    const unsigned tb = (xcd & 1) * 4 + (j & 3), bh = (xcd >> 1) * 8 + (j >> 2), br = bh >> 4, h = bh & 15;
    const unsigned og = xcd * 4 + j / 7, oc = j % 7;
    const bool has_out = j < 28;
    const unsigned rot = ROT == 1 ? ((j >> 2) + 2 * (j & 3)) % 7 : (ROT == 2 ? (2 * (j & 3)) % 7 : 0);
    auto seg = [&](int n) { const unsigned t = rot + n; return t >= 7 ? t - 7 : t; };
#define PIN_ARGS() asm volatile("" : "+s"(p.x), "+s"(p.wqkvA), "+s"(p.wqkvB), "+s"(p.wphiA), "+s"(p.wphiB), "+s"(p.woA), "+s"(p.woB), "+s"(p.gate), "+s"(p.out), "+s"(p.ws))
    PIN_ARGS();
    STAMP_DECL;
    STAMP(0);
    const rsrc_t rws = mk_rsrc(p.ws, WS_END);
    const f32x16 zero16 = {};
    const int cq = 4 * blk16 + p4;
    const unsigned rowpart = (8 * h2 + q4) * 64;
    const unsigned coloff = (8 * (h & 1) + 8 * cq) & 63;
    const unsigned laneX = cw * 8192 + r * 128 + 16 * (h2 ^ ((r >> 1) & 7));

    if (__builtin_expect(w >= 4, 1)) {
        const rsrc_t rx = mk_rsrc(p.x, S_ * XROW);
        const rsrc_t rw = mk_rsrc(br ? p.wqkvB : p.wqkvA, D_ * WROW);
        const unsigned vx0 = (lane >> 3) * XROW + 16 * ((lane & 7) ^ (lane >> 4));
        const unsigned vx1 = (lane >> 3) * XROW + 16 * ((lane & 7) ^ (4 + (lane >> 4)));
        const unsigned vw = (lane >> 2) * WROW + 16 * (lane & 3);
        const unsigned sx = (tb * 256 + cw * 64) * XROW;
        const unsigned sw = cw * 16 * WROW + h * 56 - 8 * (h & 1);
        auto issue = [&](int s) {
            dma_seg(rw, rx, vw, vx0, vx1, sw + seg(s) * (64 * WROW) + 896, sx + seg(s) * 128, slot_of(s) + SLOT_W + cw * 1024, slot_of(s) + cw * 8192);
        };
        bf16x8 xs[5][4][2];
        auto grab = [&](int s) {
#pragma unroll
            for (int ks = 0; ks < 4; ++ks) {
                const unsigned a = (laneX ^ (32 * ks)) + slot_of(s);
                xs[s][ks][0] = lds_b128(a); xs[s][ks][1] = lds_b128(a + 4096);
            }
        };
        STAMP(11);
        issue(0); issue(1);
        STAMP(1);
        WAIT_VM_LGKM_BAR(10);
        STAMP(2);
        grab(0); issue(2); WAIT_VM_LGKM_BAR(10);
        grab(1); issue(3); WAIT_VM_LGKM_BAR(10);
        grab(2); issue(4); WAIT_VM_LGKM_BAR(10);
        grab(3); issue(5); WAIT_VM_LGKM_BAR(10);
        grab(4); issue(6); WAIT_VM_LGKM_BAR(10);
        WAIT_VM_LGKM_BAR(0);
        STAMP(3);
#pragma unroll
        for (int s = 0; s < 7; ++s) { const unsigned sg = seg((s + 5) % 7); dma16(rw, vw, sw + sg * (64 * WROW), L_WQ + sg * 4096 + cw * 1024); }
        bf16x8 aphi[2];
        build_aphi(aphi, r, h2);
        PINV(aphi[0]); PINV(aphi[1]);
        u64 tag;
        asm volatile("s_waitcnt lgkmcnt(0)\n\ts_load_dwordx2 %0, %1, 0x0\n\ts_waitcnt lgkmcnt(0)" : "=&s"(tag) : "s"(p.ws) : "memory");
        tag += 1;
        STAMP(4);
        WAIT_VM_LGKM_BAR(0);
        STAMP(5);
        f32x16 qT[2] = {zero16, zero16};
        {
            const unsigned aq_real = L_WQ + rowpart + coloff, aq_pad = L_GT + rowpart;
            bf16x8 fq[3], xl[2][2];
            auto seg_of = [](int n) { return n < 8 ? 5 + (n >> 2) : (n - 8) >> 2; };
            unsigned aQ = 0;
            auto rdq = [&](int n) {
                if ((n & 3) == 0) { const unsigned sg = seg(seg_of(n)); aQ = cq == 7 ? aq_pad + 8 * sg : aq_real + 4096 * sg; PINV(aQ); }
                fq[n % 3] = tr_frag(aQ + (n & 3) * 1024, aQ + (n & 3) * 1024 + 256);
            };
            auto rdx = [&](int n) { const unsigned a = (laneX ^ (32 * (n & 3))) + slot_of(seg_of(n)); xl[n & 1][0] = lds_b128(a); xl[n & 1][1] = lds_b128(a + 4096); };
            rdq(0); rdx(0); rdq(1);
            SB();
#pragma unroll
            for (int n = 0; n < 28; ++n) {
                const int s = seg_of(n), ks = n & 3;
                if (n + 1 < 8) rdx(n + 1);
                if (n + 2 < 28) rdq(n + 2);
                SB();
                qT[0] = MFMA(fq[n % 3], n >= 8 ? xs[n >= 8 ? s : 0][ks][0] : xl[n & 1][0], qT[0]);
                qT[1] = MFMA(fq[n % 3], n >= 8 ? xs[n >= 8 ? s : 0][ks][1] : xl[n & 1][1], qT[1]);
                SB();
                if (n == 7) {
                    asm volatile("s_waitcnt lgkmcnt(0)" ::: "memory");
                    *(volatile LAS unsigned*)lds_at(L_XFREE + 4 * cw) = 1u;
                }
            }
        }
        STAMP(6);
        bf16x8 eq[2][2][2];
        float gs[2];
        {
            f32x16 pq[2];
            const bf16x8 q00 = pack8(qT[0], 0), q10 = pack8(qT[1], 0);
            pq[0] = MFMA(aphi[0], q00, zero16); pq[1] = MFMA(aphi[0], q10, zero16);
            const bf16x8 q01 = pack8(qT[0], 1), q11 = pack8(qT[1], 1);
            pq[0] = MFMA(aphi[1], q01, pq[0]); pq[1] = MFMA(aphi[1], q11, pq[1]);
#pragma unroll
            for (int b = 0; b < 2; ++b) {
                const float lg = swap_hi(qT[b][12]);
                gs[b] = __builtin_amdgcn_rcpf(1.f + __builtin_amdgcn_exp2f(br ? lg * L2E : -lg * L2E));
                PINV(gs[b]);
            }
            float cp[2], cn[2];
            row_extrema(pq[0], pq[1], cp, cn);
            PINV(cp[0]); PINV(cp[1]); PINV(cn[0]); PINV(cn[1]);
#pragma unroll
            for (int b = 0; b < 2; ++b) {
                const f32x2 l2 = {L2E, L2E}, cp2 = {cp[b], cp[b]}, cn2 = {cn[b], cn[b]};
                unsigned pe[8], ne[8];
#pragma unroll
                for (int i = 0; i < 7; ++i) {
                    const f32x2 v = {pq[b][2 * i], pq[b][2 * i + 1]};
                    const f32x2 a = __builtin_elementwise_fma(v, l2, cp2), c = __builtin_elementwise_fma(v, -l2, cn2);
                    pe[i] = cvtpk2((f32x2){__builtin_amdgcn_exp2f(a[0]), __builtin_amdgcn_exp2f(a[1])});
                    ne[i] = cvtpk2((f32x2){__builtin_amdgcn_exp2f(c[0]), __builtin_amdgcn_exp2f(c[1])});
                }
                pe[7] = 0; ne[7] = 0;
                eq[b][0][0] = __builtin_bit_cast(bf16x8, (u32x4){pe[0], pe[1], pe[2], pe[3]}); eq[b][0][1] = __builtin_bit_cast(bf16x8, (u32x4){pe[4], pe[5], pe[6], pe[7]});
                eq[b][1][0] = __builtin_bit_cast(bf16x8, (u32x4){ne[0], ne[1], ne[2], ne[3]}); eq[b][1][1] = __builtin_bit_cast(bf16x8, (u32x4){ne[4], ne[5], ne[6], ne[7]});
            }
        }
#pragma unroll
        for (int b = 0; b < 2; ++b) asm volatile("" : "+v"(eq[b][0][0]), "+v"(eq[b][0][1]), "+v"(eq[b][1][0]), "+v"(eq[b][1][1]), "+v"(gs[b]));
        STAMP(7);
        LGKM_BAR();
        __builtin_amdgcn_s_setprio(3);
        STAMP(8);
        {
            bf16x8 akv[4];
#pragma unroll
            for (int f = 0; f < 4; ++f) akv[f] = lds_b128(L_KVA + f * 1024 + lane * 16);
#pragma unroll
            for (int b = 0; b < 2; ++b) {
                f32x16 np = MFMA(akv[0], eq[b][0][0], zero16); np = MFMA(akv[1], eq[b][0][1], np);
                f32x16 nn = MFMA(akv[2], eq[b][1][0], zero16); nn = MFMA(akv[3], eq[b][1][1], nn);
                const float ia = __builtin_amdgcn_rcpf(np[15]), ib = __builtin_amdgcn_rcpf(nn[15]);
                const float den = np[14] * ia + nn[14] * ib + 1e-6f;
                const float sc = gs[b] * __builtin_amdgcn_rcpf(den);
                const float ca = ia * sc, cb = ib * sc;
                float o[14];
#pragma unroll
                for (int i = 0; i < 14; ++i) o[i] = np[i] * ca + nn[i] * cb;
                const u32x4 o0 = {cvtpk(o[0], o[1]), cvtpk(o[2], o[3]), cvtpk(o[4], o[5]), cvtpk(o[6], o[7])};
                const u32x4 o1 = {cvtpk(o[8], o[9]), cvtpk(o[10], o[11]), cvtpk(o[12], o[13]), 0u};
                const unsigned so = WS_O + (((tb * 4 + cw) * 32 + bh) * 4 + b * 2) * 1024;
                __builtin_amdgcn_raw_buffer_store_b128(o0, rws, lane * 16, so, 16);
                __builtin_amdgcn_raw_buffer_store_b128(o1, rws, lane * 16, so + 1024, 16);
            }
        }
        STAMP(9);
        WAIT_VM(0);
        if (lane == 0) __hip_atomic_store((GAS u64*)((char*)p.ws + WS_F2 + ((tb * 4 + cw) * 2 + br) * F2_GRP + h * FS), tag, __ATOMIC_RELAXED, __HIP_MEMORY_SCOPE_AGENT);
        STAMP(10);
        STAMP_FLUSH();
        return;
    }

    u64 tag;
    {
        const rsrc_t rg = mk_rsrc(p.gate, XROW);
        const rsrc_t rp = mk_rsrc(br ? p.wphiB : p.wphiA, 16 * DH * DH * 2);
        unsigned g0, g1; u32x2 wp;
        asm volatile("s_load_dwordx2 %0, %1, 0x0" : "=&s"(tag) : "s"(p.ws) : "memory");
        asm volatile("buffer_load_ushort %0, %3, %4, 0 offen\n\tbuffer_load_ushort %1, %3, %4, 0 offen offset:512\n\tbuffer_load_dwordx2 %2, %5, %6, %7 offen"
                     : "=&v"(g0), "=&v"(g1), "=&v"(wp) : "v"(tid * 2), "s"(rg), "v"(tid * 8), "s"(rp), "s"(h * (DH * DH * 2)) : "memory");
        asm volatile("s_waitcnt vmcnt(0) lgkmcnt(0)" : "+v"(g0), "+v"(g1), "+v"(wp), "+s"(tag) :: "memory");
        *(LAS u32x2*)lds_at(L_GT + (tid & 63) * 64 + (tid >> 6) * 8) = (u32x2){g0 & 0xffffu, 0u};
        *(LAS u32x2*)lds_at(L_GT + (tid & 63) * 64 + (4 + (tid >> 6)) * 8) = (u32x2){g1 & 0xffffu, 0u};
        *(LAS u32x2*)lds_at(L_WPHI + tid * 8) = wp;
        if (tid < 5) *(volatile LAS unsigned*)lds_at(L_XFREE + 4 * tid) = 0u;
    }
    tag += 1;
    __builtin_amdgcn_s_setprio(2);
    LGKM_BAR();
    STAMP(2);
    bf16x8 aphi[2];
    build_aphi(aphi, r, h2);
    PINV(aphi[0]); PINV(aphi[1]);
    const unsigned laneW = SLOT_W + rowpart + coloff;
    f32x16 vinit;
#pragma unroll
    for (int i = 0; i < 16; ++i) vinit[i] = r == 28 ? 1.f : 0.f;
    PINV(vinit);
    f32x16 kT[2] = {zero16, zero16}, vv[2] = {vinit, vinit};
    {
        unsigned aX[4], aW, aV;
        auto addr = [&](int s) {
#pragma unroll
            for (int ks = 0; ks < 4; ++ks) { aX[ks] = (laneX ^ (32 * ks)) + slot_of(s); PINV(aX[ks]); }
            aW = laneW + slot_of(s); PINV(aW);
            aV = cq == 7 ? L_GT + rowpart + 56 : aW + 4096; PINV(aV);
        };
        Frag F[2];
#define RD_X(f, ks) do { f.x0 = lds_b128(aX[ks]); } while (0)
#define RD_X1(f, ks) do { f.x1 = lds_b128(aX[ks] + 4096); } while (0)
#define RD_K(f, ks) do { f.fk = tr_frag(aW + ks * 1024, aW + ks * 1024 + 256); } while (0)
#define RD_V(f, ks) do { f.fv = tr_frag(aV + ks * 1024, aV + ks * 1024 + 256); } while (0)
        addr(0);
        RD_X(F[0], 0); RD_K(F[0], 0); RD_X1(F[0], 0); RD_V(F[0], 0); SB();
#pragma unroll
        for (int n = 0; n < 28; ++n) {
            Frag& c = F[n & 1];
            Frag& x = F[(n + 1) & 1];
            const int nks = (n + 1) & 3;
            const bool more = n + 1 < 28;
            kT[0] = MFMA(c.fk, c.x0, kT[0]); SB();
            if (more && nks == 0) { LGKM_BAR(); addr((n + 1) >> 2); SB(); }
            if (more) { RD_X(x, nks); RD_K(x, nks); } SB();
            kT[1] = MFMA(c.fk, c.x1, kT[1]); SB();
            if (more) { RD_X1(x, nks); } SB();
            vv[0] = MFMA(c.x0, c.fv, vv[0]); SB();
            if (more) { RD_V(x, nks); } SB();
            vv[1] = MFMA(c.x1, c.fv, vv[1]); SB();
        }
    }
    STAMP(3);

    const unsigned phiW = L_PHI + cw * 8192;
    f32x16 pk[2];
    {
        const bf16x8 k00 = pack8(kT[0], 0), k10 = pack8(kT[1], 0);
        pk[0] = MFMA(aphi[0], k00, zero16); pk[1] = MFMA(aphi[0], k10, zero16);
        const bf16x8 k01 = pack8(kT[0], 1), k11 = pack8(kT[1], 1);
        pk[0] = MFMA(aphi[1], k01, pk[0]); pk[1] = MFMA(aphi[1], k11, pk[1]);
    }
    bf16x8 vp[2][2];
#pragma unroll
    for (int b = 0; b < 2; ++b)
#pragma unroll
        for (int s = 0; s < 2; ++s) { vp[b][s] = pack8(vv[b], s); PINV(vp[b][s]); }
    SB();
    float cp[2], cn[2];
    row_extrema(pk[0], pk[1], cp, cn);
    const unsigned pt0 = 4 * h2 + q4;
    const unsigned pa0 = phiW + pt0 * 64 + 8 * (cq ^ ((pt0 >> 1) & 7));
    const unsigned pa1 = (pa0 ^ 32) + 8 * 64;
    f32x16 kvp = zero16, kvn = zero16;
#pragma unroll
    for (int b = 0; b < 2; ++b) {
        f32x2 ep[7], en[7];
        exp_pair(pk[b], cp[b], cn[b], ep, en);
        f32x2 sp = ep[0], sn = en[0];
#pragma unroll
        for (int i = 1; i < 7; ++i) { sp += ep[i]; sn += en[i]; }
        const float ip = __builtin_amdgcn_rcpf(xhalf_sum(sp[0] + sp[1])), in = __builtin_amdgcn_rcpf(xhalf_sum(sn[0] + sn[1]));
        const f32x2 ip2 = {ip, ip}, in2 = {in, in};
        const unsigned base = phiW + b * 4096 + r * 64;
#pragma unroll
        for (int g = 0; g < 4; ++g) {
            const unsigned a = base + 8 * ((2 * g + h2) ^ ((r >> 1) & 7));
            const u32x2 dp = {cvtpk2(ep[2 * g] * ip2), g < 3 ? cvtpk2(ep[(2 * g + 1) % 7] * ip2) : 0u};
            const u32x2 dn = {cvtpk2(en[2 * g] * in2), g < 3 ? cvtpk2(en[(2 * g + 1) % 7] * in2) : 0u};
            *(LAS u32x2*)lds_at(a) = dp;
            *(LAS u32x2*)lds_at(a + 2048) = dn;
        }
#pragma unroll
        for (int s = 0; s < 2; ++s) {
            const unsigned o = b * 4096 + s * 1024;
            kvp = MFMA(tr_frag(pa0 + o, pa1 + o), vp[b][s], kvp);
            kvn = MFMA(tr_frag(pa0 + o + 2048, pa1 + o + 2048), vp[b][s], kvn);
        }
    }
    asm volatile("s_waitcnt lgkmcnt(0)" ::: "memory");
#pragma unroll
    for (int f = 0; f < 4; ++f)
#pragma unroll
        for (int hh = 0; hh < 2; ++hh) {
            const f32x16& t = f < 2 ? kvp : kvn;
            const int o = 8 * (f & 1) + 4 * hh;
            *(LAS f32x4*)lds_at(phiW + f * 2048 + hh * 1024 + lane * 16) = (f32x4){t[o], t[o + 1], t[o + 2], t[o + 3]};
        }
    STAMP(4);
    LGKM_BAR();
    STAMP(5);
    {
        f32x4 t[4][2];
#pragma unroll
        for (int u = 0; u < 4; ++u) {
            t[u][0] = *(const LAS f32x4*)lds_at(L_PHI + u * 8192 + cw * 2048 + lane * 16);
            t[u][1] = *(const LAS f32x4*)lds_at(L_PHI + u * 8192 + cw * 2048 + 1024 + lane * 16);
        }
        SB();
        float a[8];
#pragma unroll
        for (int i = 0; i < 8; ++i) {
            float x01 = addf(t[0][i >> 2][i & 3], t[1][i >> 2][i & 3]), x23 = addf(t[2][i >> 2][i & 3], t[3][i >> 2][i & 3]);
            a[i] = addf(x01, x23);
        }
        u32x4 o = {cvtpk(a[0], a[1]), cvtpk(a[2], a[3]), cvtpk(a[4], a[5]), cvtpk(a[6], a[7])};
        STAMPX(1, o);
        __builtin_amdgcn_raw_buffer_store_b128(o, rws, lane * 16, WS_KVP + ((bh * 8 + tb) * 4 + cw) * 1024, 16);
    }
    {
        const rsrc_t roA = mk_rsrc(p.woA, D_ * XROW), roB = mk_rsrc(p.woB, D_ * XROW);
        const unsigned vwo = (lane >> 3) * XROW + 16 * ((lane & 7) ^ (4 * ((lane >> 4) & 1)));
        dma_rows<2>(roA, vwo, (256 + 16 * cw) * XROW + oc * 128, L_WO + (256 + 16 * cw) * 128);
        dma_rows<2>(roB, vwo, (576 - 448 + 16 * cw) * XROW + oc * 128, L_WO + (576 + 16 * cw) * 128);
#pragma unroll 1
        for (unsigned spins = 0; spins < SPIN_CAP; ++spins) { if (*(volatile LAS unsigned*)lds_at(L_XFREE + 4 * cw) != 0u) break; }
        dma_rows<6>(roA, vwo, 64 * cw * XROW + oc * 128, L_WO + 64 * cw * 128);
        WAIT_VM(10);
        if (lane == 0) __hip_atomic_store((GAS u64*)((char*)p.ws + WS_F1 + (bh * 4 + cw) * F1_GRP + tb * FS), tag, __ATOMIC_RELAXED, __HIP_MEMORY_SCOPE_AGENT);
        STAMP(9);
        dma_rows<2>(roA, vwo, (64 * cw + 48) * XROW + oc * 128, L_WO + (64 * cw + 48) * 128);
        const rsrc_t roC = cw < 2 ? roA : roB;
        const unsigned R = 320 + 64 * cw, Rg = cw < 2 ? R : R - 448;
        dma_rows<8>(roC, vwo, Rg * XROW + oc * 128, L_WO + R * 128);
    }
    bf16x8 sel;
    {
        const int jj = (r & 3) + 4 * (r >> 3);
        const bool on = r < 16 && ((r >> 2) & 1) == h2;
        const unsigned one = on ? (0x3F80u << (16 * (jj & 1))) : 0u;
        sel = __builtin_bit_cast(bf16x8, (u32x4){(jj >> 1) == 0 ? one : 0u, (jj >> 1) == 1 ? one : 0u, (jj >> 1) == 2 ? one : 0u, (jj >> 1) == 3 ? one : 0u});
        PINV(sel);
    }
    STAMP(6);
    wait_flags((const GAS u64*)((const char*)p.ws + WS_F1 + (bh * 4 + cw) * F1_GRP + (lane & 7) * FS), tag);
    STAMP(7);
    {
        const int hr = (r >> 2) & 1, reg = (r & 3) + 4 * (r >> 3);
        const int col = reg < 14 ? 14 * hr + reg : 28;
        const unsigned vo = (h2 * 32 + col) * 16;
        u32x4 part[8];
#pragma unroll
        for (int u = 0; u < 8; ++u) part[u] = __builtin_amdgcn_raw_buffer_load_b128(rws, vo, WS_KVP + ((bh * 8 + u) * 4 + cw) * 1024, 16);
        SB();
        f32x16 acc = zero16;
#pragma unroll
        for (int u = 0; u < 8; ++u) acc = MFMA(sel, __builtin_bit_cast(bf16x8, part[u]), acc);
        u32x4 o = __builtin_bit_cast(u32x4, pack8(acc, 0));
        if (reg == 15) o = (u32x4){ONE2, ONE2, ONE2, ONE2};
        *(LAS u32x4*)lds_at(L_KVA + cw * 1024 + lane * 16) = o;
    }
    LGKM_BAR();
    STAMP(8);
    if (!has_out) { STAMP_FLUSH(); return; }
    {
        const rsrc_t ro = mk_rsrc(p.woB, D_ * XROW);
        const unsigned vo = (lane >> 3) * XROW + 16 * ((lane & 7) ^ (4 * ((lane >> 4) & 1)));
        dma_rows<8>(ro, vo, (640 - 448 + 64 * cw) * XROW + oc * 128, L_WO + (640 + 64 * cw) * 128);
    }
    WAIT_VM(0);
    if (lane == 0) __hip_atomic_fetch_add((LAS unsigned*)lds_at(L_WOCNT), 1u, __ATOMIC_RELAXED, __HIP_MEMORY_SCOPE_WORKGROUP);
#pragma unroll 1
    for (unsigned spins = 0; spins < SPIN_CAP; ++spins) { if (*(volatile LAS unsigned*)lds_at(L_WOCNT) >= 4u) break; }
    asm volatile("" ::: "memory");
    STAMP(10);

    const unsigned tk = cw & 1, kh = cw >> 1;
    constexpr u64 HORD = 0xDB9642FECA875310ull;
    const GAS u64* f2 = (const GAS u64*)((const char*)p.ws + WS_F2 + (og * 2 + kh) * F2_GRP);
    f32x16 mine = zero16, other = zero16;
    {
        const unsigned colp = 32 * blk16 + 8 * p4;
        const unsigned r0 = kh * 448 + 14 * h2 + q4, r1 = kh * 448 + 14 * h2 + (q4 < 1 ? q4 : 1);
        unsigned a0 = L_WO + r0 * 128 + 64 * (kh ^ ((r0 >> 1) & 1)) + colp;
        unsigned a1 = L_WO + r1 * 128 + 64 * (kh ^ ((r1 >> 1) & 1)) + colp;
        unsigned a2 = a0 ^ 64, a3 = a1 ^ 64;
        asm volatile("" : "+v"(a0), "+v"(a1), "+v"(a2), "+v"(a3));
        u32x4 of[16][2];
        auto ld = [&](int i) {
            const unsigned hd = (unsigned)(HORD >> (4 * i)) & 15u;
#pragma unroll
            for (int s = 0; s < 2; ++s)
                of[i][s] = __builtin_amdgcn_raw_buffer_load_b128(rws, lane * 16, WS_O + (((og * 32 + kh * 16 + hd) * 2 + tk) * 2 + s) * 1024, 16);
        };
        bf16x8 A[2][4];
        auto rd = [&](bf16x8 (&f)[4], int i) {
            const unsigned o = ((unsigned)(HORD >> (4 * i)) & 15u) * 28 * 128;
            f[0] = tr_frag(a0 + o, a0 + o + 512); f[1] = tr_frag(a2 + o, a2 + o + 512);
            f[2] = tr_frag(a0 + o + 1024, a1 + o + 1536); f[3] = tr_frag(a2 + o + 1024, a3 + o + 1536);
        };
        auto step = [&](int i) {
            if (i + 1 < 16) rd(A[(i + 1) & 1], i + 1);
            SB();
            const bf16x8 b0 = __builtin_bit_cast(bf16x8, of[i][0]), b1 = __builtin_bit_cast(bf16x8, of[i][1]);
            mine = MFMA(A[i & 1][0], b0, mine); other = MFMA(A[i & 1][1], b0, other);
            mine = MFMA(A[i & 1][2], b1, mine); other = MFMA(A[i & 1][3], b1, other);
            SB();
        };
        wait_flags(f2 + (FS / 8) * ((HORD >> (4 * (lane % 10))) & 15), tag);
        STAMP(14);
#pragma unroll
        for (int i = 0; i < 10; ++i) ld(i);
        SB();
        rd(A[0], 0); SB();
#pragma unroll
        for (int i = 0; i < 10; ++i) step(i);
        STAMP(15);
        wait_flags(f2 + (FS / 8) * ((HORD >> (4 * (10 + lane % 6))) & 15), tag);
        STAMP(11);
        if (blockIdx.x == 0 && tid == 0) __hip_atomic_store((GAS u64*)((char*)p.ws + WS_EPOCH), tag, __ATOMIC_RELAXED, __HIP_MEMORY_SCOPE_AGENT);
#pragma unroll
        for (int i = 10; i < 16; ++i) ld(i);
        SB();
#pragma unroll
        for (int i = 10; i < 16; ++i) step(i);
    }
    STAMP(12);
#pragma unroll
    for (int g = 0; g < 4; ++g)
        *(LAS f32x4*)lds_at(L_PHI + cw * 4096 + g * 1024 + lane * 16) = (f32x4){other[4 * g], other[4 * g + 1], other[4 * g + 2], other[4 * g + 3]};
    LGKM_BAR();
    {
        unsigned d[8];
#pragma unroll
        for (int g = 0; g < 4; ++g) {
            const f32x4 t = *(const LAS f32x4*)lds_at(L_PHI + (cw ^ 2) * 4096 + g * 1024 + lane * 16);
            d[2 * g] = cvtpk(mine[4 * g] + t[0], mine[4 * g + 1] + t[1]);
            d[2 * g + 1] = cvtpk(mine[4 * g + 2] + t[2], mine[4 * g + 3] + t[3]);
        }
        const rsrc_t rout = mk_rsrc(p.out, S_ * XROW);
        const unsigned vo = r * XROW + h2 * 16, so = (og * 64 + tk * 32) * XROW + oc * 128 + kh * 64;
#pragma unroll
        for (int k = 0; k < 4; k += 2) {
            auto sx0 = __builtin_amdgcn_permlane32_swap(d[2 * k], d[2 * k + 2], false, false);
            auto sx1 = __builtin_amdgcn_permlane32_swap(d[2 * k + 1], d[2 * k + 3], false, false);
            const u32x4 o = {sx0[0], sx1[0], sx0[1], sx1[1]};
            __builtin_amdgcn_raw_buffer_store_b128(o, rout, vo, so + 16 * k, 0);
        }
    }
    STAMP(13);
    STAMP_FLUSH();
}

extern "C" void kernel_launch(void* const* d_in, const int* in_sizes, int n_in, void* d_out, int out_size, void* d_ws, size_t ws_size,
                              hipStream_t stream) {
    Params p;
    p.x = d_in[0]; p.wqkvA = d_in[1]; p.wphiA = d_in[2]; p.woA = d_in[3];
    p.wqkvB = d_in[5]; p.wphiB = d_in[6]; p.woB = d_in[7]; p.gate = d_in[8];
    p.out = d_out; p.ws = d_ws;
    (void)hipFuncSetAttribute((const void*)hedgehog_mix_kernel, hipFuncAttributeMaxDynamicSharedMemorySize, L_TOTAL);
    hedgehog_mix_kernel<<<256, 512, L_TOTAL, stream>>>(p);
}
```

```cpp
#define PREFETCH 0
#define PF_CODE 1
#define POLL_DEPTH 1
#define FS 8
#define ROT 1
#include <hip/hip_runtime.h>
#include <stdint.h>

typedef short bf16x8 __attribute__((ext_vector_type(8)));
typedef short s16x4 __attribute__((ext_vector_type(4)));
typedef short v4i16_t __attribute__((ext_vector_type(4)));
typedef __bf16 bf2_t __attribute__((ext_vector_type(2)));
typedef float f32x2 __attribute__((ext_vector_type(2)));
typedef float f32x4 __attribute__((ext_vector_type(4)));
typedef float f32x16 __attribute__((ext_vector_type(16)));
typedef unsigned u32x2 __attribute__((ext_vector_type(2)));
typedef unsigned u32x4 __attribute__((ext_vector_type(4)));
typedef unsigned long long u64;
#define LAS __attribute__((address_space(3)))
#define GAS __attribute__((address_space(1)))
#define DI __device__ __forceinline__
#define MFMA(a, b, c) __builtin_amdgcn_mfma_f32_32x32x16_bf16((a), (b), (c), 0, 0, 0)

constexpr int S_ = 2048, D_ = 448, DH = 28;
constexpr unsigned XROW = D_ * 2;
constexpr unsigned WROW = 3 * D_ * 2;
constexpr unsigned WS_EPOCH = 0;
constexpr unsigned F1_GRP = FS * 8 < 128 ? 128 : FS * 8, F2_GRP = FS * 16;
constexpr unsigned WS_F1 = 4096;
constexpr unsigned WS_F2 = WS_F1 + 32 * 4 * 1024;
constexpr unsigned WS_KVP = WS_F2 + 64 * 2048;
constexpr unsigned WS_O = WS_KVP + 32 * 8 * 4 * 1024;
constexpr unsigned WS_END = WS_O + 32 * 32 * 4 * 1024;
constexpr unsigned WS_STAMP = 8u << 20;
constexpr unsigned SLOT = 40960, SLOT_W = 32768;
constexpr unsigned L_B = 0, L_A = SLOT, L_C = 2 * SLOT;
constexpr unsigned L_WQ = 3 * SLOT;
constexpr unsigned L_GT = L_WQ + 448 * 64;
constexpr unsigned L_WPHI = L_GT + 4096;
constexpr unsigned L_SCR = L_WPHI + 2048;
constexpr unsigned L_XFREE = L_SCR + 512;
constexpr unsigned L_WOCNT = L_XFREE + 16;
constexpr unsigned L_PHI = L_B;
constexpr unsigned L_KVA = L_B + SLOT_W;
constexpr unsigned L_WO = L_A;
constexpr unsigned L_TOTAL = 163840;
static_assert(L_WPHI + 2048 <= L_TOTAL, "lds");
static_assert(L_WO + 896 * 128 == L_WPHI, "lds");
__host__ __device__ constexpr unsigned slot_of(int s) { return s % 3 == 0 ? L_A : (s % 3 == 1 ? L_B : L_C); }

struct Params {
    const void* x; const void* wqkvA; const void* wqkvB; const void* wphiA; const void* wphiB;
    const void* woA; const void* woB; const void* gate; void* out; void* ws;
};

typedef __amdgpu_buffer_rsrc_t rsrc_t;
DI rsrc_t mk_rsrc(const void* p, unsigned bytes) { return __builtin_amdgcn_make_buffer_rsrc((void*)p, (short)0, (int)bytes, 0x00020000); }
DI unsigned rfl(unsigned v) { return (unsigned)__builtin_amdgcn_readfirstlane((int)v); }
DI void dma16(rsrc_t r, unsigned voff, unsigned soff, unsigned lds) {
    asm volatile("s_mov_b32 m0, %0\n\ts_nop 0\n\tbuffer_load_dwordx4 %1, %2, %3 offen lds" ::"s"(lds), "v"(voff), "s"(r), "s"(soff) : "memory");
}
DI void dma_seg(rsrc_t rw, rsrc_t rx, unsigned vw, unsigned vx0, unsigned vx1, unsigned sw, unsigned sx, unsigned mw, unsigned mx) {
#define XP(v) "s_addk_i32 m0, 0x400\n\ts_addk_i32 %1, 0x1c00\n\tbuffer_load_dwordx4 " v ", %6, %1 offen lds\n\t"
    asm volatile("s_mov_b32 m0, %7\n\ts_nop 0\n\tbuffer_load_dwordx4 %2, %5, %0 offen lds\n\t"
                 "s_addk_i32 m0, 0x1000\n\ts_addk_i32 %0, 0x380\n\tbuffer_load_dwordx4 %2, %5, %0 offen lds\n\t"
                 "s_mov_b32 m0, %8\n\ts_nop 0\n\tbuffer_load_dwordx4 %3, %6, %1 offen lds\n\t"
                 XP("%4") XP("%3") XP("%4") XP("%3") XP("%4") XP("%3") XP("%4")
                 : "+s"(sw), "+s"(sx) : "v"(vw), "v"(vx0), "v"(vx1), "s"(rw), "s"(rx), "s"(mw), "s"(mx) : "scc", "memory");
#undef XP
}
template <int N> DI void dma_rows(rsrc_t r, unsigned v, unsigned so, unsigned m) {
    static_assert(N == 2 || N == 6 || N == 8, "");
#define RP "s_addk_i32 m0, 0x400\n\ts_addk_i32 %0, 0x1c00\n\tbuffer_load_dwordx4 %1, %2, %0 offen lds\n\t"
#define R0 "s_mov_b32 m0, %3\n\ts_nop 0\n\tbuffer_load_dwordx4 %1, %2, %0 offen lds\n\t"
    if (N == 2) asm volatile(R0 RP : "+s"(so) : "v"(v), "s"(r), "s"(m) : "scc", "memory");
    if (N == 6) asm volatile(R0 RP RP RP RP RP : "+s"(so) : "v"(v), "s"(r), "s"(m) : "scc", "memory");
    if (N == 8) asm volatile(R0 RP RP RP RP RP RP RP : "+s"(so) : "v"(v), "s"(r), "s"(m) : "scc", "memory");
#undef RP
#undef R0
}
DI void dma4(rsrc_t r, unsigned voff, unsigned soff, unsigned lds) {
    asm volatile("s_mov_b32 m0, %0\n\ts_nop 0\n\tbuffer_load_dword %1, %2, %3 offen lds" ::"s"(lds), "v"(voff), "s"(r), "s"(soff) : "memory");
}
#define PF_CODE_LINES 168
DI void gdma4(const void* a, unsigned lds) {
    asm volatile("s_mov_b32 m0, %0\n\ts_nop 0\n\tglobal_load_lds_dword %1, off" ::"s"(lds), "v"(a) : "memory");
}
#define PF_STAG 0
#define PF_WQ_SEG 1
#define PF_WO_SEG 2
#define WAIT_VM(n) asm volatile("s_waitcnt vmcnt(" #n ")" ::: "memory")
#define WAIT_VM_LGKM_BAR(n) asm volatile("s_waitcnt vmcnt(" #n ") lgkmcnt(0)\n\ts_barrier" ::: "memory")
#define LGKM_BAR() asm volatile("s_waitcnt lgkmcnt(0)\n\ts_barrier" ::: "memory")

extern __shared__ __attribute__((aligned(16))) char smem_dyn[];
DI LAS char* lds_at(unsigned off) { return (LAS char*)(uintptr_t)off; }
DI bf16x8 lds_b128(unsigned a) { return *(const LAS bf16x8*)lds_at(a); }
DI s16x4 lds_tr(unsigned a) { return __builtin_bit_cast(s16x4, __builtin_amdgcn_ds_read_tr16_b64_v4i16((LAS v4i16_t*)lds_at(a))); }
DI bf16x8 tr_frag(unsigned a0, unsigned a1) { const s16x4 lo = lds_tr(a0), hi = lds_tr(a1); return __builtin_shufflevector(lo, hi, 0, 1, 2, 3, 4, 5, 6, 7); }
DI unsigned cvtpk(float lo, float hi) { const f32x2 v = {lo, hi}; return __builtin_bit_cast(unsigned, __builtin_convertvector(v, bf2_t)); }
DI bf16x8 pack8(const f32x16& x, int s) {
    const u32x4 p = {cvtpk(x[8 * s], x[8 * s + 1]), cvtpk(x[8 * s + 2], x[8 * s + 3]), cvtpk(x[8 * s + 4], x[8 * s + 5]), cvtpk(x[8 * s + 6], x[8 * s + 7])};
    return __builtin_bit_cast(bf16x8, p);
}
DI u64 uniform64(u64 v) { return (u64)rfl((unsigned)v) | ((u64)rfl((unsigned)(v >> 32)) << 32); }
DI float addf(float a, float b) { float r; asm("v_add_f32 %0, %1, %2" : "=v"(r) : "v"(a), "v"(b)); return r; }
DI float swap_lo(float v) { auto r = __builtin_amdgcn_permlane32_swap(__float_as_uint(v), __float_as_uint(v), false, false); return __uint_as_float(r[0]); }
DI float swap_hi(float v) { auto r = __builtin_amdgcn_permlane32_swap(__float_as_uint(v), __float_as_uint(v), false, false); return __uint_as_float(r[1]); }
DI float xhalf_max(float v) { auto r = __builtin_amdgcn_permlane32_swap(__float_as_uint(v), __float_as_uint(v), false, false); return __builtin_fmaxf(__uint_as_float(r[0]), __uint_as_float(r[1])); }
DI float xhalf_min(float v) { auto r = __builtin_amdgcn_permlane32_swap(__float_as_uint(v), __float_as_uint(v), false, false); return __builtin_fminf(__uint_as_float(r[0]), __uint_as_float(r[1])); }
DI float xhalf_sum(float v) { auto r = __builtin_amdgcn_permlane32_swap(__float_as_uint(v), __float_as_uint(v), false, false); return __uint_as_float(r[0]) + __uint_as_float(r[1]); }
constexpr float L2E = 1.4426950408889634f;
constexpr unsigned ONE2 = 0x3F803F80u;
constexpr unsigned SPIN_CAP = 1u << 16;

#define STAMP_DECL do { } while (0)
#define STAMP(i) do { } while (0)
#define STAMPX(i, x) do { } while (0)
#define STAMP_FLUSH() do { } while (0)

DI void row_extrema(const f32x16& a, const f32x16& b, float (&cp)[2], float (&cn)[2]) {
    float mxa = __builtin_fmaxf(a[0], a[1]), mna = __builtin_fminf(a[0], a[1]), mxb = __builtin_fmaxf(b[0], b[1]), mnb = __builtin_fminf(b[0], b[1]);
#pragma unroll
    for (int i = 2; i < 14; i += 2) {
        mxa = __builtin_fmaxf(__builtin_fmaxf(mxa, a[i]), a[i + 1]); mxb = __builtin_fmaxf(__builtin_fmaxf(mxb, b[i]), b[i + 1]);
        mna = __builtin_fminf(__builtin_fminf(mna, a[i]), a[i + 1]); mnb = __builtin_fminf(__builtin_fminf(mnb, b[i]), b[i + 1]);
    }
    cp[0] = -xhalf_max(mxa) * L2E; cp[1] = -xhalf_max(mxb) * L2E;
    cn[0] = xhalf_min(mna) * L2E; cn[1] = xhalf_min(mnb) * L2E;
}
DI void exp_pair(const f32x16& pp, float cp, float cn, f32x2 (&ep)[7], f32x2 (&en)[7]) {
    const f32x2 l2 = {L2E, L2E}, cp2 = {cp, cp}, cn2 = {cn, cn};
#pragma unroll
    for (int i = 0; i < 7; ++i) {
        const f32x2 v = {pp[2 * i], pp[2 * i + 1]};
        const f32x2 a = __builtin_elementwise_fma(v, l2, cp2), b = __builtin_elementwise_fma(v, -l2, cn2);
        ep[i] = (f32x2){__builtin_amdgcn_exp2f(a[0]), __builtin_amdgcn_exp2f(a[1])};
        en[i] = (f32x2){__builtin_amdgcn_exp2f(b[0]), __builtin_amdgcn_exp2f(b[1])};
    }
}
DI unsigned cvtpk2(f32x2 v) { return __builtin_bit_cast(unsigned, __builtin_convertvector(v, bf2_t)); }

DI void wait_flags(const GAS u64* f, u64 tag) {
#pragma unroll 1
    for (unsigned spins = 0; spins < SPIN_CAP; ++spins) {
        const u64 v = __hip_atomic_load(f, __ATOMIC_RELAXED, __HIP_MEMORY_SCOPE_AGENT);
        if (__builtin_amdgcn_ballot_w64(v != tag) == 0) break;
        __builtin_amdgcn_s_sleep(1);
    }
    asm volatile("" ::: "memory");
}

#define SB() __builtin_amdgcn_sched_barrier(0)
#define PINV(x) asm volatile("" : "+v"(x))
struct Frag { bf16x8 x0, x1, fk, fv; };

DI void build_aphi(bf16x8 (&aphi)[2], int r, int h2) {
    const int hr = (r >> 2) & 1, reg = (r & 3) + 4 * (r >> 3), f = 14 * hr + (reg < 14 ? reg : 0);
    const unsigned a = L_WPHI + 2 * (4 * h2 * DH + f);
#pragma unroll
    for (int s = 0; s < 2; ++s)
#pragma unroll
        for (int jj = 0; jj < 8; ++jj) {
            const int d0 = 16 * s + 8 * (jj >> 2) + (jj & 3);
            const bool ok = reg < 14 && (d0 + 4 < DH || h2 == 0);
            const unsigned short v = *(const LAS unsigned short*)lds_at(a + 2 * d0 * DH);
            aphi[s][jj] = ok ? (short)v : (short)0;
        }
}

__global__ void __launch_bounds__(512, 2) hedgehog_mix_kernel(Params p) {
    const int tid = threadIdx.x, lane = tid & 63;
    const unsigned w = rfl(tid >> 6), cw = w & 3;
    if (w == 4) {
        u64 pc; asm volatile("s_getpc_b64 %0" : "=s"(pc));
        const unsigned l0 = (blockIdx.x >> 3) * 6 + (lane < 6 ? lane : 5), l = l0 < PF_CODE_LINES ? l0 : 0;
        gdma4((const char*)(pc & ~127ull) + l * 128, L_SCR + 256);
    }
    const int r = lane & 31, h2 = lane >> 5, i16 = lane & 15, q4 = i16 >> 2, p4 = i16 & 3, blk16 = (lane >> 4) & 1;
    const unsigned xcd = blockIdx.x & 7, j = blockIdx.x >> 3;
    const unsigned tb = (xcd & 1) * 4 + (j & 3), bh = (xcd >> 1) * 8 + (j >> 2), br = bh >> 4, h = bh & 15;
    const unsigned og = xcd * 4 + j / 7, oc = j % 7;
    const bool has_out = j < 28;
    const unsigned rot = ROT == 1 ? ((j >> 2) + 2 * (j & 3)) % 7 : (ROT == 2 ? (2 * (j & 3)) % 7 : 0);
    auto seg = [&](int n) { const unsigned t = rot + n; return t >= 7 ? t - 7 : t; };
#define PIN_ARGS() asm volatile("" : "+s"(p.x), "+s"(p.wqkvA), "+s"(p.wqkvB), "+s"(p.wphiA), "+s"(p.wphiB), "+s"(p.woA), "+s"(p.woB), "+s"(p.gate), "+s"(p.out), "+s"(p.ws))
    PIN_ARGS();
    STAMP_DECL;
    STAMP(0);
    const rsrc_t rws = mk_rsrc(p.ws, WS_END);
    const f32x16 zero16 = {};
    const int cq = 4 * blk16 + p4;
    const unsigned rowpart = (8 * h2 + q4) * 64;
    const unsigned coloff = (8 * (h & 1) + 8 * cq) & 63;
    const unsigned laneX = cw * 8192 + r * 128 + 16 * (h2 ^ ((r >> 1) & 7));

    if (__builtin_expect(w >= 4, 1)) {
        const rsrc_t rx = mk_rsrc(p.x, S_ * XROW);
        const rsrc_t rw = mk_rsrc(br ? p.wqkvB : p.wqkvA, D_ * WROW);
        const unsigned vx0 = (lane >> 3) * XROW + 16 * ((lane & 7) ^ (lane >> 4));
        const unsigned vx1 = (lane >> 3) * XROW + 16 * ((lane & 7) ^ (4 + (lane >> 4)));
        const unsigned vw = (lane >> 2) * WROW + 16 * (lane & 3);
        const unsigned sx = (tb * 256 + cw * 64) * XROW;
        const unsigned sw = cw * 16 * WROW + h * 56 - 8 * (h & 1);
        auto issue = [&](int s) {
            dma_seg(rw, rx, vw, vx0, vx1, sw + seg(s) * (64 * WROW) + 896, sx + seg(s) * 128, slot_of(s) + SLOT_W + cw * 1024, slot_of(s) + cw * 8192);
        };
        bf16x8 xs[5][4][2];
        auto grab = [&](int s) {
#pragma unroll
            for (int ks = 0; ks < 4; ++ks) {
                const unsigned a = (laneX ^ (32 * ks)) + slot_of(s);
                xs[s][ks][0] = lds_b128(a); xs[s][ks][1] = lds_b128(a + 4096);
            }
        };
        STAMP(11);
        issue(0); issue(1);
        STAMP(1);
        WAIT_VM_LGKM_BAR(10);
        STAMP(2);
        grab(0); issue(2); WAIT_VM_LGKM_BAR(10);
        grab(1); issue(3); WAIT_VM_LGKM_BAR(10);
        grab(2); issue(4); WAIT_VM_LGKM_BAR(10);
        grab(3); issue(5); WAIT_VM_LGKM_BAR(10);
        grab(4); issue(6); WAIT_VM_LGKM_BAR(10);
        WAIT_VM_LGKM_BAR(0);
        STAMP(3);
#pragma unroll
        for (int s = 0; s < 7; ++s) { const unsigned sg = seg((s + 5) % 7); dma16(rw, vw, sw + sg * (64 * WROW), L_WQ + sg * 4096 + cw * 1024); }
        bf16x8 aphi[2];
        build_aphi(aphi, r, h2);
        PINV(aphi[0]); PINV(aphi[1]);
        u64 ev;
        asm volatile("global_load_dwordx2 %0, %1, %2 sc1" : "=&v"(ev) : "v"(0u), "s"(p.ws) : "memory");
        STAMP(4);
        asm volatile("s_waitcnt vmcnt(0) lgkmcnt(0)\n\ts_barrier" : "+v"(ev) :: "memory");
        const u64 tag = uniform64(ev) + 1;
        STAMP(5);
        f32x16 qT[2] = {zero16, zero16};
        {
            const unsigned aq_real = L_WQ + rowpart + coloff, aq_pad = L_GT + rowpart;
            bf16x8 fq[3], xl[2][2];
            auto seg_of = [](int n) { return n < 8 ? 5 + (n >> 2) : (n - 8) >> 2; };
            unsigned aQ = 0;
            auto rdq = [&](int n) {
                if ((n & 3) == 0) { const unsigned sg = seg(seg_of(n)); aQ = cq == 7 ? aq_pad + 8 * sg : aq_real + 4096 * sg; PINV(aQ); }
                fq[n % 3] = tr_frag(aQ + (n & 3) * 1024, aQ + (n & 3) * 1024 + 256);
            };
            auto rdx = [&](int n) { const unsigned a = (laneX ^ (32 * (n & 3))) + slot_of(seg_of(n)); xl[n & 1][0] = lds_b128(a); xl[n & 1][1] = lds_b128(a + 4096); };
            rdq(0); rdx(0); rdq(1);
            SB();
#pragma unroll
            for (int n = 0; n < 28; ++n) {
                const int s = seg_of(n), ks = n & 3;
                if (n + 1 < 8) rdx(n + 1);
                if (n + 2 < 28) rdq(n + 2);
                SB();
                qT[0] = MFMA(fq[n % 3], n >= 8 ? xs[n >= 8 ? s : 0][ks][0] : xl[n & 1][0], qT[0]);
                qT[1] = MFMA(fq[n % 3], n >= 8 ? xs[n >= 8 ? s : 0][ks][1] : xl[n & 1][1], qT[1]);
                SB();
                if (n == 7) {
                    asm volatile("s_waitcnt lgkmcnt(0)" ::: "memory");
                    *(volatile LAS unsigned*)lds_at(L_XFREE + 4 * cw) = 1u;
                }
            }
        }
        STAMP(6);
        bf16x8 eq[2][2][2];
        float gs[2];
        {
            f32x16 pq[2];
            const bf16x8 q00 = pack8(qT[0], 0), q10 = pack8(qT[1], 0);
            pq[0] = MFMA(aphi[0], q00, zero16); pq[1] = MFMA(aphi[0], q10, zero16);
            const bf16x8 q01 = pack8(qT[0], 1), q11 = pack8(qT[1], 1);
            pq[0] = MFMA(aphi[1], q01, pq[0]); pq[1] = MFMA(aphi[1], q11, pq[1]);
#pragma unroll
            for (int b = 0; b < 2; ++b) {
                const float lg = swap_hi(qT[b][12]);
                gs[b] = __builtin_amdgcn_rcpf(1.f + __builtin_amdgcn_exp2f(br ? lg * L2E : -lg * L2E));
                PINV(gs[b]);
            }
            float cp[2], cn[2];
            row_extrema(pq[0], pq[1], cp, cn);
            PINV(cp[0]); PINV(cp[1]); PINV(cn[0]); PINV(cn[1]);
#pragma unroll
            for (int b = 0; b < 2; ++b) {
                const f32x2 l2 = {L2E, L2E}, cp2 = {cp[b], cp[b]}, cn2 = {cn[b], cn[b]};
                unsigned pe[8], ne[8];
#pragma unroll
                for (int i = 0; i < 7; ++i) {
                    const f32x2 v = {pq[b][2 * i], pq[b][2 * i + 1]};
                    const f32x2 a = __builtin_elementwise_fma(v, l2, cp2), c = __builtin_elementwise_fma(v, -l2, cn2);
                    pe[i] = cvtpk2((f32x2){__builtin_amdgcn_exp2f(a[0]), __builtin_amdgcn_exp2f(a[1])});
                    ne[i] = cvtpk2((f32x2){__builtin_amdgcn_exp2f(c[0]), __builtin_amdgcn_exp2f(c[1])});
                }
                pe[7] = 0; ne[7] = 0;
                eq[b][0][0] = __builtin_bit_cast(bf16x8, (u32x4){pe[0], pe[1], pe[2], pe[3]}); eq[b][0][1] = __builtin_bit_cast(bf16x8, (u32x4){pe[4], pe[5], pe[6], pe[7]});
                eq[b][1][0] = __builtin_bit_cast(bf16x8, (u32x4){ne[0], ne[1], ne[2], ne[3]}); eq[b][1][1] = __builtin_bit_cast(bf16x8, (u32x4){ne[4], ne[5], ne[6], ne[7]});
            }
        }
#pragma unroll
        for (int b = 0; b < 2; ++b) asm volatile("" : "+v"(eq[b][0][0]), "+v"(eq[b][0][1]), "+v"(eq[b][1][0]), "+v"(eq[b][1][1]), "+v"(gs[b]));
        STAMP(7);
        LGKM_BAR();
        __builtin_amdgcn_s_setprio(3);
        STAMP(8);
        {
            bf16x8 akv[4];
#pragma unroll
            for (int f = 0; f < 4; ++f) akv[f] = lds_b128(L_KVA + f * 1024 + lane * 16);
#pragma unroll
            for (int b = 0; b < 2; ++b) {
                f32x16 np = MFMA(akv[0], eq[b][0][0], zero16); np = MFMA(akv[1], eq[b][0][1], np);
                f32x16 nn = MFMA(akv[2], eq[b][1][0], zero16); nn = MFMA(akv[3], eq[b][1][1], nn);
                const float ia = __builtin_amdgcn_rcpf(np[15]), ib = __builtin_amdgcn_rcpf(nn[15]);
                const float den = np[14] * ia + nn[14] * ib + 1e-6f;
                const float sc = gs[b] * __builtin_amdgcn_rcpf(den);
                const float ca = ia * sc, cb = ib * sc;
                float o[14];
#pragma unroll
                for (int i = 0; i < 14; ++i) o[i] = np[i] * ca + nn[i] * cb;
                const u32x4 o0 = {cvtpk(o[0], o[1]), cvtpk(o[2], o[3]), cvtpk(o[4], o[5]), cvtpk(o[6], o[7])};
                const u32x4 o1 = {cvtpk(o[8], o[9]), cvtpk(o[10], o[11]), cvtpk(o[12], o[13]), 0u};
                const unsigned so = WS_O + (((tb * 4 + cw) * 32 + bh) * 4 + b * 2) * 1024;
                __builtin_amdgcn_raw_buffer_store_b128(o0, rws, lane * 16, so, 16);
                __builtin_amdgcn_raw_buffer_store_b128(o1, rws, lane * 16, so + 1024, 16);
            }
        }
        STAMP(9);
        WAIT_VM(0);
        if (lane == 0) __hip_atomic_store((GAS u64*)((char*)p.ws + WS_F2 + ((tb * 4 + cw) * 2 + br) * F2_GRP + h * FS), tag, __ATOMIC_RELAXED, __HIP_MEMORY_SCOPE_AGENT);
        STAMP(10);
        STAMP_FLUSH();
        return;
    }

    u64 tag;
    {
        const rsrc_t rg = mk_rsrc(p.gate, XROW);
        const rsrc_t rp = mk_rsrc(br ? p.wphiB : p.wphiA, 16 * DH * DH * 2);
        unsigned g0, g1; u32x2 wp;
        u64 ev;
        asm volatile("global_load_dwordx2 %0, %1, %2 sc1" : "=&v"(ev) : "v"(0u), "s"(p.ws) : "memory");
        asm volatile("buffer_load_ushort %0, %3, %4, 0 offen\n\tbuffer_load_ushort %1, %3, %4, 0 offen offset:512\n\tbuffer_load_dwordx2 %2, %5, %6, %7 offen"
                     : "=&v"(g0), "=&v"(g1), "=&v"(wp) : "v"(tid * 2), "s"(rg), "v"(tid * 8), "s"(rp), "s"(h * (DH * DH * 2)) : "memory");
        asm volatile("s_waitcnt vmcnt(0) lgkmcnt(0)" : "+v"(g0), "+v"(g1), "+v"(wp), "+v"(ev) :: "memory");
        tag = uniform64(ev) + 1;
        *(LAS u32x2*)lds_at(L_GT + (tid & 63) * 64 + (tid >> 6) * 8) = (u32x2){g0 & 0xffffu, 0u};
        *(LAS u32x2*)lds_at(L_GT + (tid & 63) * 64 + (4 + (tid >> 6)) * 8) = (u32x2){g1 & 0xffffu, 0u};
        *(LAS u32x2*)lds_at(L_WPHI + tid * 8) = wp;
        if (tid < 5) *(volatile LAS unsigned*)lds_at(L_XFREE + 4 * tid) = 0u;
    }
    __builtin_amdgcn_s_setprio(2);
    LGKM_BAR();
    STAMP(2);
    bf16x8 aphi[2];
    build_aphi(aphi, r, h2);
    PINV(aphi[0]); PINV(aphi[1]);
    const unsigned laneW = SLOT_W + rowpart + coloff;
    f32x16 vinit;
#pragma unroll
    for (int i = 0; i < 16; ++i) vinit[i] = r == 28 ? 1.f : 0.f;
    PINV(vinit);
    f32x16 kT[2] = {zero16, zero16}, vv[2] = {vinit, vinit};
    {
        unsigned aX[4], aW, aV;
        auto addr = [&](int s) {
#pragma unroll
            for (int ks = 0; ks < 4; ++ks) { aX[ks] = (laneX ^ (32 * ks)) + slot_of(s); PINV(aX[ks]); }
            aW = laneW + slot_of(s); PINV(aW);
            aV = cq == 7 ? L_GT + rowpart + 56 : aW + 4096; PINV(aV);
        };
        Frag F[2];
#define RD_X(f, ks) do { f.x0 = lds_b128(aX[ks]); } while (0)
#define RD_X1(f, ks) do { f.x1 = lds_b128(aX[ks] + 4096); } while (0)
#define RD_K(f, ks) do { f.fk = tr_frag(aW + ks * 1024, aW + ks * 1024 + 256); } while (0)
#define RD_V(f, ks) do { f.fv = tr_frag(aV + ks * 1024, aV + ks * 1024 + 256); } while (0)
        addr(0);
        RD_X(F[0], 0); RD_K(F[0], 0); RD_X1(F[0], 0); RD_V(F[0], 0); SB();
#pragma unroll
        for (int n = 0; n < 28; ++n) {
            Frag& c = F[n & 1];
            Frag& x = F[(n + 1) & 1];
            const int nks = (n + 1) & 3;
            const bool more = n + 1 < 28;
            kT[0] = MFMA(c.fk, c.x0, kT[0]); SB();
            if (more && nks == 0) { LGKM_BAR(); addr((n + 1) >> 2); SB(); }
            if (more) { RD_X(x, nks); RD_K(x, nks); } SB();
            kT[1] = MFMA(c.fk, c.x1, kT[1]); SB();
            if (more) { RD_X1(x, nks); } SB();
            vv[0] = MFMA(c.x0, c.fv, vv[0]); SB();
            if (more) { RD_V(x, nks); } SB();
            vv[1] = MFMA(c.x1, c.fv, vv[1]); SB();
        }
    }
    STAMP(3);

    const unsigned phiW = L_PHI + cw * 8192;
    f32x16 pk[2];
    {
        const bf16x8 k00 = pack8(kT[0], 0), k10 = pack8(kT[1], 0);
        pk[0] = MFMA(aphi[0], k00, zero16); pk[1] = MFMA(aphi[0], k10, zero16);
        const bf16x8 k01 = pack8(kT[0], 1), k11 = pack8(kT[1], 1);
        pk[0] = MFMA(aphi[1], k01, pk[0]); pk[1] = MFMA(aphi[1], k11, pk[1]);
    }
    bf16x8 vp[2][2];
#pragma unroll
    for (int b = 0; b < 2; ++b)
#pragma unroll
        for (int s = 0; s < 2; ++s) { vp[b][s] = pack8(vv[b], s); PINV(vp[b][s]); }
    SB();
    float cp[2], cn[2];
    row_extrema(pk[0], pk[1], cp, cn);
    const unsigned pt0 = 4 * h2 + q4;
    const unsigned pa0 = phiW + pt0 * 64 + 8 * (cq ^ ((pt0 >> 1) & 7));
    const unsigned pa1 = (pa0 ^ 32) + 8 * 64;
    f32x16 kvp = zero16, kvn = zero16;
#pragma unroll
    for (int b = 0; b < 2; ++b) {
        f32x2 ep[7], en[7];
        exp_pair(pk[b], cp[b], cn[b], ep, en);
        f32x2 sp = ep[0], sn = en[0];
#pragma unroll
        for (int i = 1; i < 7; ++i) { sp += ep[i]; sn += en[i]; }
        const float ip = __builtin_amdgcn_rcpf(xhalf_sum(sp[0] + sp[1])), in = __builtin_amdgcn_rcpf(xhalf_sum(sn[0] + sn[1]));
        const f32x2 ip2 = {ip, ip}, in2 = {in, in};
        const unsigned base = phiW + b * 4096 + r * 64;
#pragma unroll
        for (int g = 0; g < 4; ++g) {
            const unsigned a = base + 8 * ((2 * g + h2) ^ ((r >> 1) & 7));
            const u32x2 dp = {cvtpk2(ep[2 * g] * ip2), g < 3 ? cvtpk2(ep[(2 * g + 1) % 7] * ip2) : 0u};
            const u32x2 dn = {cvtpk2(en[2 * g] * in2), g < 3 ? cvtpk2(en[(2 * g + 1) % 7] * in2) : 0u};
            *(LAS u32x2*)lds_at(a) = dp;
            *(LAS u32x2*)lds_at(a + 2048) = dn;
        }
#pragma unroll
        for (int s = 0; s < 2; ++s) {
            const unsigned o = b * 4096 + s * 1024;
            kvp = MFMA(tr_frag(pa0 + o, pa1 + o), vp[b][s], kvp);
            kvn = MFMA(tr_frag(pa0 + o + 2048, pa1 + o + 2048), vp[b][s], kvn);
        }
    }
    asm volatile("s_waitcnt lgkmcnt(0)" ::: "memory");
#pragma unroll
    for (int f = 0; f < 4; ++f)
#pragma unroll
        for (int hh = 0; hh < 2; ++hh) {
            const f32x16& t = f < 2 ? kvp : kvn;
            const int o = 8 * (f & 1) + 4 * hh;
            *(LAS f32x4*)lds_at(phiW + f * 2048 + hh * 1024 + lane * 16) = (f32x4){t[o], t[o + 1], t[o + 2], t[o + 3]};
        }
    STAMP(4);
    LGKM_BAR();
    STAMP(5);
    {
        f32x4 t[4][2];
#pragma unroll
        for (int u = 0; u < 4; ++u) {
            t[u][0] = *(const LAS f32x4*)lds_at(L_PHI + u * 8192 + cw * 2048 + lane * 16);
            t[u][1] = *(const LAS f32x4*)lds_at(L_PHI + u * 8192 + cw * 2048 + 1024 + lane * 16);
        }
        SB();
        float a[8];
#pragma unroll
        for (int i = 0; i < 8; ++i) {
            float x01 = addf(t[0][i >> 2][i & 3], t[1][i >> 2][i & 3]), x23 = addf(t[2][i >> 2][i & 3], t[3][i >> 2][i & 3]);
            a[i] = addf(x01, x23);
        }
        u32x4 o = {cvtpk(a[0], a[1]), cvtpk(a[2], a[3]), cvtpk(a[4], a[5]), cvtpk(a[6], a[7])};
        STAMPX(1, o);
        __builtin_amdgcn_raw_buffer_store_b128(o, rws, lane * 16, WS_KVP + ((bh * 8 + tb) * 4 + cw) * 1024, 16);
    }
    {
        const rsrc_t roA = mk_rsrc(p.woA, D_ * XROW), roB = mk_rsrc(p.woB, D_ * XROW);
        const unsigned vwo = (lane >> 3) * XROW + 16 * ((lane & 7) ^ (4 * ((lane >> 4) & 1)));
        dma_rows<2>(roA, vwo, (256 + 16 * cw) * XROW + oc * 128, L_WO + (256 + 16 * cw) * 128);
        dma_rows<2>(roB, vwo, (576 - 448 + 16 * cw) * XROW + oc * 128, L_WO + (576 + 16 * cw) * 128);
#pragma unroll 1
        for (unsigned spins = 0; spins < SPIN_CAP; ++spins) { if (*(volatile LAS unsigned*)lds_at(L_XFREE + 4 * cw) != 0u) break; }
        dma_rows<6>(roA, vwo, 64 * cw * XROW + oc * 128, L_WO + 64 * cw * 128);
        WAIT_VM(10);
        if (lane == 0) __hip_atomic_store((GAS u64*)((char*)p.ws + WS_F1 + (bh * 4 + cw) * F1_GRP + tb * FS), tag, __ATOMIC_RELAXED, __HIP_MEMORY_SCOPE_AGENT);
        STAMP(9);
        dma_rows<2>(roA, vwo, (64 * cw + 48) * XROW + oc * 128, L_WO + (64 * cw + 48) * 128);
        const rsrc_t roC = cw < 2 ? roA : roB;
        const unsigned R = 320 + 64 * cw, Rg = cw < 2 ? R : R - 448;
        dma_rows<8>(roC, vwo, Rg * XROW + oc * 128, L_WO + R * 128);
    }
    bf16x8 sel;
    {
        const int jj = (r & 3) + 4 * (r >> 3);
        const bool on = r < 16 && ((r >> 2) & 1) == h2;
        const unsigned one = on ? (0x3F80u << (16 * (jj & 1))) : 0u;
        sel = __builtin_bit_cast(bf16x8, (u32x4){(jj >> 1) == 0 ? one : 0u, (jj >> 1) == 1 ? one : 0u, (jj >> 1) == 2 ? one : 0u, (jj >> 1) == 3 ? one : 0u});
        PINV(sel);
    }
    STAMP(6);
    wait_flags((const GAS u64*)((const char*)p.ws + WS_F1 + (bh * 4 + cw) * F1_GRP + (lane & 7) * FS), tag);
    STAMP(7);
    {
        const int hr = (r >> 2) & 1, reg = (r & 3) + 4 * (r >> 3);
        const int col = reg < 14 ? 14 * hr + reg : 28;
        const unsigned vo = (h2 * 32 + col) * 16;
        u32x4 part[8];
#pragma unroll
        for (int u = 0; u < 8; ++u) part[u] = __builtin_amdgcn_raw_buffer_load_b128(rws, vo, WS_KVP + ((bh * 8 + u) * 4 + cw) * 1024, 16);
        SB();
        f32x16 acc = zero16;
#pragma unroll
        for (int u = 0; u < 8; ++u) acc = MFMA(sel, __builtin_bit_cast(bf16x8, part[u]), acc);
        u32x4 o = __builtin_bit_cast(u32x4, pack8(acc, 0));
        if (reg == 15) o = (u32x4){ONE2, ONE2, ONE2, ONE2};
        *(LAS u32x4*)lds_at(L_KVA + cw * 1024 + lane * 16) = o;
    }
    LGKM_BAR();
    STAMP(8);
    if (!has_out) { STAMP_FLUSH(); return; }
    {
        const rsrc_t ro = mk_rsrc(p.woB, D_ * XROW);
        const unsigned vo = (lane >> 3) * XROW + 16 * ((lane & 7) ^ (4 * ((lane >> 4) & 1)));
        dma_rows<8>(ro, vo, (640 - 448 + 64 * cw) * XROW + oc * 128, L_WO + (640 + 64 * cw) * 128);
    }
    WAIT_VM(0);
    if (lane == 0) __hip_atomic_fetch_add((LAS unsigned*)lds_at(L_WOCNT), 1u, __ATOMIC_RELAXED, __HIP_MEMORY_SCOPE_WORKGROUP);
#pragma unroll 1
    for (unsigned spins = 0; spins < SPIN_CAP; ++spins) { if (*(volatile LAS unsigned*)lds_at(L_WOCNT) >= 4u) break; }
    asm volatile("" ::: "memory");
    STAMP(10);

    const unsigned tk = cw & 1, kh = cw >> 1;
    constexpr u64 HORD = 0xDB9642FECA875310ull;
    const GAS u64* f2 = (const GAS u64*)((const char*)p.ws + WS_F2 + (og * 2 + kh) * F2_GRP);
    f32x16 mine = zero16, other = zero16;
    {
        const unsigned colp = 32 * blk16 + 8 * p4;
        const unsigned r0 = kh * 448 + 14 * h2 + q4, r1 = kh * 448 + 14 * h2 + (q4 < 1 ? q4 : 1);
        unsigned a0 = L_WO + r0 * 128 + 64 * (kh ^ ((r0 >> 1) & 1)) + colp;
        unsigned a1 = L_WO + r1 * 128 + 64 * (kh ^ ((r1 >> 1) & 1)) + colp;
        unsigned a2 = a0 ^ 64, a3 = a1 ^ 64;
        asm volatile("" : "+v"(a0), "+v"(a1), "+v"(a2), "+v"(a3));
        u32x4 of[16][2];
        auto ld = [&](int i) {
            const unsigned hd = (unsigned)(HORD >> (4 * i)) & 15u;
#pragma unroll
            for (int s = 0; s < 2; ++s)
                of[i][s] = __builtin_amdgcn_raw_buffer_load_b128(rws, lane * 16, WS_O + (((og * 32 + kh * 16 + hd) * 2 + tk) * 2 + s) * 1024, 16);
        };
        bf16x8 A[2][4];
        auto rd = [&](bf16x8 (&f)[4], int i) {
            const unsigned o = ((unsigned)(HORD >> (4 * i)) & 15u) * 28 * 128;
            f[0] = tr_frag(a0 + o, a0 + o + 512); f[1] = tr_frag(a2 + o, a2 + o + 512);
            f[2] = tr_frag(a0 + o + 1024, a1 + o + 1536); f[3] = tr_frag(a2 + o + 1024, a3 + o + 1536);
        };
        auto step = [&](int i) {
            if (i + 1 < 16) rd(A[(i + 1) & 1], i + 1);
            SB();
            const bf16x8 b0 = __builtin_bit_cast(bf16x8, of[i][0]), b1 = __builtin_bit_cast(bf16x8, of[i][1]);
            mine = MFMA(A[i & 1][0], b0, mine); other = MFMA(A[i & 1][1], b0, other);
            mine = MFMA(A[i & 1][2], b1, mine); other = MFMA(A[i & 1][3], b1, other);
            SB();
        };
        wait_flags(f2 + (FS / 8) * ((HORD >> (4 * (lane % 10))) & 15), tag);
        STAMP(14);
#pragma unroll
        for (int i = 0; i < 10; ++i) ld(i);
        SB();
        rd(A[0], 0); SB();
#pragma unroll
        for (int i = 0; i < 10; ++i) step(i);
        STAMP(15);
        wait_flags(f2 + (FS / 8) * ((HORD >> (4 * (10 + lane % 6))) & 15), tag);
        STAMP(11);
#pragma unroll
        for (int i = 10; i < 16; ++i) ld(i);
        SB();
#pragma unroll
        for (int i = 10; i < 16; ++i) step(i);
    }
    STAMP(12);
#pragma unroll
    for (int g = 0; g < 4; ++g)
        *(LAS f32x4*)lds_at(L_PHI + cw * 4096 + g * 1024 + lane * 16) = (f32x4){other[4 * g], other[4 * g + 1], other[4 * g + 2], other[4 * g + 3]};
    LGKM_BAR();
    if (blockIdx.x == 0 && tid == 0) __hip_atomic_store((GAS u64*)((char*)p.ws + WS_EPOCH), tag, __ATOMIC_RELAXED, __HIP_MEMORY_SCOPE_AGENT);
    {
        unsigned d[8];
#pragma unroll
        for (int g = 0; g < 4; ++g) {
            const f32x4 t = *(const LAS f32x4*)lds_at(L_PHI + (cw ^ 2) * 4096 + g * 1024 + lane * 16);
            d[2 * g] = cvtpk(mine[4 * g] + t[0], mine[4 * g + 1] + t[1]);
            d[2 * g + 1] = cvtpk(mine[4 * g + 2] + t[2], mine[4 * g + 3] + t[3]);
        }
        const rsrc_t rout = mk_rsrc(p.out, S_ * XROW);
        const unsigned vo = r * XROW + h2 * 16, so = (og * 64 + tk * 32) * XROW + oc * 128 + kh * 64;
#pragma unroll
        for (int k = 0; k < 4; k += 2) {
            auto sx0 = __builtin_amdgcn_permlane32_swap(d[2 * k], d[2 * k + 2], false, false);
            auto sx1 = __builtin_amdgcn_permlane32_swap(d[2 * k + 1], d[2 * k + 3], false, false);
            const u32x4 o = {sx0[0], sx1[0], sx0[1], sx1[1]};
            __builtin_amdgcn_raw_buffer_store_b128(o, rout, vo, so + 16 * k, 0);
        }
    }
    STAMP(13);
    STAMP_FLUSH();
}

extern "C" void kernel_launch(void* const* d_in, const int* in_sizes, int n_in, void* d_out, int out_size, void* d_ws, size_t ws_size,
                              hipStream_t stream) {
    Params p;
    p.x = d_in[0]; p.wqkvA = d_in[1]; p.wphiA = d_in[2]; p.woA = d_in[3];
    p.wqkvB = d_in[5]; p.wphiB = d_in[6]; p.woB = d_in[7]; p.gate = d_in[8];
    p.out = d_out; p.ws = d_ws;
    (void)hipFuncSetAttribute((const void*)hedgehog_mix_kernel, hipFuncAttributeMaxDynamicSharedMemorySize, L_TOTAL);
    hedgehog_mix_kernel<<<256, 512, L_TOTAL, stream>>>(p);
}
```

```cpp
#define PREFETCH 0
#define PF_CODE 1
#define DATAPOLL 1
#include <hip/hip_runtime.h>
#include <stdint.h>

typedef short bf16x8 __attribute__((ext_vector_type(8)));
typedef short s16x4 __attribute__((ext_vector_type(4)));
typedef short v4i16_t __attribute__((ext_vector_type(4)));
typedef __bf16 bf2_t __attribute__((ext_vector_type(2)));
typedef float f32x2 __attribute__((ext_vector_type(2)));
typedef float f32x4 __attribute__((ext_vector_type(4)));
typedef float f32x16 __attribute__((ext_vector_type(16)));
typedef unsigned u32x2 __attribute__((ext_vector_type(2)));
typedef unsigned u32x4 __attribute__((ext_vector_type(4)));
typedef unsigned long long u64;
#define LAS __attribute__((address_space(3)))
#define GAS __attribute__((address_space(1)))
#define DI __device__ __forceinline__
#define MFMA(a, b, c) __builtin_amdgcn_mfma_f32_32x32x16_bf16((a), (b), (c), 0, 0, 0)

constexpr int S_ = 2048, D_ = 448, DH = 28;
constexpr unsigned XROW = D_ * 2;
constexpr unsigned WROW = 3 * D_ * 2;
constexpr unsigned WS_EPOCH = 0;
#define FS 128
#define PZ_STORE 1
#define PZ_FLAG 9
constexpr unsigned F1_GRP = FS * 8 < 128 ? 128 : FS * 8, F2_GRP = FS * 16;
constexpr unsigned WS_F1 = 4096;
constexpr unsigned WS_F2 = WS_F1 + 32 * 4 * 1024;
constexpr unsigned WS_KVP = WS_F2 + 64 * 2048;
constexpr unsigned WS_O = WS_KVP + 32 * 8 * 4 * 1024;
constexpr unsigned WS_END = WS_O + 32 * 32 * 4 * 1024;
constexpr unsigned WS_STAMP = 8u << 20;
constexpr unsigned SLOT = 40960, SLOT_W = 32768;
constexpr unsigned L_B = 0, L_A = SLOT, L_C = 2 * SLOT;
constexpr unsigned L_WQ = 3 * SLOT;
constexpr unsigned L_GT = L_WQ + 448 * 64;
constexpr unsigned L_WPHI = L_GT + 4096;
constexpr unsigned L_SCR = L_WPHI + 2048;
constexpr unsigned L_XFREE = L_SCR + 512;
constexpr unsigned L_WOCNT = L_XFREE + 16;
constexpr unsigned L_PHI = L_B;
constexpr unsigned L_KVA = L_B + SLOT_W;
constexpr unsigned L_WO = L_A;
constexpr unsigned L_TOTAL = 163840;
static_assert(L_WPHI + 2048 <= L_TOTAL, "lds");
static_assert(L_WO + 896 * 128 == L_WPHI, "lds");
__host__ __device__ constexpr unsigned slot_of(int s) { return s % 3 == 0 ? L_A : (s % 3 == 1 ? L_B : L_C); }

struct Params {
    const void* x; const void* wqkvA; const void* wqkvB; const void* wphiA; const void* wphiB;
    const void* woA; const void* woB; const void* gate; void* out; void* ws;
};

typedef __amdgpu_buffer_rsrc_t rsrc_t;
DI rsrc_t mk_rsrc(const void* p, unsigned bytes) { return __builtin_amdgcn_make_buffer_rsrc((void*)p, (short)0, (int)bytes, 0x00020000); }
DI unsigned rfl(unsigned v) { return (unsigned)__builtin_amdgcn_readfirstlane((int)v); }
DI void dma16(rsrc_t r, unsigned voff, unsigned soff, unsigned lds) {
    asm volatile("s_mov_b32 m0, %0\n\ts_nop 0\n\tbuffer_load_dwordx4 %1, %2, %3 offen lds" ::"s"(lds), "v"(voff), "s"(r), "s"(soff) : "memory");
}
DI void dma_seg(rsrc_t rw, rsrc_t rx, unsigned vw, unsigned vx0, unsigned vx1, unsigned sw, unsigned sx, unsigned mw, unsigned mx) {
#define XP(v) "s_addk_i32 m0, 0x400\n\ts_addk_i32 %1, 0x1c00\n\tbuffer_load_dwordx4 " v ", %6, %1 offen lds\n\t"
    asm volatile("s_mov_b32 m0, %7\n\ts_nop 0\n\tbuffer_load_dwordx4 %2, %5, %0 offen lds\n\t"
                 "s_addk_i32 m0, 0x1000\n\ts_addk_i32 %0, 0x380\n\tbuffer_load_dwordx4 %2, %5, %0 offen lds\n\t"
                 "s_mov_b32 m0, %8\n\ts_nop 0\n\tbuffer_load_dwordx4 %3, %6, %1 offen lds\n\t"
                 XP("%4") XP("%3") XP("%4") XP("%3") XP("%4") XP("%3") XP("%4")
                 : "+s"(sw), "+s"(sx) : "v"(vw), "v"(vx0), "v"(vx1), "s"(rw), "s"(rx), "s"(mw), "s"(mx) : "scc", "memory");
#undef XP
}
template <int N> DI void dma_rows(rsrc_t r, unsigned v, unsigned so, unsigned m) {
    static_assert(N == 2 || N == 6 || N == 8, "");
#define RP "s_addk_i32 m0, 0x400\n\ts_addk_i32 %0, 0x1c00\n\tbuffer_load_dwordx4 %1, %2, %0 offen lds\n\t"
#define R0 "s_mov_b32 m0, %3\n\ts_nop 0\n\tbuffer_load_dwordx4 %1, %2, %0 offen lds\n\t"
    if (N == 2) asm volatile(R0 RP : "+s"(so) : "v"(v), "s"(r), "s"(m) : "scc", "memory");
    if (N == 6) asm volatile(R0 RP RP RP RP RP : "+s"(so) : "v"(v), "s"(r), "s"(m) : "scc", "memory");
    if (N == 8) asm volatile(R0 RP RP RP RP RP RP RP : "+s"(so) : "v"(v), "s"(r), "s"(m) : "scc", "memory");
#undef RP
#undef R0
}
DI u32x4 load_sc1_asm(rsrc_t r, unsigned voff, unsigned soff) {
    u32x4 v; asm volatile("buffer_load_dwordx4 %0, %1, %2, %3 offen sc1" : "=&v"(v) : "v"(voff), "s"(r), "s"(soff) : "memory"); return v;
}
DI unsigned umax3(unsigned a, unsigned b, unsigned c) { const unsigned t = a > b ? a : b; return t > c ? t : c; }
DI void dma4(rsrc_t r, unsigned voff, unsigned soff, unsigned lds) {
    asm volatile("s_mov_b32 m0, %0\n\ts_nop 0\n\tbuffer_load_dword %1, %2, %3 offen lds" ::"s"(lds), "v"(voff), "s"(r), "s"(soff) : "memory");
}
#define PF_CODE_LINES 168
DI void gdma4(const void* a, unsigned lds) {
    asm volatile("s_mov_b32 m0, %0\n\ts_nop 0\n\tglobal_load_lds_dword %1, off" ::"s"(lds), "v"(a) : "memory");
}
#define PF_STAG 0
#define PF_WQ_SEG 1
#define PF_WO_SEG 2
#define WAIT_VM(n) asm volatile("s_waitcnt vmcnt(" #n ")" ::: "memory")
#define WAIT_VM_LGKM_BAR(n) asm volatile("s_waitcnt vmcnt(" #n ") lgkmcnt(0)\n\ts_barrier" ::: "memory")
#define LGKM_BAR() asm volatile("s_waitcnt lgkmcnt(0)\n\ts_barrier" ::: "memory")

extern __shared__ __attribute__((aligned(16))) char smem_dyn[];
DI LAS char* lds_at(unsigned off) { return (LAS char*)(uintptr_t)off; }
DI bf16x8 lds_b128(unsigned a) { return *(const LAS bf16x8*)lds_at(a); }
DI s16x4 lds_tr(unsigned a) { return __builtin_bit_cast(s16x4, __builtin_amdgcn_ds_read_tr16_b64_v4i16((LAS v4i16_t*)lds_at(a))); }
DI bf16x8 tr_frag(unsigned a0, unsigned a1) { const s16x4 lo = lds_tr(a0), hi = lds_tr(a1); return __builtin_shufflevector(lo, hi, 0, 1, 2, 3, 4, 5, 6, 7); }
DI unsigned cvtpk(float lo, float hi) { const f32x2 v = {lo, hi}; return __builtin_bit_cast(unsigned, __builtin_convertvector(v, bf2_t)); }
DI bf16x8 pack8(const f32x16& x, int s) {
    const u32x4 p = {cvtpk(x[8 * s], x[8 * s + 1]), cvtpk(x[8 * s + 2], x[8 * s + 3]), cvtpk(x[8 * s + 4], x[8 * s + 5]), cvtpk(x[8 * s + 6], x[8 * s + 7])};
    return __builtin_bit_cast(bf16x8, p);
}
DI u64 uniform64(u64 v) { return (u64)rfl((unsigned)v) | ((u64)rfl((unsigned)(v >> 32)) << 32); }
DI float addf(float a, float b) { float r; asm("v_add_f32 %0, %1, %2" : "=v"(r) : "v"(a), "v"(b)); return r; }
DI float swap_lo(float v) { auto r = __builtin_amdgcn_permlane32_swap(__float_as_uint(v), __float_as_uint(v), false, false); return __uint_as_float(r[0]); }
DI float swap_hi(float v) { auto r = __builtin_amdgcn_permlane32_swap(__float_as_uint(v), __float_as_uint(v), false, false); return __uint_as_float(r[1]); }
DI float xhalf_max(float v) { auto r = __builtin_amdgcn_permlane32_swap(__float_as_uint(v), __float_as_uint(v), false, false); return __builtin_fmaxf(__uint_as_float(r[0]), __uint_as_float(r[1])); }
DI float xhalf_min(float v) { auto r = __builtin_amdgcn_permlane32_swap(__float_as_uint(v), __float_as_uint(v), false, false); return __builtin_fminf(__uint_as_float(r[0]), __uint_as_float(r[1])); }
DI float xhalf_sum(float v) { auto r = __builtin_amdgcn_permlane32_swap(__float_as_uint(v), __float_as_uint(v), false, false); return __uint_as_float(r[0]) + __uint_as_float(r[1]); }
constexpr float L2E = 1.4426950408889634f;
constexpr unsigned ONE2 = 0x3F803F80u;
constexpr unsigned SPIN_CAP = 1u << 16;

#define STAMP_DECL do { } while (0)
#define STAMP(i) do { } while (0)
#define STAMPX(i, x) do { } while (0)
#define STAMP_FLUSH() do { } while (0)

DI void row_extrema(const f32x16& a, const f32x16& b, float (&cp)[2], float (&cn)[2]) {
    float mxa = __builtin_fmaxf(a[0], a[1]), mna = __builtin_fminf(a[0], a[1]), mxb = __builtin_fmaxf(b[0], b[1]), mnb = __builtin_fminf(b[0], b[1]);
#pragma unroll
    for (int i = 2; i < 14; i += 2) {
        mxa = __builtin_fmaxf(__builtin_fmaxf(mxa, a[i]), a[i + 1]); mxb = __builtin_fmaxf(__builtin_fmaxf(mxb, b[i]), b[i + 1]);
        mna = __builtin_fminf(__builtin_fminf(mna, a[i]), a[i + 1]); mnb = __builtin_fminf(__builtin_fminf(mnb, b[i]), b[i + 1]);
    }
    cp[0] = -xhalf_max(mxa) * L2E; cp[1] = -xhalf_max(mxb) * L2E;
    cn[0] = xhalf_min(mna) * L2E; cn[1] = xhalf_min(mnb) * L2E;
}
DI void exp_pair(const f32x16& pp, float cp, float cn, f32x2 (&ep)[7], f32x2 (&en)[7]) {
    const f32x2 l2 = {L2E, L2E}, cp2 = {cp, cp}, cn2 = {cn, cn};
#pragma unroll
    for (int i = 0; i < 7; ++i) {
        const f32x2 v = {pp[2 * i], pp[2 * i + 1]};
        const f32x2 a = __builtin_elementwise_fma(v, l2, cp2), b = __builtin_elementwise_fma(v, -l2, cn2);
        ep[i] = (f32x2){__builtin_amdgcn_exp2f(a[0]), __builtin_amdgcn_exp2f(a[1])};
        en[i] = (f32x2){__builtin_amdgcn_exp2f(b[0]), __builtin_amdgcn_exp2f(b[1])};
    }
}
DI unsigned cvtpk2(f32x2 v) { return __builtin_bit_cast(unsigned, __builtin_convertvector(v, bf2_t)); }

#define POLL_SLEEP 1
#define POLL_DEPTH 1
DI void wait_flags(const GAS u64* f, u64 tag) {
#pragma unroll 1
    for (unsigned spins = 0; spins < SPIN_CAP; ++spins) {
        const u64 v = __hip_atomic_load(f, __ATOMIC_RELAXED, __HIP_MEMORY_SCOPE_AGENT);
        if (__builtin_amdgcn_ballot_w64(v != tag) == 0) break;
        __builtin_amdgcn_s_sleep(POLL_SLEEP);
    }
    asm volatile("" ::: "memory");
}

#define SB() __builtin_amdgcn_sched_barrier(0)
#define PINV(x) asm volatile("" : "+v"(x))
struct Frag { bf16x8 x0, x1, fk, fv; };

DI void build_aphi(bf16x8 (&aphi)[2], int r, int h2) {
    const int hr = (r >> 2) & 1, reg = (r & 3) + 4 * (r >> 3), f = 14 * hr + (reg < 14 ? reg : 0);
    const unsigned a = L_WPHI + 2 * (4 * h2 * DH + f);
#pragma unroll
    for (int s = 0; s < 2; ++s)
#pragma unroll
        for (int jj = 0; jj < 8; ++jj) {
            const int d0 = 16 * s + 8 * (jj >> 2) + (jj & 3);
            const bool ok = reg < 14 && (d0 + 4 < DH || h2 == 0);
            const unsigned short v = *(const LAS unsigned short*)lds_at(a + 2 * d0 * DH);
            aphi[s][jj] = ok ? (short)v : (short)0;
        }
}

__global__ void __launch_bounds__(512, 2) hedgehog_mix_kernel(Params p) {
    const int tid = threadIdx.x, lane = tid & 63;
    const unsigned w = rfl(tid >> 6), cw = w & 3;
    if (w == 4) {
        u64 pc; asm volatile("s_getpc_b64 %0" : "=s"(pc));
        const unsigned l0 = (blockIdx.x >> 3) * 6 + (lane < 6 ? lane : 5), l = l0 < PF_CODE_LINES ? l0 : 0;
        gdma4((const char*)(pc & ~127ull) + l * 128, L_SCR + 256);
    }
    const int r = lane & 31, h2 = lane >> 5, i16 = lane & 15, q4 = i16 >> 2, p4 = i16 & 3, blk16 = (lane >> 4) & 1;
    const unsigned xcd = blockIdx.x & 7, j = blockIdx.x >> 3;
    const unsigned tb = (xcd & 1) * 4 + (j & 3), bh = (xcd >> 1) * 8 + (j >> 2), br = bh >> 4, h = bh & 15;
    const unsigned og = xcd * 4 + j / 7, oc = j % 7;
    const bool has_out = j < 28;
#define ROT 1
    const unsigned rot = ROT == 1 ? ((j >> 2) + 2 * (j & 3)) % 7 : (ROT == 2 ? (2 * (j & 3)) % 7 : 0);
    auto seg = [&](int n) { const unsigned t = rot + n; return t >= 7 ? t - 7 : t; };
#define PIN_ARGS() asm volatile("" : "+s"(p.x), "+s"(p.wqkvA), "+s"(p.wqkvB), "+s"(p.wphiA), "+s"(p.wphiB), "+s"(p.woA), "+s"(p.woB), "+s"(p.gate), "+s"(p.out), "+s"(p.ws))
    PIN_ARGS();
    STAMP_DECL;
    STAMP(0);
    const rsrc_t rws = mk_rsrc(p.ws, WS_END);
    const f32x16 zero16 = {};
    const int cq = 4 * blk16 + p4;
    const unsigned rowpart = (8 * h2 + q4) * 64;
    const unsigned coloff = (8 * (h & 1) + 8 * cq) & 63;
    const unsigned laneX = cw * 8192 + r * 128 + 16 * (h2 ^ ((r >> 1) & 7));

    if (__builtin_expect(w >= 4, 1)) {
        const rsrc_t rx = mk_rsrc(p.x, S_ * XROW);
        const rsrc_t rw = mk_rsrc(br ? p.wqkvB : p.wqkvA, D_ * WROW);
        const unsigned vx0 = (lane >> 3) * XROW + 16 * ((lane & 7) ^ (lane >> 4));
        const unsigned vx1 = (lane >> 3) * XROW + 16 * ((lane & 7) ^ (4 + (lane >> 4)));
        const unsigned vw = (lane >> 2) * WROW + 16 * (lane & 3);
        const unsigned sx = (tb * 256 + cw * 64) * XROW;
        const unsigned sw = cw * 16 * WROW + h * 56 - 8 * (h & 1);
        auto issue = [&](int s) {
            dma_seg(rw, rx, vw, vx0, vx1, sw + seg(s) * (64 * WROW) + 896, sx + seg(s) * 128, slot_of(s) + SLOT_W + cw * 1024, slot_of(s) + cw * 8192);
        };
        bf16x8 xs[5][4][2];
        auto grab = [&](int s) {
#pragma unroll
            for (int ks = 0; ks < 4; ++ks) {
                const unsigned a = (laneX ^ (32 * ks)) + slot_of(s);
                xs[s][ks][0] = lds_b128(a); xs[s][ks][1] = lds_b128(a + 4096);
            }
        };
        STAMP(11);
        issue(0); issue(1);
        u64 ev;
        asm volatile("global_load_dwordx2 %0, %1, %2 sc1" : "=&v"(ev) : "v"(0u), "s"(p.ws) : "memory");
        STAMP(1);
        WAIT_VM_LGKM_BAR(11);
        STAMP(2);
        grab(0); issue(2);
        __builtin_amdgcn_raw_buffer_store_b128((u32x4){~0u, ~0u, ~0u, ~0u}, rws, lane * 16, WS_KVP + ((bh * 8 + tb) * 4 + cw) * 1024, 16);
        asm volatile("s_waitcnt vmcnt(11) lgkmcnt(0)\n\ts_barrier" : "+v"(ev) :: "memory");
        const u64 tag = uniform64(ev) + 1;
        grab(1); issue(3); WAIT_VM_LGKM_BAR(10);
        if (lane == 0) __hip_atomic_store((GAS u64*)((char*)p.ws + WS_F1 + (bh * 4 + cw) * F1_GRP + tb * FS), tag, __ATOMIC_RELAXED, __HIP_MEMORY_SCOPE_AGENT);
        grab(2); issue(4); WAIT_VM_LGKM_BAR(11);
        grab(3); issue(5); WAIT_VM_LGKM_BAR(10);
        grab(4); issue(6); WAIT_VM_LGKM_BAR(10);
        WAIT_VM_LGKM_BAR(0);
        STAMP(3);
        const GAS u64* const f1p = (const GAS u64*)((const char*)p.ws + WS_F1 + (bh * 4 + cw) * F1_GRP + (lane & 7) * FS);
        u64 f1v;
        asm volatile("global_load_dwordx2 %0, %1, off sc1" : "=&v"(f1v) : "v"(f1p) : "memory");
#pragma unroll
        for (int s = 0; s < 7; ++s) { const unsigned sg = seg((s + 5) % 7); dma16(rw, vw, sw + sg * (64 * WROW), L_WQ + sg * 4096 + cw * 1024); }
        bf16x8 aphi[2];
        build_aphi(aphi, r, h2);
        PINV(aphi[0]); PINV(aphi[1]);
        asm volatile("s_waitcnt vmcnt(7)" : "+v"(f1v) :: "memory");
        if (__builtin_amdgcn_ballot_w64(f1v != tag) != 0) wait_flags(f1p, tag);
        STAMP(4);
        WAIT_VM_LGKM_BAR(0);
        STAMP(5);
        f32x16 qT[2] = {zero16, zero16};
        {
            const unsigned aq_real = L_WQ + rowpart + coloff, aq_pad = L_GT + rowpart;
            bf16x8 fq[3], xl[2][2];
            auto seg_of = [](int n) { return n < 8 ? 5 + (n >> 2) : (n - 8) >> 2; };
            unsigned aQ = 0;
            auto rdq = [&](int n) {
                if ((n & 3) == 0) { const unsigned sg = seg(seg_of(n)); aQ = cq == 7 ? aq_pad + 8 * sg : aq_real + 4096 * sg; PINV(aQ); }
                fq[n % 3] = tr_frag(aQ + (n & 3) * 1024, aQ + (n & 3) * 1024 + 256);
            };
            auto rdx = [&](int n) { const unsigned a = (laneX ^ (32 * (n & 3))) + slot_of(seg_of(n)); xl[n & 1][0] = lds_b128(a); xl[n & 1][1] = lds_b128(a + 4096); };
            rdq(0); rdx(0); rdq(1);
            SB();
#pragma unroll
            for (int n = 0; n < 28; ++n) {
                const int s = seg_of(n), ks = n & 3;
                if (n + 1 < 8) rdx(n + 1);
                if (n + 2 < 28) rdq(n + 2);
                SB();
                qT[0] = MFMA(fq[n % 3], n >= 8 ? xs[n >= 8 ? s : 0][ks][0] : xl[n & 1][0], qT[0]);
                qT[1] = MFMA(fq[n % 3], n >= 8 ? xs[n >= 8 ? s : 0][ks][1] : xl[n & 1][1], qT[1]);
                SB();
                if (n == 7) {
                    asm volatile("s_waitcnt lgkmcnt(0)" ::: "memory");
                    *(volatile LAS unsigned*)lds_at(L_XFREE + 4 * cw) = 1u;
                }
            }
        }
        STAMP(6);
        bf16x8 eq[2][2][2];
        float gs[2];
        {
            f32x16 pq[2];
            const bf16x8 q00 = pack8(qT[0], 0), q10 = pack8(qT[1], 0);
            pq[0] = MFMA(aphi[0], q00, zero16); pq[1] = MFMA(aphi[0], q10, zero16);
            const bf16x8 q01 = pack8(qT[0], 1), q11 = pack8(qT[1], 1);
            pq[0] = MFMA(aphi[1], q01, pq[0]); pq[1] = MFMA(aphi[1], q11, pq[1]);
#pragma unroll
            for (int b = 0; b < 2; ++b) {
                const float lg = swap_hi(qT[b][12]);
                gs[b] = __builtin_amdgcn_rcpf(1.f + __builtin_amdgcn_exp2f(br ? lg * L2E : -lg * L2E));
                PINV(gs[b]);
            }
            float cp[2], cn[2];
            row_extrema(pq[0], pq[1], cp, cn);
            PINV(cp[0]); PINV(cp[1]); PINV(cn[0]); PINV(cn[1]);
#pragma unroll
            for (int b = 0; b < 2; ++b) {
                const f32x2 l2 = {L2E, L2E}, cp2 = {cp[b], cp[b]}, cn2 = {cn[b], cn[b]};
                unsigned pe[8], ne[8];
#pragma unroll
                for (int i = 0; i < 7; ++i) {
                    const f32x2 v = {pq[b][2 * i], pq[b][2 * i + 1]};
                    const f32x2 a = __builtin_elementwise_fma(v, l2, cp2), c = __builtin_elementwise_fma(v, -l2, cn2);
                    pe[i] = cvtpk2((f32x2){__builtin_amdgcn_exp2f(a[0]), __builtin_amdgcn_exp2f(a[1])});
                    ne[i] = cvtpk2((f32x2){__builtin_amdgcn_exp2f(c[0]), __builtin_amdgcn_exp2f(c[1])});
                }
                pe[7] = 0; ne[7] = 0;
                eq[b][0][0] = __builtin_bit_cast(bf16x8, (u32x4){pe[0], pe[1], pe[2], pe[3]}); eq[b][0][1] = __builtin_bit_cast(bf16x8, (u32x4){pe[4], pe[5], pe[6], pe[7]});
                eq[b][1][0] = __builtin_bit_cast(bf16x8, (u32x4){ne[0], ne[1], ne[2], ne[3]}); eq[b][1][1] = __builtin_bit_cast(bf16x8, (u32x4){ne[4], ne[5], ne[6], ne[7]});
            }
        }
#pragma unroll
        for (int b = 0; b < 2; ++b) asm volatile("" : "+v"(eq[b][0][0]), "+v"(eq[b][0][1]), "+v"(eq[b][1][0]), "+v"(eq[b][1][1]), "+v"(gs[b]));
        STAMP(7);
        LGKM_BAR();
        __builtin_amdgcn_s_setprio(3);
        STAMP(8);
        {
            bf16x8 akv[4];
#pragma unroll
            for (int f = 0; f < 4; ++f) akv[f] = lds_b128(L_KVA + f * 1024 + lane * 16);
#pragma unroll
            for (int b = 0; b < 2; ++b) {
                f32x16 np = MFMA(akv[0], eq[b][0][0], zero16); np = MFMA(akv[1], eq[b][0][1], np);
                f32x16 nn = MFMA(akv[2], eq[b][1][0], zero16); nn = MFMA(akv[3], eq[b][1][1], nn);
                const float ia = __builtin_amdgcn_rcpf(np[15]), ib = __builtin_amdgcn_rcpf(nn[15]);
                const float den = np[14] * ia + nn[14] * ib + 1e-6f;
                const float sc = gs[b] * __builtin_amdgcn_rcpf(den);
                const float ca = ia * sc, cb = ib * sc;
                float o[14];
#pragma unroll
                for (int i = 0; i < 14; ++i) o[i] = np[i] * ca + nn[i] * cb;
                const u32x4 o0 = {cvtpk(o[0], o[1]), cvtpk(o[2], o[3]), cvtpk(o[4], o[5]), cvtpk(o[6], o[7])};
                const u32x4 o1 = {cvtpk(o[8], o[9]), cvtpk(o[10], o[11]), cvtpk(o[12], o[13]), 0u};
                const unsigned so = WS_O + (((tb * 4 + cw) * 32 + bh) * 4 + b * 2) * 1024;
                __builtin_amdgcn_raw_buffer_store_b128(o0, rws, lane * 16, so, 16);
                __builtin_amdgcn_raw_buffer_store_b128(o1, rws, lane * 16, so + 1024, 16);
            }
        }
        STAMP(9);
        WAIT_VM(0);
        if (lane == 0) __hip_atomic_store((GAS u64*)((char*)p.ws + WS_F2 + ((tb * 4 + cw) * 2 + br) * F2_GRP + h * FS), tag, __ATOMIC_RELAXED, __HIP_MEMORY_SCOPE_AGENT);
        STAMP(10);
        STAMP_FLUSH();
        return;
    }

    u64 tag;
    {
        const rsrc_t rg = mk_rsrc(p.gate, XROW);
        const rsrc_t rp = mk_rsrc(br ? p.wphiB : p.wphiA, 16 * DH * DH * 2);
        unsigned g0, g1; u32x2 wp;
        u64 ev;
        asm volatile("global_load_dwordx2 %0, %1, %2 sc1" : "=&v"(ev) : "v"(0u), "s"(p.ws) : "memory");
        asm volatile("buffer_load_ushort %0, %3, %4, 0 offen\n\tbuffer_load_ushort %1, %3, %4, 0 offen offset:512\n\tbuffer_load_dwordx2 %2, %5, %6, %7 offen"
                     : "=&v"(g0), "=&v"(g1), "=&v"(wp) : "v"(tid * 2), "s"(rg), "v"(tid * 8), "s"(rp), "s"(h * (DH * DH * 2)) : "memory");
        asm volatile("s_waitcnt vmcnt(0) lgkmcnt(0)" : "+v"(g0), "+v"(g1), "+v"(wp), "+v"(ev) :: "memory");
        tag = uniform64(ev) + 1;
        *(LAS u32x2*)lds_at(L_GT + (tid & 63) * 64 + (tid >> 6) * 8) = (u32x2){g0 & 0xffffu, 0u};
        *(LAS u32x2*)lds_at(L_GT + (tid & 63) * 64 + (4 + (tid >> 6)) * 8) = (u32x2){g1 & 0xffffu, 0u};
        *(LAS u32x2*)lds_at(L_WPHI + tid * 8) = wp;
        if (tid < 5) *(volatile LAS unsigned*)lds_at(L_XFREE + 4 * tid) = 0u;
    }
    __builtin_amdgcn_s_setprio(2);
    LGKM_BAR();
    STAMP(2);
    bf16x8 aphi[2];
    build_aphi(aphi, r, h2);
    PINV(aphi[0]); PINV(aphi[1]);
    const unsigned laneW = SLOT_W + rowpart + coloff;
    f32x16 vinit;
#pragma unroll
    for (int i = 0; i < 16; ++i) vinit[i] = r == 28 ? 1.f : 0.f;
    PINV(vinit);
    f32x16 kT[2] = {zero16, zero16}, vv[2] = {vinit, vinit};
    {
        unsigned aX[4], aW, aV;
        auto addr = [&](int s) {
#pragma unroll
            for (int ks = 0; ks < 4; ++ks) { aX[ks] = (laneX ^ (32 * ks)) + slot_of(s); PINV(aX[ks]); }
            aW = laneW + slot_of(s); PINV(aW);
            aV = cq == 7 ? L_GT + rowpart + 56 : aW + 4096; PINV(aV);
        };
        Frag F[2];
#define RD_X(f, ks) do { f.x0 = lds_b128(aX[ks]); } while (0)
#define RD_X1(f, ks) do { f.x1 = lds_b128(aX[ks] + 4096); } while (0)
#define RD_K(f, ks) do { f.fk = tr_frag(aW + ks * 1024, aW + ks * 1024 + 256); } while (0)
#define RD_V(f, ks) do { f.fv = tr_frag(aV + ks * 1024, aV + ks * 1024 + 256); } while (0)
        addr(0);
        RD_X(F[0], 0); RD_K(F[0], 0); RD_X1(F[0], 0); RD_V(F[0], 0); SB();
#pragma unroll
        for (int n = 0; n < 28; ++n) {
            Frag& c = F[n & 1];
            Frag& x = F[(n + 1) & 1];
            const int nks = (n + 1) & 3;
            const bool more = n + 1 < 28;
            kT[0] = MFMA(c.fk, c.x0, kT[0]); SB();
            if (more && nks == 0) { LGKM_BAR(); addr((n + 1) >> 2); SB(); }
            if (more) { RD_X(x, nks); RD_K(x, nks); } SB();
            kT[1] = MFMA(c.fk, c.x1, kT[1]); SB();
            if (more) { RD_X1(x, nks); } SB();
            vv[0] = MFMA(c.x0, c.fv, vv[0]); SB();
            if (more) { RD_V(x, nks); } SB();
            vv[1] = MFMA(c.x1, c.fv, vv[1]); SB();
        }
    }
    STAMP(3);

    const unsigned phiW = L_PHI + cw * 8192;
    f32x16 pk[2];
    {
        const bf16x8 k00 = pack8(kT[0], 0), k10 = pack8(kT[1], 0);
        pk[0] = MFMA(aphi[0], k00, zero16); pk[1] = MFMA(aphi[0], k10, zero16);
        const bf16x8 k01 = pack8(kT[0], 1), k11 = pack8(kT[1], 1);
        pk[0] = MFMA(aphi[1], k01, pk[0]); pk[1] = MFMA(aphi[1], k11, pk[1]);
    }
    bf16x8 vp[2][2];
#pragma unroll
    for (int b = 0; b < 2; ++b)
#pragma unroll
        for (int s = 0; s < 2; ++s) { vp[b][s] = pack8(vv[b], s); PINV(vp[b][s]); }
    SB();
    float cp[2], cn[2];
    row_extrema(pk[0], pk[1], cp, cn);
    const unsigned pt0 = 4 * h2 + q4;
    const unsigned pa0 = phiW + pt0 * 64 + 8 * (cq ^ ((pt0 >> 1) & 7));
    const unsigned pa1 = (pa0 ^ 32) + 8 * 64;
    f32x16 kvp = zero16, kvn = zero16;
#pragma unroll
    for (int b = 0; b < 2; ++b) {
        f32x2 ep[7], en[7];
        exp_pair(pk[b], cp[b], cn[b], ep, en);
        f32x2 sp = ep[0], sn = en[0];
#pragma unroll
        for (int i = 1; i < 7; ++i) { sp += ep[i]; sn += en[i]; }
        const float ip = __builtin_amdgcn_rcpf(xhalf_sum(sp[0] + sp[1])), in = __builtin_amdgcn_rcpf(xhalf_sum(sn[0] + sn[1]));
        const f32x2 ip2 = {ip, ip}, in2 = {in, in};
        const unsigned base = phiW + b * 4096 + r * 64;
#pragma unroll
        for (int g = 0; g < 4; ++g) {
            const unsigned a = base + 8 * ((2 * g + h2) ^ ((r >> 1) & 7));
            const u32x2 dp = {cvtpk2(ep[2 * g] * ip2), g < 3 ? cvtpk2(ep[(2 * g + 1) % 7] * ip2) : 0u};
            const u32x2 dn = {cvtpk2(en[2 * g] * in2), g < 3 ? cvtpk2(en[(2 * g + 1) % 7] * in2) : 0u};
            *(LAS u32x2*)lds_at(a) = dp;
            *(LAS u32x2*)lds_at(a + 2048) = dn;
        }
#pragma unroll
        for (int s = 0; s < 2; ++s) {
            const unsigned o = b * 4096 + s * 1024;
            kvp = MFMA(tr_frag(pa0 + o, pa1 + o), vp[b][s], kvp);
            kvn = MFMA(tr_frag(pa0 + o + 2048, pa1 + o + 2048), vp[b][s], kvn);
        }
    }
    asm volatile("s_waitcnt lgkmcnt(0)" ::: "memory");
#pragma unroll
    for (int f = 0; f < 4; ++f)
#pragma unroll
        for (int hh = 0; hh < 2; ++hh) {
            const f32x16& t = f < 2 ? kvp : kvn;
            const int o = 8 * (f & 1) + 4 * hh;
            *(LAS f32x4*)lds_at(phiW + f * 2048 + hh * 1024 + lane * 16) = (f32x4){t[o], t[o + 1], t[o + 2], t[o + 3]};
        }
    STAMP(4);
    LGKM_BAR();
    STAMP(5);
    {
        f32x4 t[4][2];
#pragma unroll
        for (int u = 0; u < 4; ++u) {
            t[u][0] = *(const LAS f32x4*)lds_at(L_PHI + u * 8192 + cw * 2048 + lane * 16);
            t[u][1] = *(const LAS f32x4*)lds_at(L_PHI + u * 8192 + cw * 2048 + 1024 + lane * 16);
        }
        SB();
        float a[8];
#pragma unroll
        for (int i = 0; i < 8; ++i) {
            float x01 = addf(t[0][i >> 2][i & 3], t[1][i >> 2][i & 3]), x23 = addf(t[2][i >> 2][i & 3], t[3][i >> 2][i & 3]);
            a[i] = addf(x01, x23);
        }
        u32x4 o = {cvtpk(a[0], a[1]), cvtpk(a[2], a[3]), cvtpk(a[4], a[5]), cvtpk(a[6], a[7])};
        STAMPX(1, o);
        __builtin_amdgcn_raw_buffer_store_b128(o, rws, lane * 16, WS_KVP + ((bh * 8 + tb) * 4 + cw) * 1024, 16);
    }
    bf16x8 sel;
    {
        const int jj = (r & 3) + 4 * (r >> 3);
        const bool on = r < 16 && ((r >> 2) & 1) == h2;
        const unsigned one = on ? (0x3F80u << (16 * (jj & 1))) : 0u;
        sel = __builtin_bit_cast(bf16x8, (u32x4){(jj >> 1) == 0 ? one : 0u, (jj >> 1) == 1 ? one : 0u, (jj >> 1) == 2 ? one : 0u, (jj >> 1) == 3 ? one : 0u});
        PINV(sel);
    }
    {
        const rsrc_t roA = mk_rsrc(p.woA, D_ * XROW), roB = mk_rsrc(p.woB, D_ * XROW);
        const unsigned vwo = (lane >> 3) * XROW + 16 * ((lane & 7) ^ (4 * ((lane >> 4) & 1)));
        dma_rows<2>(roA, vwo, (256 + 16 * cw) * XROW + oc * 128, L_WO + (256 + 16 * cw) * 128);
        dma_rows<2>(roB, vwo, (576 - 448 + 16 * cw) * XROW + oc * 128, L_WO + (576 + 16 * cw) * 128);
#pragma unroll 1
        for (unsigned spins = 0; spins < SPIN_CAP; ++spins) { if (*(volatile LAS unsigned*)lds_at(L_XFREE + 4 * cw) != 0u) break; }
        dma_rows<6>(roA, vwo, 64 * cw * XROW + oc * 128, L_WO + 64 * cw * 128);
        const rsrc_t roC = cw < 2 ? roA : roB;
        const unsigned R = 320 + 64 * cw, Rg = cw < 2 ? R : R - 448;
        const int hr = (r >> 2) & 1, reg = (r & 3) + 4 * (r >> 3);
        const int col = reg < 14 ? 14 * hr + reg : 28;
        const unsigned vo = (h2 * 32 + col) * 16;
        u32x4 part[8];
        STAMP(9);
#pragma unroll
        for (int u = 0; u < 8; ++u) part[u] = load_sc1_asm(rws, vo, WS_KVP + ((bh * 8 + u) * 4 + cw) * 1024);
        dma_rows<2>(roA, vwo, (64 * cw + 48) * XROW + oc * 128, L_WO + (64 * cw + 48) * 128);
        dma_rows<8>(roC, vwo, Rg * XROW + oc * 128, L_WO + R * 128);
        asm volatile("s_waitcnt vmcnt(10)" : "+v"(part[0]), "+v"(part[1]), "+v"(part[2]), "+v"(part[3]), "+v"(part[4]), "+v"(part[5]), "+v"(part[6]), "+v"(part[7]) :: "memory");
        STAMP(6);
#pragma unroll 1
        for (unsigned spins = 0; spins < SPIN_CAP; ++spins) {
            unsigned m = 0;
#pragma unroll
            for (int u = 0; u < 8; ++u) { m = umax3(m, part[u][0], part[u][1]); m = umax3(m, part[u][2], part[u][3]); }
            if (__builtin_amdgcn_ballot_w64(m == ~0u) == 0) break;
#pragma unroll
            for (int u = 0; u < 8; ++u) part[u] = load_sc1_asm(rws, vo, WS_KVP + ((bh * 8 + u) * 4 + cw) * 1024);
            asm volatile("s_waitcnt vmcnt(0)" : "+v"(part[0]), "+v"(part[1]), "+v"(part[2]), "+v"(part[3]), "+v"(part[4]), "+v"(part[5]), "+v"(part[6]), "+v"(part[7]) :: "memory");
        }
        STAMP(7);
        SB();
        f32x16 acc = zero16;
#pragma unroll
        for (int u = 0; u < 8; ++u) acc = MFMA(sel, __builtin_bit_cast(bf16x8, part[u]), acc);
        u32x4 o = __builtin_bit_cast(u32x4, pack8(acc, 0));
        if (reg == 15) o = (u32x4){ONE2, ONE2, ONE2, ONE2};
        *(LAS u32x4*)lds_at(L_KVA + cw * 1024 + lane * 16) = o;
    }
    LGKM_BAR();
    STAMP(8);
    if (!has_out) { STAMP_FLUSH(); return; }
    {
        const rsrc_t ro = mk_rsrc(p.woB, D_ * XROW);
        const unsigned vo = (lane >> 3) * XROW + 16 * ((lane & 7) ^ (4 * ((lane >> 4) & 1)));
        dma_rows<8>(ro, vo, (640 - 448 + 64 * cw) * XROW + oc * 128, L_WO + (640 + 64 * cw) * 128);
    }
    WAIT_VM(0);
    if (lane == 0) __hip_atomic_fetch_add((LAS unsigned*)lds_at(L_WOCNT), 1u, __ATOMIC_RELAXED, __HIP_MEMORY_SCOPE_WORKGROUP);
#pragma unroll 1
    for (unsigned spins = 0; spins < SPIN_CAP; ++spins) { if (*(volatile LAS unsigned*)lds_at(L_WOCNT) >= 4u) break; }
    asm volatile("" ::: "memory");
    STAMP(10);

    const unsigned tk = cw & 1, kh = cw >> 1;
    constexpr u64 HORD = 0xDB9642FECA875310ull;
    const GAS u64* f2 = (const GAS u64*)((const char*)p.ws + WS_F2 + (og * 2 + kh) * F2_GRP);
    f32x16 mine = zero16, other = zero16;
    {
        const unsigned colp = 32 * blk16 + 8 * p4;
        const unsigned r0 = kh * 448 + 14 * h2 + q4, r1 = kh * 448 + 14 * h2 + (q4 < 1 ? q4 : 1);
        unsigned a0 = L_WO + r0 * 128 + 64 * (kh ^ ((r0 >> 1) & 1)) + colp;
        unsigned a1 = L_WO + r1 * 128 + 64 * (kh ^ ((r1 >> 1) & 1)) + colp;
        unsigned a2 = a0 ^ 64, a3 = a1 ^ 64;
        asm volatile("" : "+v"(a0), "+v"(a1), "+v"(a2), "+v"(a3));
        u32x4 of[16][2];
        auto ld = [&](int i) {
            const unsigned hd = (unsigned)(HORD >> (4 * i)) & 15u;
#pragma unroll
            for (int s = 0; s < 2; ++s)
                of[i][s] = __builtin_amdgcn_raw_buffer_load_b128(rws, lane * 16, WS_O + (((og * 32 + kh * 16 + hd) * 2 + tk) * 2 + s) * 1024, 16);
        };
        bf16x8 A[2][4];
        auto rd = [&](bf16x8 (&f)[4], int i) {
            const unsigned o = ((unsigned)(HORD >> (4 * i)) & 15u) * 28 * 128;
            f[0] = tr_frag(a0 + o, a0 + o + 512); f[1] = tr_frag(a2 + o, a2 + o + 512);
            f[2] = tr_frag(a0 + o + 1024, a1 + o + 1536); f[3] = tr_frag(a2 + o + 1024, a3 + o + 1536);
        };
        auto step = [&](int i) {
            if (i + 1 < 16) rd(A[(i + 1) & 1], i + 1);
            SB();
            const bf16x8 b0 = __builtin_bit_cast(bf16x8, of[i][0]), b1 = __builtin_bit_cast(bf16x8, of[i][1]);
            mine = MFMA(A[i & 1][0], b0, mine); other = MFMA(A[i & 1][1], b0, other);
            mine = MFMA(A[i & 1][2], b1, mine); other = MFMA(A[i & 1][3], b1, other);
            SB();
        };
        wait_flags(f2 + (FS / 8) * ((HORD >> (4 * (lane % 10))) & 15), tag);
        STAMP(14);
#pragma unroll
        for (int i = 0; i < 10; ++i) ld(i);
        SB();
        rd(A[0], 0); SB();
#pragma unroll
        for (int i = 0; i < 10; ++i) step(i);
        STAMP(15);
        wait_flags(f2 + (FS / 8) * ((HORD >> (4 * (10 + lane % 6))) & 15), tag);
        STAMP(11);
#pragma unroll
        for (int i = 10; i < 16; ++i) ld(i);
        SB();
#pragma unroll
        for (int i = 10; i < 16; ++i) step(i);
    }
    STAMP(12);
#pragma unroll
    for (int g = 0; g < 4; ++g)
        *(LAS f32x4*)lds_at(L_PHI + cw * 4096 + g * 1024 + lane * 16) = (f32x4){other[4 * g], other[4 * g + 1], other[4 * g + 2], other[4 * g + 3]};
    LGKM_BAR();
    if (blockIdx.x == 0 && tid == 0) __hip_atomic_store((GAS u64*)((char*)p.ws + WS_EPOCH), tag, __ATOMIC_RELAXED, __HIP_MEMORY_SCOPE_AGENT);
    {
        unsigned d[8];
#pragma unroll
        for (int g = 0; g < 4; ++g) {
            const f32x4 t = *(const LAS f32x4*)lds_at(L_PHI + (cw ^ 2) * 4096 + g * 1024 + lane * 16);
            d[2 * g] = cvtpk(mine[4 * g] + t[0], mine[4 * g + 1] + t[1]);
            d[2 * g + 1] = cvtpk(mine[4 * g + 2] + t[2], mine[4 * g + 3] + t[3]);
        }
        const rsrc_t rout = mk_rsrc(p.out, S_ * XROW);
        const unsigned vo = r * XROW + h2 * 16, so = (og * 64 + tk * 32) * XROW + oc * 128 + kh * 64;
#pragma unroll
        for (int k = 0; k < 4; k += 2) {
            auto sx0 = __builtin_amdgcn_permlane32_swap(d[2 * k], d[2 * k + 2], false, false);
            auto sx1 = __builtin_amdgcn_permlane32_swap(d[2 * k + 1], d[2 * k + 3], false, false);
            const u32x4 o = {sx0[0], sx1[0], sx0[1], sx1[1]};
            __builtin_amdgcn_raw_buffer_store_b128(o, rout, vo, so + 16 * k, 0);
        }
    }
    STAMP(13);
    STAMP_FLUSH();
}

extern "C" void kernel_launch(void* const* d_in, const int* in_sizes, int n_in, void* d_out, int out_size, void* d_ws, size_t ws_size,
                              hipStream_t stream) {
    Params p;
    p.x = d_in[0]; p.wqkvA = d_in[1]; p.wphiA = d_in[2]; p.woA = d_in[3];
    p.wqkvB = d_in[5]; p.wphiB = d_in[6]; p.woB = d_in[7]; p.gate = d_in[8];
    p.out = d_out; p.ws = d_ws;
    (void)hipFuncSetAttribute((const void*)hedgehog_mix_kernel, hipFuncAttributeMaxDynamicSharedMemorySize, L_TOTAL);
    hedgehog_mix_kernel<<<256, 512, L_TOTAL, stream>>>(p);
}
```

```cpp
#define PREFETCH 0
#define PF_CODE 1
#define ROT 1
#include <hip/hip_runtime.h>
#include <stdint.h>

typedef short bf16x8 __attribute__((ext_vector_type(8)));
typedef short s16x4 __attribute__((ext_vector_type(4)));
typedef short v4i16_t __attribute__((ext_vector_type(4)));
typedef __bf16 bf2_t __attribute__((ext_vector_type(2)));
typedef float f32x2 __attribute__((ext_vector_type(2)));
typedef float f32x4 __attribute__((ext_vector_type(4)));
typedef float f32x16 __attribute__((ext_vector_type(16)));
typedef unsigned u32x2 __attribute__((ext_vector_type(2)));
typedef unsigned u32x4 __attribute__((ext_vector_type(4)));
typedef unsigned long long u64;
#define LAS __attribute__((address_space(3)))
#define GAS __attribute__((address_space(1)))
#define DI __device__ __forceinline__
#define MFMA(a, b, c) __builtin_amdgcn_mfma_f32_32x32x16_bf16((a), (b), (c), 0, 0, 0)

constexpr int S_ = 2048, D_ = 448, DH = 28;
constexpr unsigned XROW = D_ * 2;
constexpr unsigned WROW = 3 * D_ * 2;
constexpr unsigned WS_EPOCH = 0;
#define FS 128
#define PZ_STORE 1
#define PZ_FLAG 9
#define SPLITK 0
#define WO2_BY_H 1
#define STAGES 3
#define DATAPOLL 1
constexpr unsigned F1_GRP = FS * 8 < 128 ? 128 : FS * 8, F2_GRP = FS * 16;
constexpr unsigned WS_F1 = 4096;
constexpr unsigned WS_F2 = WS_F1 + 32 * 4 * 1024;
constexpr unsigned WS_KVP = WS_F2 + 64 * 2048;
constexpr unsigned WS_O = WS_KVP + 32 * 8 * 4 * 1024;
constexpr unsigned WS_END = WS_O + 32 * 32 * 4 * 1024;
constexpr unsigned WS_STAMP = 8u << 20;
constexpr unsigned SLOT = 40960, SLOT_W = 32768;
constexpr unsigned L_B = 0, L_A = SLOT, L_C = 2 * SLOT;
constexpr unsigned L_WQ = 3 * SLOT;
constexpr unsigned L_GT = L_WQ + 448 * 64;
constexpr unsigned L_WPHI = L_GT + 4096;
constexpr unsigned L_SCR = L_WPHI + 2048;
constexpr unsigned L_XFREE = L_SCR + 512;
constexpr unsigned L_WOCNT = L_XFREE + 16;
constexpr unsigned L_REDCNT = L_XFREE + 20;
constexpr unsigned L_WQCNT = L_XFREE + 24;
constexpr unsigned L_K1F = L_XFREE + 32;
constexpr unsigned L_PHI1F = L_XFREE + 48;
constexpr unsigned L_PHI = L_B;
constexpr unsigned L_KVA = L_B + SLOT_W;
constexpr unsigned L_WO = L_A;
constexpr unsigned L_TOTAL = 163840;
static_assert(L_WPHI + 2048 <= L_TOTAL, "lds");
static_assert(L_WO + 896 * 128 == L_WPHI, "lds");
__host__ __device__ constexpr unsigned slot_of(int s) { return s % 3 == 0 ? L_A : (s % 3 == 1 ? L_B : L_C); }

struct Params {
    const void* x; const void* wqkvA; const void* wqkvB; const void* wphiA; const void* wphiB;
    const void* woA; const void* woB; const void* gate; void* out; void* ws;
};

typedef __amdgpu_buffer_rsrc_t rsrc_t;
DI rsrc_t mk_rsrc(const void* p, unsigned bytes) { return __builtin_amdgcn_make_buffer_rsrc((void*)p, (short)0, (int)bytes, 0x00020000); }
DI unsigned rfl(unsigned v) { return (unsigned)__builtin_amdgcn_readfirstlane((int)v); }
DI void dma16(rsrc_t r, unsigned voff, unsigned soff, unsigned lds) {
    asm volatile("s_mov_b32 m0, %0\n\ts_nop 0\n\tbuffer_load_dwordx4 %1, %2, %3 offen lds" ::"s"(lds), "v"(voff), "s"(r), "s"(soff) : "memory");
}
DI void dma_seg(rsrc_t rw, rsrc_t rx, unsigned vw, unsigned vx0, unsigned vx1, unsigned sw, unsigned sx, unsigned mw, unsigned mx) {
#define XP(v) "s_addk_i32 m0, 0x400\n\ts_addk_i32 %1, 0x1c00\n\tbuffer_load_dwordx4 " v ", %6, %1 offen lds\n\t"
    asm volatile("s_mov_b32 m0, %7\n\ts_nop 0\n\tbuffer_load_dwordx4 %2, %5, %0 offen lds\n\t"
                 "s_addk_i32 m0, 0x1000\n\ts_addk_i32 %0, 0x380\n\tbuffer_load_dwordx4 %2, %5, %0 offen lds\n\t"
                 "s_mov_b32 m0, %8\n\ts_nop 0\n\tbuffer_load_dwordx4 %3, %6, %1 offen lds\n\t"
                 XP("%4") XP("%3") XP("%4") XP("%3") XP("%4") XP("%3") XP("%4")
                 : "+s"(sw), "+s"(sx) : "v"(vw), "v"(vx0), "v"(vx1), "s"(rw), "s"(rx), "s"(mw), "s"(mx) : "scc", "memory");
#undef XP
}
template <int N> DI void dma_rows(rsrc_t r, unsigned v, unsigned so, unsigned m) {
    static_assert(N == 2 || N == 6 || N == 8, "");
#define RP "s_addk_i32 m0, 0x400\n\ts_addk_i32 %0, 0x1c00\n\tbuffer_load_dwordx4 %1, %2, %0 offen lds\n\t"
#define R0 "s_mov_b32 m0, %3\n\ts_nop 0\n\tbuffer_load_dwordx4 %1, %2, %0 offen lds\n\t"
    if (N == 2) asm volatile(R0 RP : "+s"(so) : "v"(v), "s"(r), "s"(m) : "scc", "memory");
    if (N == 6) asm volatile(R0 RP RP RP RP RP : "+s"(so) : "v"(v), "s"(r), "s"(m) : "scc", "memory");
    if (N == 8) asm volatile(R0 RP RP RP RP RP RP RP : "+s"(so) : "v"(v), "s"(r), "s"(m) : "scc", "memory");
#undef RP
#undef R0
}
DI u32x4 load_sc1_asm(rsrc_t r, unsigned voff, unsigned soff) {
    u32x4 v; asm volatile("buffer_load_dwordx4 %0, %1, %2, %3 offen sc1" : "=&v"(v) : "v"(voff), "s"(r), "s"(soff) : "memory"); return v;
}
DI unsigned umax3(unsigned a, unsigned b, unsigned c) { const unsigned t = a > b ? a : b; return t > c ? t : c; }
DI void dma4(rsrc_t r, unsigned voff, unsigned soff, unsigned lds) {
    asm volatile("s_mov_b32 m0, %0\n\ts_nop 0\n\tbuffer_load_dword %1, %2, %3 offen lds" ::"s"(lds), "v"(voff), "s"(r), "s"(soff) : "memory");
}
#define PF_CODE_LINES 168
DI void gdma4(const void* a, unsigned lds) {
    asm volatile("s_mov_b32 m0, %0\n\ts_nop 0\n\tglobal_load_lds_dword %1, off" ::"s"(lds), "v"(a) : "memory");
}
#define PF_STAG 0
#define PF_WQ_SEG 1
#define PF_WO_SEG 2
#define WAIT_VM(n) asm volatile("s_waitcnt vmcnt(" #n ")" ::: "memory")
#define WAIT_VM_LGKM_BAR(n) asm volatile("s_waitcnt vmcnt(" #n ") lgkmcnt(0)\n\ts_barrier" ::: "memory")
#define LGKM_BAR() asm volatile("s_waitcnt lgkmcnt(0)\n\ts_barrier" ::: "memory")

extern __shared__ __attribute__((aligned(16))) char smem_dyn[];
DI LAS char* lds_at(unsigned off) { return (LAS char*)(uintptr_t)off; }
DI bf16x8 lds_b128(unsigned a) { return *(const LAS bf16x8*)lds_at(a); }
DI s16x4 lds_tr(unsigned a) { return __builtin_bit_cast(s16x4, __builtin_amdgcn_ds_read_tr16_b64_v4i16((LAS v4i16_t*)lds_at(a))); }
DI bf16x8 tr_frag(unsigned a0, unsigned a1) { const s16x4 lo = lds_tr(a0), hi = lds_tr(a1); return __builtin_shufflevector(lo, hi, 0, 1, 2, 3, 4, 5, 6, 7); }
DI unsigned cvtpk(float lo, float hi) { const f32x2 v = {lo, hi}; return __builtin_bit_cast(unsigned, __builtin_convertvector(v, bf2_t)); }
DI bf16x8 pack8(const f32x16& x, int s) {
    const u32x4 p = {cvtpk(x[8 * s], x[8 * s + 1]), cvtpk(x[8 * s + 2], x[8 * s + 3]), cvtpk(x[8 * s + 4], x[8 * s + 5]), cvtpk(x[8 * s + 6], x[8 * s + 7])};
    return __builtin_bit_cast(bf16x8, p);
}
DI u64 uniform64(u64 v) { return (u64)rfl((unsigned)v) | ((u64)rfl((unsigned)(v >> 32)) << 32); }
DI float addf(float a, float b) { float r; asm("v_add_f32 %0, %1, %2" : "=v"(r) : "v"(a), "v"(b)); return r; }
DI float swap_lo(float v) { auto r = __builtin_amdgcn_permlane32_swap(__float_as_uint(v), __float_as_uint(v), false, false); return __uint_as_float(r[0]); }
DI float swap_hi(float v) { auto r = __builtin_amdgcn_permlane32_swap(__float_as_uint(v), __float_as_uint(v), false, false); return __uint_as_float(r[1]); }
DI float xhalf_max(float v) { auto r = __builtin_amdgcn_permlane32_swap(__float_as_uint(v), __float_as_uint(v), false, false); return __builtin_fmaxf(__uint_as_float(r[0]), __uint_as_float(r[1])); }
DI float xhalf_min(float v) { auto r = __builtin_amdgcn_permlane32_swap(__float_as_uint(v), __float_as_uint(v), false, false); return __builtin_fminf(__uint_as_float(r[0]), __uint_as_float(r[1])); }
DI float xhalf_sum(float v) { auto r = __builtin_amdgcn_permlane32_swap(__float_as_uint(v), __float_as_uint(v), false, false); return __uint_as_float(r[0]) + __uint_as_float(r[1]); }
constexpr float L2E = 1.4426950408889634f;
constexpr unsigned ONE2 = 0x3F803F80u;
constexpr unsigned SPIN_CAP = 1u << 16;

#define STAMP_DECL do { } while (0)
#define STAMP(i) do { } while (0)
#define STAMPX(i, x) do { } while (0)
#define STAMP_FLUSH() do { } while (0)

DI void row_extrema(const f32x16& a, const f32x16& b, float (&cp)[2], float (&cn)[2]) {
    float mxa = __builtin_fmaxf(a[0], a[1]), mna = __builtin_fminf(a[0], a[1]), mxb = __builtin_fmaxf(b[0], b[1]), mnb = __builtin_fminf(b[0], b[1]);
#pragma unroll
    for (int i = 2; i < 14; i += 2) {
        mxa = __builtin_fmaxf(__builtin_fmaxf(mxa, a[i]), a[i + 1]); mxb = __builtin_fmaxf(__builtin_fmaxf(mxb, b[i]), b[i + 1]);
        mna = __builtin_fminf(__builtin_fminf(mna, a[i]), a[i + 1]); mnb = __builtin_fminf(__builtin_fminf(mnb, b[i]), b[i + 1]);
    }
    cp[0] = -xhalf_max(mxa) * L2E; cp[1] = -xhalf_max(mxb) * L2E;
    cn[0] = xhalf_min(mna) * L2E; cn[1] = xhalf_min(mnb) * L2E;
}
DI void exp_pair(const f32x16& pp, float cp, float cn, f32x2 (&ep)[7], f32x2 (&en)[7]) {
    const f32x2 l2 = {L2E, L2E}, cp2 = {cp, cp}, cn2 = {cn, cn};
#pragma unroll
    for (int i = 0; i < 7; ++i) {
        const f32x2 v = {pp[2 * i], pp[2 * i + 1]};
        const f32x2 a = __builtin_elementwise_fma(v, l2, cp2), b = __builtin_elementwise_fma(v, -l2, cn2);
        ep[i] = (f32x2){__builtin_amdgcn_exp2f(a[0]), __builtin_amdgcn_exp2f(a[1])};
        en[i] = (f32x2){__builtin_amdgcn_exp2f(b[0]), __builtin_amdgcn_exp2f(b[1])};
    }
}
DI void lds_wait_ge(unsigned addr, unsigned v) {
#pragma unroll 1
    for (unsigned spins = 0; spins < SPIN_CAP; ++spins) { if (*(volatile LAS unsigned*)lds_at(addr) >= v) break; }
    asm volatile("" ::: "memory");
}
DI void lds_set(unsigned addr) { asm volatile("s_waitcnt lgkmcnt(0)" ::: "memory"); *(volatile LAS unsigned*)lds_at(addr) = 1u; }
DI void lds_count(unsigned addr, int lane) {
    asm volatile("s_waitcnt lgkmcnt(0)" ::: "memory");
    if (lane == 0) __hip_atomic_fetch_add((LAS unsigned*)lds_at(addr), 1u, __ATOMIC_RELAXED, __HIP_MEMORY_SCOPE_WORKGROUP);
}
DI unsigned cvtpk2(f32x2 v) { return __builtin_bit_cast(unsigned, __builtin_convertvector(v, bf2_t)); }

DI void phi_tile(const f32x16& pk, unsigned base, int r, int h2) {
    float mx = __builtin_fmaxf(pk[0], pk[1]), mn = __builtin_fminf(pk[0], pk[1]);
#pragma unroll
    for (int i = 2; i < 14; i += 2) { mx = __builtin_fmaxf(__builtin_fmaxf(mx, pk[i]), pk[i + 1]); mn = __builtin_fminf(__builtin_fminf(mn, pk[i]), pk[i + 1]); }
    const float cp = -xhalf_max(mx) * L2E, cn = xhalf_min(mn) * L2E;
    f32x2 ep[7], en[7];
    exp_pair(pk, cp, cn, ep, en);
    f32x2 sp = ep[0], sn = en[0];
#pragma unroll
    for (int i = 1; i < 7; ++i) { sp += ep[i]; sn += en[i]; }
    const float ip = __builtin_amdgcn_rcpf(xhalf_sum(sp[0] + sp[1])), in = __builtin_amdgcn_rcpf(xhalf_sum(sn[0] + sn[1]));
    const f32x2 ip2 = {ip, ip}, in2 = {in, in};
#pragma unroll
    for (int g = 0; g < 4; ++g) {
        const unsigned a = base + r * 64 + 8 * ((2 * g + h2) ^ ((r >> 1) & 7));
        const u32x2 dp = {cvtpk2(ep[2 * g] * ip2), g < 3 ? cvtpk2(ep[(2 * g + 1) % 7] * ip2) : 0u};
        const u32x2 dn = {cvtpk2(en[2 * g] * in2), g < 3 ? cvtpk2(en[(2 * g + 1) % 7] * in2) : 0u};
        *(LAS u32x2*)lds_at(a) = dp;
        *(LAS u32x2*)lds_at(a + 2048) = dn;
    }
}

#define POLL_SLEEP 4
#define POLL_DEPTH 1
DI void wait_flags(const GAS u64* f, u64 tag) {
#pragma unroll 1
    for (unsigned spins = 0; spins < SPIN_CAP; ++spins) {
        const u64 v = __hip_atomic_load(f, __ATOMIC_RELAXED, __HIP_MEMORY_SCOPE_AGENT);
        if (__builtin_amdgcn_ballot_w64(v != tag) == 0) break;
        __builtin_amdgcn_s_sleep(POLL_SLEEP);
    }
    asm volatile("" ::: "memory");
}

#define SB() __builtin_amdgcn_sched_barrier(0)
#define PINV(x) asm volatile("" : "+v"(x))
struct Frag { bf16x8 x0, x1, fk, fv; };

DI void build_aphi(bf16x8 (&aphi)[2], int r, int h2) {
    const int hr = (r >> 2) & 1, reg = (r & 3) + 4 * (r >> 3), f = 14 * hr + (reg < 14 ? reg : 0);
    const unsigned a = L_WPHI + 2 * (4 * h2 * DH + f);
#pragma unroll
    for (int s = 0; s < 2; ++s)
#pragma unroll
        for (int jj = 0; jj < 8; ++jj) {
            const int d0 = 16 * s + 8 * (jj >> 2) + (jj & 3);
            const bool ok = reg < 14 && (d0 + 4 < DH || h2 == 0);
            const unsigned short v = *(const LAS unsigned short*)lds_at(a + 2 * d0 * DH);
            aphi[s][jj] = ok ? (short)v : (short)0;
        }
}

__global__ void __launch_bounds__(512, 2) hedgehog_mix_kernel(Params p) {
    const int tid = threadIdx.x, lane = tid & 63;
    const unsigned w = rfl(tid >> 6), cw = w & 3;
    if (w == 4) {
        u64 pc; asm volatile("s_getpc_b64 %0" : "=s"(pc));
        const unsigned l0 = (blockIdx.x >> 3) * 6 + (lane < 6 ? lane : 5), l = l0 < PF_CODE_LINES ? l0 : 0;
        gdma4((const char*)(pc & ~127ull) + l * 128, L_SCR + 256);
    }
    const int r = lane & 31, h2 = lane >> 5, i16 = lane & 15, q4 = i16 >> 2, p4 = i16 & 3, blk16 = (lane >> 4) & 1;
    const unsigned xcd = blockIdx.x & 7, j = blockIdx.x >> 3;
    const unsigned tb = (xcd & 1) * 4 + (j & 3), bh = (xcd >> 1) * 8 + (j >> 2), br = bh >> 4, h = bh & 15;
    const unsigned og = xcd * 4 + j / 7, oc = j % 7;
    const bool has_out = j < 28;
    const unsigned rot = ROT == 1 ? ((j >> 2) + 2 * (j & 3)) % 7 : ROT == 2 ? (2 * (j & 3)) % 7 : ROT == 3 ? (2 * (j & 3) + (j >> 3)) % 7
                       : ROT == 4 ? (2 * (j & 3) + (j >> 4)) % 7 : ROT == 5 ? (2 * (j >> 2) + 2 * (j & 3)) % 7 : ROT == 6 ? ((j >> 2) + 3 * (j & 3)) % 7 : 0;
    auto seg = [&](int n) { const unsigned t = rot + n; return t >= 7 ? t - 7 : t; };
#define PIN_ARGS() asm volatile("" : "+s"(p.x), "+s"(p.wqkvA), "+s"(p.wqkvB), "+s"(p.wphiA), "+s"(p.wphiB), "+s"(p.woA), "+s"(p.woB), "+s"(p.gate), "+s"(p.out), "+s"(p.ws))
    PIN_ARGS();
    STAMP_DECL;
    STAMP(0);
    const rsrc_t rws = mk_rsrc(p.ws, WS_END);
    const f32x16 zero16 = {};
    const int cq = 4 * blk16 + p4;
    const unsigned rowpart = (8 * h2 + q4) * 64;
    const unsigned coloff = (8 * (h & 1) + 8 * cq) & 63;
    const unsigned laneX = cw * 8192 + r * 128 + 16 * (h2 ^ ((r >> 1) & 7));

    if (__builtin_expect(w >= 4, 1)) {
        const rsrc_t rx = mk_rsrc(p.x, S_ * XROW);
        const rsrc_t rw = mk_rsrc(br ? p.wqkvB : p.wqkvA, D_ * WROW);
        const unsigned vx0 = (lane >> 3) * XROW + 16 * ((lane & 7) ^ (lane >> 4));
        const unsigned vx1 = (lane >> 3) * XROW + 16 * ((lane & 7) ^ (4 + (lane >> 4)));
        const unsigned vw = (lane >> 2) * WROW + 16 * (lane & 3);
        const unsigned sx = (tb * 256 + cw * 64) * XROW;
        const unsigned sw = cw * 16 * WROW + h * 56 - 8 * (h & 1);
        auto issue = [&](int s) {
            dma_seg(rw, rx, vw, vx0, vx1, sw + seg(s) * (64 * WROW) + 896, sx + seg(s) * 128, slot_of(s) + SLOT_W + cw * 1024, slot_of(s) + cw * 8192);
        };
        bf16x8 xs[5][4][2];
        auto grab = [&](int s) {
#pragma unroll
            for (int ks = 0; ks < 4; ++ks) {
                const unsigned a = (laneX ^ (32 * ks)) + slot_of(s);
                xs[s][ks][0] = lds_b128(a); xs[s][ks][1] = lds_b128(a + 4096);
            }
        };
        STAMP(11);
        issue(0); issue(1);
        u64 ev;
        asm volatile("global_load_dwordx2 %0, %1, %2 sc1" : "=&v"(ev) : "v"(0u), "s"(p.ws) : "memory");
        STAMP(1);
        WAIT_VM_LGKM_BAR(11);
        STAMP(2);
        grab(0); issue(2);
        __builtin_amdgcn_raw_buffer_store_b128((u32x4){~0u, ~0u, ~0u, ~0u}, rws, lane * 16, WS_KVP + ((bh * 8 + tb) * 4 + cw) * 1024, 16);
        asm volatile("s_waitcnt vmcnt(11) lgkmcnt(0)\n\ts_barrier" : "+v"(ev) :: "memory");
        const u64 tag = uniform64(ev) + 1;
        grab(1); issue(3); WAIT_VM_LGKM_BAR(10);
        if (lane == 0) __hip_atomic_store((GAS u64*)((char*)p.ws + WS_F1 + (bh * 4 + cw) * F1_GRP + tb * FS), tag, __ATOMIC_RELAXED, __HIP_MEMORY_SCOPE_AGENT);
        grab(2); issue(4); WAIT_VM_LGKM_BAR(11);
        grab(3); issue(5); WAIT_VM_LGKM_BAR(10);
        grab(4); issue(6); WAIT_VM_LGKM_BAR(10);
        WAIT_VM_LGKM_BAR(0);
        STAMP(3);
        const GAS u64* const f1p = (const GAS u64*)((const char*)p.ws + WS_F1 + (bh * 4 + cw) * F1_GRP + (lane & 7) * FS);
        u64 f1v;
        asm volatile("global_load_dwordx2 %0, %1, off sc1" : "=&v"(f1v) : "v"(f1p) : "memory");
#pragma unroll
        for (int s = 0; s < 7; ++s) { const unsigned sg = seg((s + 5) % 7); dma16(rw, vw, sw + sg * (64 * WROW), L_WQ + sg * 4096 + cw * 1024); }
        bf16x8 aphi[2];
        build_aphi(aphi, r, h2);
        PINV(aphi[0]); PINV(aphi[1]);
        asm volatile("s_waitcnt vmcnt(7)" : "+v"(f1v) :: "memory");
        if (__builtin_amdgcn_ballot_w64(f1v != tag) != 0) wait_flags(f1p, tag);
        STAMP(4);
        WAIT_VM_LGKM_BAR(0);
        STAMP(5);
        f32x16 qT[2] = {zero16, zero16};
        {
            const unsigned aq_real = L_WQ + rowpart + coloff, aq_pad = L_GT + rowpart;
            bf16x8 fq[3], xl[2][2];
            auto seg_of = [](int n) { return n < 8 ? 5 + (n >> 2) : (n - 8) >> 2; };
            unsigned aQ = 0;
            auto rdq = [&](int n) {
                if ((n & 3) == 0) { const unsigned sg = seg(seg_of(n)); aQ = cq == 7 ? aq_pad + 8 * sg : aq_real + 4096 * sg; PINV(aQ); }
                fq[n % 3] = tr_frag(aQ + (n & 3) * 1024, aQ + (n & 3) * 1024 + 256);
            };
            auto rdx = [&](int n) { const unsigned a = (laneX ^ (32 * (n & 3))) + slot_of(seg_of(n)); xl[n & 1][0] = lds_b128(a); xl[n & 1][1] = lds_b128(a + 4096); };
            rdq(0); rdx(0); rdq(1);
            SB();
#pragma unroll
            for (int n = 0; n < 28; ++n) {
                const int s = seg_of(n), ks = n & 3;
                if (n + 1 < 8) rdx(n + 1);
                if (n + 2 < 28) rdq(n + 2);
                SB();
                qT[0] = MFMA(fq[n % 3], n >= 8 ? xs[n >= 8 ? s : 0][ks][0] : xl[n & 1][0], qT[0]);
                qT[1] = MFMA(fq[n % 3], n >= 8 ? xs[n >= 8 ? s : 0][ks][1] : xl[n & 1][1], qT[1]);
                SB();
                if (n == 7) {
                    asm volatile("s_waitcnt lgkmcnt(0)" ::: "memory");
                    *(volatile LAS unsigned*)lds_at(L_XFREE + 4 * cw) = 1u;
                }
            }
        }
        STAMP(6);
        bf16x8 eq[2][2][2];
        float gs[2];
        {
            f32x16 pq[2];
            const bf16x8 q00 = pack8(qT[0], 0), q10 = pack8(qT[1], 0);
            pq[0] = MFMA(aphi[0], q00, zero16); pq[1] = MFMA(aphi[0], q10, zero16);
            const bf16x8 q01 = pack8(qT[0], 1), q11 = pack8(qT[1], 1);
            pq[0] = MFMA(aphi[1], q01, pq[0]); pq[1] = MFMA(aphi[1], q11, pq[1]);
#pragma unroll
            for (int b = 0; b < 2; ++b) {
                const float lg = swap_hi(qT[b][12]);
                gs[b] = __builtin_amdgcn_rcpf(1.f + __builtin_amdgcn_exp2f(br ? lg * L2E : -lg * L2E));
                PINV(gs[b]);
            }
            float cp[2], cn[2];
            row_extrema(pq[0], pq[1], cp, cn);
            PINV(cp[0]); PINV(cp[1]); PINV(cn[0]); PINV(cn[1]);
#pragma unroll
            for (int b = 0; b < 2; ++b) {
                const f32x2 l2 = {L2E, L2E}, cp2 = {cp[b], cp[b]}, cn2 = {cn[b], cn[b]};
                unsigned pe[8], ne[8];
#pragma unroll
                for (int i = 0; i < 7; ++i) {
                    const f32x2 v = {pq[b][2 * i], pq[b][2 * i + 1]};
                    const f32x2 a = __builtin_elementwise_fma(v, l2, cp2), c = __builtin_elementwise_fma(v, -l2, cn2);
                    pe[i] = cvtpk2((f32x2){__builtin_amdgcn_exp2f(a[0]), __builtin_amdgcn_exp2f(a[1])});
                    ne[i] = cvtpk2((f32x2){__builtin_amdgcn_exp2f(c[0]), __builtin_amdgcn_exp2f(c[1])});
                }
                pe[7] = 0; ne[7] = 0;
                eq[b][0][0] = __builtin_bit_cast(bf16x8, (u32x4){pe[0], pe[1], pe[2], pe[3]}); eq[b][0][1] = __builtin_bit_cast(bf16x8, (u32x4){pe[4], pe[5], pe[6], pe[7]});
                eq[b][1][0] = __builtin_bit_cast(bf16x8, (u32x4){ne[0], ne[1], ne[2], ne[3]}); eq[b][1][1] = __builtin_bit_cast(bf16x8, (u32x4){ne[4], ne[5], ne[6], ne[7]});
            }
        }
#pragma unroll
        for (int b = 0; b < 2; ++b) asm volatile("" : "+v"(eq[b][0][0]), "+v"(eq[b][0][1]), "+v"(eq[b][1][0]), "+v"(eq[b][1][1]), "+v"(gs[b]));
        STAMP(7);
        LGKM_BAR();
        __builtin_amdgcn_s_setprio(3);
        STAMP(8);
        {
            bf16x8 akv[4];
#pragma unroll
            for (int f = 0; f < 4; ++f) akv[f] = lds_b128(L_KVA + f * 1024 + lane * 16);
#pragma unroll
            for (int b = 0; b < 2; ++b) {
                f32x16 np = MFMA(akv[0], eq[b][0][0], zero16); np = MFMA(akv[1], eq[b][0][1], np);
                f32x16 nn = MFMA(akv[2], eq[b][1][0], zero16); nn = MFMA(akv[3], eq[b][1][1], nn);
                const float ia = __builtin_amdgcn_rcpf(np[15]), ib = __builtin_amdgcn_rcpf(nn[15]);
                const float den = np[14] * ia + nn[14] * ib + 1e-6f;
                const float sc = gs[b] * __builtin_amdgcn_rcpf(den);
                const float ca = ia * sc, cb = ib * sc;
                float o[14];
#pragma unroll
                for (int i = 0; i < 14; ++i) o[i] = np[i] * ca + nn[i] * cb;
                const u32x4 o0 = {cvtpk(o[0], o[1]), cvtpk(o[2], o[3]), cvtpk(o[4], o[5]), cvtpk(o[6], o[7])};
                const u32x4 o1 = {cvtpk(o[8], o[9]), cvtpk(o[10], o[11]), cvtpk(o[12], o[13]), 0u};
                const unsigned so = WS_O + (((tb * 4 + cw) * 32 + bh) * 4 + b * 2) * 1024;
                __builtin_amdgcn_raw_buffer_store_b128(o0, rws, lane * 16, so, 16);
                __builtin_amdgcn_raw_buffer_store_b128(o1, rws, lane * 16, so + 1024, 16);
            }
        }
        STAMP(9);
        if (has_out) {
            const unsigned vwo = (lane >> 3) * XROW + 16 * ((lane & 7) ^ (4 * ((lane >> 4) & 1)));
            dma_rows<8>(mk_rsrc(p.woB, D_ * XROW), vwo, (640 - 448 + 64 * cw) * XROW + oc * 128, L_WO + (640 + 64 * cw) * 128);
            WAIT_VM(8);
        } else WAIT_VM(0);
        if (lane == 0) __hip_atomic_store((GAS u64*)((char*)p.ws + WS_F2 + ((tb * 4 + cw) * 2 + br) * F2_GRP + h * FS), tag, __ATOMIC_RELAXED, __HIP_MEMORY_SCOPE_AGENT);
        STAMP(10);
        if (has_out) {
            WAIT_VM(1);
            if (lane == 0) __hip_atomic_fetch_add((LAS unsigned*)lds_at(L_WOCNT), 1u, __ATOMIC_RELAXED, __HIP_MEMORY_SCOPE_WORKGROUP);
        }
        STAMP_FLUSH();
        return;
    }

    u64 tag;
    {
        const rsrc_t rg = mk_rsrc(p.gate, XROW);
        const rsrc_t rp = mk_rsrc(br ? p.wphiB : p.wphiA, 16 * DH * DH * 2);
        unsigned g0, g1; u32x2 wp;
        u64 ev;
        asm volatile("global_load_dwordx2 %0, %1, %2 sc1" : "=&v"(ev) : "v"(0u), "s"(p.ws) : "memory");
        asm volatile("buffer_load_ushort %0, %3, %4, 0 offen\n\tbuffer_load_ushort %1, %3, %4, 0 offen offset:512\n\tbuffer_load_dwordx2 %2, %5, %6, %7 offen"
                     : "=&v"(g0), "=&v"(g1), "=&v"(wp) : "v"(tid * 2), "s"(rg), "v"(tid * 8), "s"(rp), "s"(h * (DH * DH * 2)) : "memory");
        asm volatile("s_waitcnt vmcnt(0) lgkmcnt(0)" : "+v"(g0), "+v"(g1), "+v"(wp), "+v"(ev) :: "memory");
        tag = uniform64(ev) + 1;
        *(LAS u32x2*)lds_at(L_GT + (tid & 63) * 64 + (tid >> 6) * 8) = (u32x2){g0 & 0xffffu, 0u};
        *(LAS u32x2*)lds_at(L_GT + (tid & 63) * 64 + (4 + (tid >> 6)) * 8) = (u32x2){g1 & 0xffffu, 0u};
        *(LAS u32x2*)lds_at(L_WPHI + tid * 8) = wp;
        if (tid < 16) *(volatile LAS unsigned*)lds_at(L_XFREE + 4 * tid) = 0u;
    }
    __builtin_amdgcn_s_setprio(2);
    LGKM_BAR();
    STAMP(2);
    bf16x8 aphi[2];
    build_aphi(aphi, r, h2);
    PINV(aphi[0]); PINV(aphi[1]);
    const unsigned laneW = SLOT_W + rowpart + coloff;
    f32x16 vinit;
#pragma unroll
    for (int i = 0; i < 16; ++i) vinit[i] = r == 28 ? 1.f : 0.f;
    PINV(vinit);
    f32x16 kT[2] = {zero16, zero16}, vv[2] = {vinit, vinit};
    {
        unsigned aX[4], aW, aV;
        auto addr = [&](int s) {
#pragma unroll
            for (int ks = 0; ks < 4; ++ks) { aX[ks] = (laneX ^ (32 * ks)) + slot_of(s); PINV(aX[ks]); }
            aW = laneW + slot_of(s); PINV(aW);
            aV = cq == 7 ? L_GT + rowpart + 56 : aW + 4096; PINV(aV);
        };
        Frag F[2];
#define RD_X(f, ks) do { f.x0 = lds_b128(aX[ks]); } while (0)
#define RD_X1(f, ks) do { f.x1 = lds_b128(aX[ks] + 4096); } while (0)
#define RD_K(f, ks) do { f.fk = tr_frag(aW + ks * 1024, aW + ks * 1024 + 256); } while (0)
#define RD_V(f, ks) do { f.fv = tr_frag(aV + ks * 1024, aV + ks * 1024 + 256); } while (0)
        addr(0);
        RD_X(F[0], 0); RD_K(F[0], 0); RD_X1(F[0], 0); RD_V(F[0], 0); SB();
#pragma unroll
        for (int n = 0; n < 28; ++n) {
            Frag& c = F[n & 1];
            Frag& x = F[(n + 1) & 1];
            const int nks = (n + 1) & 3;
            const bool more = n + 1 < 28;
            kT[0] = MFMA(c.fk, c.x0, kT[0]); SB();
            if (more && nks == 0) { LGKM_BAR(); addr((n + 1) >> 2); SB(); }
            if (more) { RD_X(x, nks); RD_K(x, nks); } SB();
            kT[1] = MFMA(c.fk, c.x1, kT[1]); SB();
            if (more) { RD_X1(x, nks); } SB();
            vv[0] = MFMA(c.x0, c.fv, vv[0]); SB();
            if (more) { RD_V(x, nks); } SB();
            vv[1] = MFMA(c.x1, c.fv, vv[1]); SB();
        }
    }
    STAMP(3);

    const unsigned phiW = L_PHI + cw * 8192;
    const unsigned pt0 = 4 * h2 + q4;
    const unsigned pa0 = phiW + pt0 * 64 + 8 * (cq ^ ((pt0 >> 1) & 7));
    const unsigned pa1 = (pa0 ^ 32) + 8 * 64;
    f32x16 kvp = zero16, kvn = zero16;
    f32x16 pk[2];
    {
        const bf16x8 k00 = pack8(kT[0], 0), k10 = pack8(kT[1], 0);
        pk[0] = MFMA(aphi[0], k00, zero16); pk[1] = MFMA(aphi[0], k10, zero16);
        const bf16x8 k01 = pack8(kT[0], 1), k11 = pack8(kT[1], 1);
        pk[0] = MFMA(aphi[1], k01, pk[0]); pk[1] = MFMA(aphi[1], k11, pk[1]);
    }
    bf16x8 vp[2][2];
#pragma unroll
    for (int b = 0; b < 2; ++b)
#pragma unroll
        for (int s = 0; s < 2; ++s) { vp[b][s] = pack8(vv[b], s); PINV(vp[b][s]); }
    SB();
#pragma unroll
    for (int b = 0; b < 2; ++b) {
        phi_tile(pk[b], phiW + b * 4096, r, h2);
#pragma unroll
        for (int s = 0; s < 2; ++s) {
            const unsigned o = b * 4096 + s * 1024;
            kvp = MFMA(tr_frag(pa0 + o, pa1 + o), vp[b][s], kvp);
            kvn = MFMA(tr_frag(pa0 + o + 2048, pa1 + o + 2048), vp[b][s], kvn);
        }
    }
    asm volatile("s_waitcnt lgkmcnt(0)" ::: "memory");
#pragma unroll
    for (int f = 0; f < 4; ++f)
#pragma unroll
        for (int hh = 0; hh < 2; ++hh) {
            const f32x16& t = f < 2 ? kvp : kvn;
            const int o = 8 * (f & 1) + 4 * hh;
            *(LAS f32x4*)lds_at(phiW + f * 2048 + hh * 1024 + lane * 16) = (f32x4){t[o], t[o + 1], t[o + 2], t[o + 3]};
        }
    STAMP(4);
    LGKM_BAR();
    STAMP(5);
    {
        f32x4 t[4][2];
#pragma unroll
        for (int u = 0; u < 4; ++u) {
            t[u][0] = *(const LAS f32x4*)lds_at(L_PHI + u * 8192 + cw * 2048 + lane * 16);
            t[u][1] = *(const LAS f32x4*)lds_at(L_PHI + u * 8192 + cw * 2048 + 1024 + lane * 16);
        }
        SB();
        float a[8];
#pragma unroll
        for (int i = 0; i < 8; ++i) {
            float x01 = addf(t[0][i >> 2][i & 3], t[1][i >> 2][i & 3]), x23 = addf(t[2][i >> 2][i & 3], t[3][i >> 2][i & 3]);
            a[i] = addf(x01, x23);
        }
        u32x4 o = {cvtpk(a[0], a[1]), cvtpk(a[2], a[3]), cvtpk(a[4], a[5]), cvtpk(a[6], a[7])};
        STAMPX(1, o);
        __builtin_amdgcn_raw_buffer_store_b128(o, rws, lane * 16, WS_KVP + ((bh * 8 + tb) * 4 + cw) * 1024, 16);
    }
    bf16x8 sel;
    {
        const int jj = (r & 3) + 4 * (r >> 3);
        const bool on = r < 16 && ((r >> 2) & 1) == h2;
        const unsigned one = on ? (0x3F80u << (16 * (jj & 1))) : 0u;
        sel = __builtin_bit_cast(bf16x8, (u32x4){(jj >> 1) == 0 ? one : 0u, (jj >> 1) == 1 ? one : 0u, (jj >> 1) == 2 ? one : 0u, (jj >> 1) == 3 ? one : 0u});
        PINV(sel);
    }
    {
        const rsrc_t roA = mk_rsrc(p.woA, D_ * XROW), roB = mk_rsrc(p.woB, D_ * XROW);
        const unsigned vwo = (lane >> 3) * XROW + 16 * ((lane & 7) ^ (4 * ((lane >> 4) & 1)));
        dma_rows<2>(roA, vwo, (256 + 16 * cw) * XROW + oc * 128, L_WO + (256 + 16 * cw) * 128);
        dma_rows<2>(roB, vwo, (576 - 448 + 16 * cw) * XROW + oc * 128, L_WO + (576 + 16 * cw) * 128);
#pragma unroll 1
        for (unsigned spins = 0; spins < SPIN_CAP; ++spins) { if (*(volatile LAS unsigned*)lds_at(L_XFREE + 4 * cw) != 0u) break; }
        dma_rows<6>(roA, vwo, 64 * cw * XROW + oc * 128, L_WO + 64 * cw * 128);
        const rsrc_t roC = cw < 2 ? roA : roB;
        const unsigned R = 320 + 64 * cw, Rg = cw < 2 ? R : R - 448;
        const int hr = (r >> 2) & 1, reg = (r & 3) + 4 * (r >> 3);
        const int col = reg < 14 ? 14 * hr + reg : 28;
        const unsigned vo = (h2 * 32 + col) * 16;
        u32x4 part[8];
        STAMP(9);
#pragma unroll
        for (int u = 0; u < 8; ++u) part[u] = load_sc1_asm(rws, vo, WS_KVP + ((bh * 8 + u) * 4 + cw) * 1024);
        dma_rows<2>(roA, vwo, (64 * cw + 48) * XROW + oc * 128, L_WO + (64 * cw + 48) * 128);
        dma_rows<8>(roC, vwo, Rg * XROW + oc * 128, L_WO + R * 128);
        asm volatile("s_waitcnt vmcnt(10)" : "+v"(part[0]), "+v"(part[1]), "+v"(part[2]), "+v"(part[3]), "+v"(part[4]), "+v"(part[5]), "+v"(part[6]), "+v"(part[7]) :: "memory");
        STAMP(6);
#pragma unroll 1
        for (unsigned spins = 0; spins < SPIN_CAP; ++spins) {
            unsigned m = 0;
#pragma unroll
            for (int u = 0; u < 8; ++u) { m = umax3(m, part[u][0], part[u][1]); m = umax3(m, part[u][2], part[u][3]); }
            if (__builtin_amdgcn_ballot_w64(m == ~0u) == 0) break;
#pragma unroll
            for (int u = 0; u < 8; ++u) part[u] = load_sc1_asm(rws, vo, WS_KVP + ((bh * 8 + u) * 4 + cw) * 1024);
            asm volatile("s_waitcnt vmcnt(0)" : "+v"(part[0]), "+v"(part[1]), "+v"(part[2]), "+v"(part[3]), "+v"(part[4]), "+v"(part[5]), "+v"(part[6]), "+v"(part[7]) :: "memory");
        }
        STAMP(7);
        SB();
        f32x16 acc = zero16;
#pragma unroll
        for (int u = 0; u < 8; ++u) acc = MFMA(sel, __builtin_bit_cast(bf16x8, part[u]), acc);
        u32x4 o = __builtin_bit_cast(u32x4, pack8(acc, 0));
        if (reg == 15) o = (u32x4){ONE2, ONE2, ONE2, ONE2};
        *(LAS u32x4*)lds_at(L_KVA + cw * 1024 + lane * 16) = o;
    }
    LGKM_BAR();
    STAMP(8);
    if (!has_out) { STAMP_FLUSH(); return; }
    WAIT_VM(0);
    if (lane == 0) __hip_atomic_fetch_add((LAS unsigned*)lds_at(L_WOCNT), 1u, __ATOMIC_RELAXED, __HIP_MEMORY_SCOPE_WORKGROUP);
    auto wait_wo = [&]() {
#pragma unroll 1
        for (unsigned spins = 0; spins < SPIN_CAP; ++spins) { if (*(volatile LAS unsigned*)lds_at(L_WOCNT) >= (WO2_BY_H ? 8u : 4u)) break; }
        asm volatile("" ::: "memory");
    };

    const unsigned tk = cw & 1, kh = cw >> 1;
    constexpr u64 HORD = 0xDB9642FECA875310ull;
    const GAS u64* f2 = (const GAS u64*)((const char*)p.ws + WS_F2 + (og * 2 + kh) * F2_GRP);
    f32x16 mine = zero16, other = zero16;
    {
        const unsigned colp = 32 * blk16 + 8 * p4;
        const unsigned r0 = kh * 448 + 14 * h2 + q4, r1 = kh * 448 + 14 * h2 + (q4 < 1 ? q4 : 1);
        unsigned a0 = L_WO + r0 * 128 + 64 * (kh ^ ((r0 >> 1) & 1)) + colp;
        unsigned a1 = L_WO + r1 * 128 + 64 * (kh ^ ((r1 >> 1) & 1)) + colp;
        unsigned a2 = a0 ^ 64, a3 = a1 ^ 64;
        asm volatile("" : "+v"(a0), "+v"(a1), "+v"(a2), "+v"(a3));
        u32x4 of[16][2];
        auto ld = [&](int i) {
            const unsigned hd = (unsigned)(HORD >> (4 * i)) & 15u;
#pragma unroll
            for (int s = 0; s < 2; ++s)
                of[i][s] = __builtin_amdgcn_raw_buffer_load_b128(rws, lane * 16, WS_O + (((og * 32 + kh * 16 + hd) * 2 + tk) * 2 + s) * 1024, 16);
        };
        bf16x8 A[2][4];
        auto rd = [&](bf16x8 (&f)[4], int i) {
            const unsigned o = ((unsigned)(HORD >> (4 * i)) & 15u) * 28 * 128;
            f[0] = tr_frag(a0 + o, a0 + o + 512); f[1] = tr_frag(a2 + o, a2 + o + 512);
            f[2] = tr_frag(a0 + o + 1024, a1 + o + 1536); f[3] = tr_frag(a2 + o + 1024, a3 + o + 1536);
        };
        auto step = [&](int i) {
            if (i + 1 < 16) rd(A[(i + 1) & 1], i + 1);
            SB();
            const bf16x8 b0 = __builtin_bit_cast(bf16x8, of[i][0]), b1 = __builtin_bit_cast(bf16x8, of[i][1]);
            mine = MFMA(A[i & 1][0], b0, mine); other = MFMA(A[i & 1][1], b0, other);
            mine = MFMA(A[i & 1][2], b1, mine); other = MFMA(A[i & 1][3], b1, other);
            SB();
        };
        wait_wo();
        STAMP(10);
        wait_flags(f2 + (FS / 8) * (lane & 15), tag);
        STAMP(11);
#pragma unroll
        for (int i = 0; i < 16; ++i) ld(i);
        SB();
        rd(A[0], 0); SB();
#pragma unroll
        for (int i = 0; i < 16; ++i) step(i);
    }
    STAMP(12);
#pragma unroll
    for (int g = 0; g < 4; ++g)
        *(LAS f32x4*)lds_at(L_PHI + cw * 4096 + g * 1024 + lane * 16) = (f32x4){other[4 * g], other[4 * g + 1], other[4 * g + 2], other[4 * g + 3]};
    LGKM_BAR();
    if (blockIdx.x == 0 && tid == 0) __hip_atomic_store((GAS u64*)((char*)p.ws + WS_EPOCH), tag, __ATOMIC_RELAXED, __HIP_MEMORY_SCOPE_AGENT);
    {
        unsigned d[8];
#pragma unroll
        for (int g = 0; g < 4; ++g) {
            const f32x4 t = *(const LAS f32x4*)lds_at(L_PHI + (cw ^ 2) * 4096 + g * 1024 + lane * 16);
            d[2 * g] = cvtpk(mine[4 * g] + t[0], mine[4 * g + 1] + t[1]);
            d[2 * g + 1] = cvtpk(mine[4 * g + 2] + t[2], mine[4 * g + 3] + t[3]);
        }
        const rsrc_t rout = mk_rsrc(p.out, S_ * XROW);
        const unsigned vo = r * XROW + h2 * 16, so = (og * 64 + tk * 32) * XROW + oc * 128 + kh * 64;
#pragma unroll
        for (int k = 0; k < 4; k += 2) {
            auto sx0 = __builtin_amdgcn_permlane32_swap(d[2 * k], d[2 * k + 2], false, false);
            auto sx1 = __builtin_amdgcn_permlane32_swap(d[2 * k + 1], d[2 * k + 3], false, false);
            const u32x4 o = {sx0[0], sx1[0], sx0[1], sx1[1]};
            __builtin_amdgcn_raw_buffer_store_b128(o, rout, vo, so + 16 * k, 0);
        }
    }
    STAMP(13);
    STAMP_FLUSH();
}

extern "C" void kernel_launch(void* const* d_in, const int* in_sizes, int n_in, void* d_out, int out_size, void* d_ws, size_t ws_size,
                              hipStream_t stream) {
    Params p;
    p.x = d_in[0]; p.wqkvA = d_in[1]; p.wphiA = d_in[2]; p.woA = d_in[3];
    p.wqkvB = d_in[5]; p.wphiB = d_in[6]; p.woB = d_in[7]; p.gate = d_in[8];
    p.out = d_out; p.ws = d_ws;
    (void)hipFuncSetAttribute((const void*)hedgehog_mix_kernel, hipFuncAttributeMaxDynamicSharedMemorySize, L_TOTAL);
    hedgehog_mix_kernel<<<256, 512, L_TOTAL, stream>>>(p);
}
```

```cpp
#define PREFETCH 0
#define PF_CODE 1
#define ROT 1
#include <hip/hip_runtime.h>
#include <stdint.h>

typedef short bf16x8 __attribute__((ext_vector_type(8)));
typedef short s16x4 __attribute__((ext_vector_type(4)));
typedef short v4i16_t __attribute__((ext_vector_type(4)));
typedef __bf16 bf2_t __attribute__((ext_vector_type(2)));
typedef float f32x2 __attribute__((ext_vector_type(2)));
typedef float f32x4 __attribute__((ext_vector_type(4)));
typedef float f32x16 __attribute__((ext_vector_type(16)));
typedef unsigned u32x2 __attribute__((ext_vector_type(2)));
typedef unsigned u32x4 __attribute__((ext_vector_type(4)));
typedef unsigned long long u64;
#define LAS __attribute__((address_space(3)))
#define GAS __attribute__((address_space(1)))
#define DI __device__ __forceinline__
#define MFMA(a, b, c) __builtin_amdgcn_mfma_f32_32x32x16_bf16((a), (b), (c), 0, 0, 0)

constexpr int S_ = 2048, D_ = 448, DH = 28;
constexpr unsigned XROW = D_ * 2;
constexpr unsigned WROW = 3 * D_ * 2;
constexpr unsigned WS_EPOCH = 0;
#define FS 128
#define PZ_STORE 1
#define PZ_FLAG 9
#define SPLITK 0
#define OD 5
#define OROT 0
#define XLD 1
#define GATE 0
#define ONEPOLL 1
#define PF_WO2 0
#define WO2_BY_H 1
#define STAGES 3
#define DATAPOLL 1
constexpr unsigned F1_GRP = FS * 8 < 128 ? 128 : FS * 8, F2_GRP = FS * 16;
constexpr unsigned WS_F1 = 4096;
constexpr unsigned WS_F2 = WS_F1 + 32 * 4 * 1024;
constexpr unsigned WS_KVP = WS_F2 + 64 * 2048;
constexpr unsigned WS_O = WS_KVP + 32 * 8 * 4 * 1024;
constexpr unsigned WS_END = WS_O + 32 * 32 * 4 * 1024;
constexpr unsigned WS_STAMP = 8u << 20;
constexpr unsigned SLOT = 40960, SLOT_W = 32768;
constexpr unsigned L_B = 0, L_A = SLOT, L_C = 2 * SLOT;
constexpr unsigned L_WQ = 3 * SLOT;
constexpr unsigned L_GT = L_WQ + 448 * 64;
constexpr unsigned L_WPHI = L_GT + 4096;
constexpr unsigned L_SCR = L_WPHI + 2048;
constexpr unsigned L_XFREE = L_SCR + 512;
constexpr unsigned L_WOCNT = L_XFREE + 16;
constexpr unsigned L_REDCNT = L_XFREE + 20;
constexpr unsigned L_WQCNT = L_XFREE + 24;
constexpr unsigned L_FSET = L_XFREE + 28;
constexpr unsigned L_F2OK = L_XFREE + 64;
constexpr unsigned L_K1F = L_XFREE + 32;
constexpr unsigned L_PHI1F = L_XFREE + 48;
constexpr unsigned L_PHI = L_B;
constexpr unsigned L_KVA = L_B + SLOT_W;
constexpr unsigned L_WO = L_A;
constexpr unsigned L_TOTAL = 163840;
static_assert(L_WPHI + 2048 <= L_TOTAL, "lds");
static_assert(L_WO + 896 * 128 == L_WPHI, "lds");
__host__ __device__ constexpr unsigned slot_of(int s) { return s % 3 == 0 ? L_A : (s % 3 == 1 ? L_B : L_C); }

struct Params {
    const void* x; const void* wqkvA; const void* wqkvB; const void* wphiA; const void* wphiB;
    const void* woA; const void* woB; const void* gate; void* out; void* ws;
};

typedef __amdgpu_buffer_rsrc_t rsrc_t;
DI rsrc_t mk_rsrc(const void* p, unsigned bytes) { return __builtin_amdgcn_make_buffer_rsrc((void*)p, (short)0, (int)bytes, 0x00020000); }
DI unsigned rfl(unsigned v) { return (unsigned)__builtin_amdgcn_readfirstlane((int)v); }
DI void dma16(rsrc_t r, unsigned voff, unsigned soff, unsigned lds) {
    asm volatile("s_mov_b32 m0, %0\n\ts_nop 0\n\tbuffer_load_dwordx4 %1, %2, %3 offen lds" ::"s"(lds), "v"(voff), "s"(r), "s"(soff) : "memory");
}
DI void dma_seg(rsrc_t rw, rsrc_t rx, unsigned vw, unsigned vx0, unsigned vx1, unsigned sw, unsigned sx, unsigned mw, unsigned mx) {
#define XP(v) "s_addk_i32 m0, 0x400\n\ts_addk_i32 %1, 0x1c00\n\tbuffer_load_dwordx4 " v ", %6, %1 offen lds\n\t"
    asm volatile("s_mov_b32 m0, %7\n\ts_nop 0\n\tbuffer_load_dwordx4 %2, %5, %0 offen lds\n\t"
                 "s_addk_i32 m0, 0x1000\n\ts_addk_i32 %0, 0x380\n\tbuffer_load_dwordx4 %2, %5, %0 offen lds\n\t"
                 "s_mov_b32 m0, %8\n\ts_nop 0\n\tbuffer_load_dwordx4 %3, %6, %1 offen lds\n\t"
                 XP("%4") XP("%3") XP("%4") XP("%3") XP("%4") XP("%3") XP("%4")
                 : "+s"(sw), "+s"(sx) : "v"(vw), "v"(vx0), "v"(vx1), "s"(rw), "s"(rx), "s"(mw), "s"(mx) : "scc", "memory");
#undef XP
}
template <int N> DI void dma_rows(rsrc_t r, unsigned v, unsigned so, unsigned m) {
    static_assert(N == 2 || N == 6 || N == 8, "");
#define RP "s_addk_i32 m0, 0x400\n\ts_addk_i32 %0, 0x1c00\n\tbuffer_load_dwordx4 %1, %2, %0 offen lds\n\t"
#define R0 "s_mov_b32 m0, %3\n\ts_nop 0\n\tbuffer_load_dwordx4 %1, %2, %0 offen lds\n\t"
    if (N == 2) asm volatile(R0 RP : "+s"(so) : "v"(v), "s"(r), "s"(m) : "scc", "memory");
    if (N == 6) asm volatile(R0 RP RP RP RP RP : "+s"(so) : "v"(v), "s"(r), "s"(m) : "scc", "memory");
    if (N == 8) asm volatile(R0 RP RP RP RP RP RP RP : "+s"(so) : "v"(v), "s"(r), "s"(m) : "scc", "memory");
#undef RP
#undef R0
}
DI u32x4 load_sc1_asm(rsrc_t r, unsigned voff, unsigned soff) {
    u32x4 v; asm volatile("buffer_load_dwordx4 %0, %1, %2, %3 offen sc1" : "=&v"(v) : "v"(voff), "s"(r), "s"(soff) : "memory"); return v;
}
DI unsigned umax3(unsigned a, unsigned b, unsigned c) { const unsigned t = a > b ? a : b; return t > c ? t : c; }
DI void dma4(rsrc_t r, unsigned voff, unsigned soff, unsigned lds) {
    asm volatile("s_mov_b32 m0, %0\n\ts_nop 0\n\tbuffer_load_dword %1, %2, %3 offen lds" ::"s"(lds), "v"(voff), "s"(r), "s"(soff) : "memory");
}
#define PF_CODE_LINES 168
DI void gdma4(const void* a, unsigned lds) {
    asm volatile("s_mov_b32 m0, %0\n\ts_nop 0\n\tglobal_load_lds_dword %1, off" ::"s"(lds), "v"(a) : "memory");
}
#define PF_STAG 0
#define PF_WQ_SEG 1
#define PF_WO_SEG 2
#define WAIT_VM(n) asm volatile("s_waitcnt vmcnt(" #n ")" ::: "memory")
#define WAIT_VM_LGKM_BAR(n) asm volatile("s_waitcnt vmcnt(" #n ") lgkmcnt(0)\n\ts_barrier" ::: "memory")
#define LGKM_BAR() asm volatile("s_waitcnt lgkmcnt(0)\n\ts_barrier" ::: "memory")

extern __shared__ __attribute__((aligned(16))) char smem_dyn[];
DI LAS char* lds_at(unsigned off) { return (LAS char*)(uintptr_t)off; }
DI bf16x8 lds_b128(unsigned a) { return *(const LAS bf16x8*)lds_at(a); }
DI s16x4 lds_tr(unsigned a) { return __builtin_bit_cast(s16x4, __builtin_amdgcn_ds_read_tr16_b64_v4i16((LAS v4i16_t*)lds_at(a))); }
DI bf16x8 tr_frag(unsigned a0, unsigned a1) { const s16x4 lo = lds_tr(a0), hi = lds_tr(a1); return __builtin_shufflevector(lo, hi, 0, 1, 2, 3, 4, 5, 6, 7); }
DI unsigned cvtpk(float lo, float hi) { const f32x2 v = {lo, hi}; return __builtin_bit_cast(unsigned, __builtin_convertvector(v, bf2_t)); }
DI bf16x8 pack8(const f32x16& x, int s) {
    const u32x4 p = {cvtpk(x[8 * s], x[8 * s + 1]), cvtpk(x[8 * s + 2], x[8 * s + 3]), cvtpk(x[8 * s + 4], x[8 * s + 5]), cvtpk(x[8 * s + 6], x[8 * s + 7])};
    return __builtin_bit_cast(bf16x8, p);
}
DI u64 uniform64(u64 v) { return (u64)rfl((unsigned)v) | ((u64)rfl((unsigned)(v >> 32)) << 32); }
DI float addf(float a, float b) { float r; asm("v_add_f32 %0, %1, %2" : "=v"(r) : "v"(a), "v"(b)); return r; }
DI float swap_lo(float v) { auto r = __builtin_amdgcn_permlane32_swap(__float_as_uint(v), __float_as_uint(v), false, false); return __uint_as_float(r[0]); }
DI float swap_hi(float v) { auto r = __builtin_amdgcn_permlane32_swap(__float_as_uint(v), __float_as_uint(v), false, false); return __uint_as_float(r[1]); }
DI float xhalf_max(float v) { auto r = __builtin_amdgcn_permlane32_swap(__float_as_uint(v), __float_as_uint(v), false, false); return __builtin_fmaxf(__uint_as_float(r[0]), __uint_as_float(r[1])); }
DI float xhalf_min(float v) { auto r = __builtin_amdgcn_permlane32_swap(__float_as_uint(v), __float_as_uint(v), false, false); return __builtin_fminf(__uint_as_float(r[0]), __uint_as_float(r[1])); }
DI float xhalf_sum(float v) { auto r = __builtin_amdgcn_permlane32_swap(__float_as_uint(v), __float_as_uint(v), false, false); return __uint_as_float(r[0]) + __uint_as_float(r[1]); }
constexpr float L2E = 1.4426950408889634f;
constexpr unsigned ONE2 = 0x3F803F80u;
constexpr unsigned SPIN_CAP = 1u << 16;

#define STAMP_DECL do { } while (0)
#define STAMP(i) do { } while (0)
#define STAMPX(i, x) do { } while (0)
#define STAMP_FLUSH() do { } while (0)

DI void row_extrema(const f32x16& a, const f32x16& b, float (&cp)[2], float (&cn)[2]) {
    float mxa = __builtin_fmaxf(a[0], a[1]), mna = __builtin_fminf(a[0], a[1]), mxb = __builtin_fmaxf(b[0], b[1]), mnb = __builtin_fminf(b[0], b[1]);
#pragma unroll
    for (int i = 2; i < 14; i += 2) {
        mxa = __builtin_fmaxf(__builtin_fmaxf(mxa, a[i]), a[i + 1]); mxb = __builtin_fmaxf(__builtin_fmaxf(mxb, b[i]), b[i + 1]);
        mna = __builtin_fminf(__builtin_fminf(mna, a[i]), a[i + 1]); mnb = __builtin_fminf(__builtin_fminf(mnb, b[i]), b[i + 1]);
    }
    cp[0] = -xhalf_max(mxa) * L2E; cp[1] = -xhalf_max(mxb) * L2E;
    cn[0] = xhalf_min(mna) * L2E; cn[1] = xhalf_min(mnb) * L2E;
}
DI void exp_pair(const f32x16& pp, float cp, float cn, f32x2 (&ep)[7], f32x2 (&en)[7]) {
    const f32x2 l2 = {L2E, L2E}, cp2 = {cp, cp}, cn2 = {cn, cn};
#pragma unroll
    for (int i = 0; i < 7; ++i) {
        const f32x2 v = {pp[2 * i], pp[2 * i + 1]};
        const f32x2 a = __builtin_elementwise_fma(v, l2, cp2), b = __builtin_elementwise_fma(v, -l2, cn2);
        ep[i] = (f32x2){__builtin_amdgcn_exp2f(a[0]), __builtin_amdgcn_exp2f(a[1])};
        en[i] = (f32x2){__builtin_amdgcn_exp2f(b[0]), __builtin_amdgcn_exp2f(b[1])};
    }
}
DI void lds_wait_ge(unsigned addr, unsigned v) {
#pragma unroll 1
    for (unsigned spins = 0; spins < SPIN_CAP; ++spins) { if (*(volatile LAS unsigned*)lds_at(addr) >= v) break; }
    asm volatile("" ::: "memory");
}
DI void lds_set(unsigned addr) { asm volatile("s_waitcnt lgkmcnt(0)" ::: "memory"); *(volatile LAS unsigned*)lds_at(addr) = 1u; }
DI void lds_count(unsigned addr, int lane) {
    asm volatile("s_waitcnt lgkmcnt(0)" ::: "memory");
    if (lane == 0) __hip_atomic_fetch_add((LAS unsigned*)lds_at(addr), 1u, __ATOMIC_RELAXED, __HIP_MEMORY_SCOPE_WORKGROUP);
}
DI unsigned cvtpk2(f32x2 v) { return __builtin_bit_cast(unsigned, __builtin_convertvector(v, bf2_t)); }

DI void phi_tile(const f32x16& pk, unsigned base, int r, int h2) {
    float mx = __builtin_fmaxf(pk[0], pk[1]), mn = __builtin_fminf(pk[0], pk[1]);
#pragma unroll
    for (int i = 2; i < 14; i += 2) { mx = __builtin_fmaxf(__builtin_fmaxf(mx, pk[i]), pk[i + 1]); mn = __builtin_fminf(__builtin_fminf(mn, pk[i]), pk[i + 1]); }
    const float cp = -xhalf_max(mx) * L2E, cn = xhalf_min(mn) * L2E;
    f32x2 ep[7], en[7];
    exp_pair(pk, cp, cn, ep, en);
    f32x2 sp = ep[0], sn = en[0];
#pragma unroll
    for (int i = 1; i < 7; ++i) { sp += ep[i]; sn += en[i]; }
    const float ip = __builtin_amdgcn_rcpf(xhalf_sum(sp[0] + sp[1])), in = __builtin_amdgcn_rcpf(xhalf_sum(sn[0] + sn[1]));
    const f32x2 ip2 = {ip, ip}, in2 = {in, in};
#pragma unroll
    for (int g = 0; g < 4; ++g) {
        const unsigned a = base + r * 64 + 8 * ((2 * g + h2) ^ ((r >> 1) & 7));
        const u32x2 dp = {cvtpk2(ep[2 * g] * ip2), g < 3 ? cvtpk2(ep[(2 * g + 1) % 7] * ip2) : 0u};
        const u32x2 dn = {cvtpk2(en[2 * g] * in2), g < 3 ? cvtpk2(en[(2 * g + 1) % 7] * in2) : 0u};
        *(LAS u32x2*)lds_at(a) = dp;
        *(LAS u32x2*)lds_at(a + 2048) = dn;
    }
}

#define POLL_SLEEP 1
#define POLL_DEPTH 1
DI void wait_flags(const GAS u64* f, u64 tag) {
#pragma unroll 1
    for (unsigned spins = 0; spins < SPIN_CAP; ++spins) {
        const u64 v = __hip_atomic_load(f, __ATOMIC_RELAXED, __HIP_MEMORY_SCOPE_AGENT);
        if (__builtin_amdgcn_ballot_w64(v != tag) == 0) break;
        __builtin_amdgcn_s_sleep(POLL_SLEEP);
    }
    asm volatile("" ::: "memory");
}

#define SB() __builtin_amdgcn_sched_barrier(0)
#define PINV(x) asm volatile("" : "+v"(x))
struct Frag { bf16x8 x0, x1, fk, fv; };

DI void build_aphi(bf16x8 (&aphi)[2], int r, int h2) {
    const int hr = (r >> 2) & 1, reg = (r & 3) + 4 * (r >> 3), f = 14 * hr + (reg < 14 ? reg : 0);
    const unsigned a = L_WPHI + 2 * (4 * h2 * DH + f);
#pragma unroll
    for (int s = 0; s < 2; ++s)
#pragma unroll
        for (int jj = 0; jj < 8; ++jj) {
            const int d0 = 16 * s + 8 * (jj >> 2) + (jj & 3);
            const bool ok = reg < 14 && (d0 + 4 < DH || h2 == 0);
            const unsigned short v = *(const LAS unsigned short*)lds_at(a + 2 * d0 * DH);
            aphi[s][jj] = ok ? (short)v : (short)0;
        }
}

__global__ void __launch_bounds__(512, 2) hedgehog_mix_kernel(Params p) {
    const int tid = threadIdx.x, lane = tid & 63;
    const unsigned w = rfl(tid >> 6), cw = w & 3;
    if (w == 4) {
        u64 pc; asm volatile("s_getpc_b64 %0" : "=s"(pc));
        const unsigned l0 = (blockIdx.x >> 3) * 6 + (lane < 6 ? lane : 5), l = l0 < PF_CODE_LINES ? l0 : 0;
        gdma4((const char*)(pc & ~127ull) + l * 128, L_SCR + 256);
    }
    const int r = lane & 31, h2 = lane >> 5, i16 = lane & 15, q4 = i16 >> 2, p4 = i16 & 3, blk16 = (lane >> 4) & 1;
    const unsigned xcd = blockIdx.x & 7, j = blockIdx.x >> 3;
    const unsigned tb = (xcd & 1) * 4 + (j & 3), bh = (xcd >> 1) * 8 + (j >> 2), br = bh >> 4, h = bh & 15;
    const unsigned og = xcd * 4 + j / 7, oc = j % 7;
    const bool has_out = j < 28;
    const unsigned rot = ROT == 1 ? ((j >> 2) + 2 * (j & 3)) % 7 : ROT == 2 ? (2 * (j & 3)) % 7 : ROT == 3 ? (2 * (j & 3) + (j >> 3)) % 7
                       : ROT == 4 ? (2 * (j & 3) + (j >> 4)) % 7 : ROT == 5 ? (2 * (j >> 2) + 2 * (j & 3)) % 7 : ROT == 6 ? ((j >> 2) + 3 * (j & 3)) % 7 : 0;
    auto seg = [&](int n) { const unsigned t = rot + n; return t >= 7 ? t - 7 : t; };
#define PIN_ARGS() asm volatile("" : "+s"(p.x), "+s"(p.wqkvA), "+s"(p.wqkvB), "+s"(p.wphiA), "+s"(p.wphiB), "+s"(p.woA), "+s"(p.woB), "+s"(p.gate), "+s"(p.out), "+s"(p.ws))
    PIN_ARGS();
    STAMP_DECL;
    STAMP(0);
    const rsrc_t rws = mk_rsrc(p.ws, WS_END);
    const f32x16 zero16 = {};
    const int cq = 4 * blk16 + p4;
    const unsigned rowpart = (8 * h2 + q4) * 64;
    const unsigned coloff = (8 * (h & 1) + 8 * cq) & 63;
    const unsigned laneX = cw * 8192 + r * 128 + 16 * (h2 ^ ((r >> 1) & 7));

    if (__builtin_expect(w >= 4, 1)) {
        const rsrc_t rx = mk_rsrc(p.x, S_ * XROW);
        const rsrc_t rw = mk_rsrc(br ? p.wqkvB : p.wqkvA, D_ * WROW);
        const unsigned vx0 = (lane >> 3) * XROW + 16 * ((lane & 7) ^ (lane >> 4));
        const unsigned vx1 = (lane >> 3) * XROW + 16 * ((lane & 7) ^ (4 + (lane >> 4)));
        const unsigned vw = (lane >> 2) * WROW + 16 * (lane & 3);
        const unsigned sx = (tb * 256 + cw * 64) * XROW;
        const unsigned sw = cw * 16 * WROW + h * 56 - 8 * (h & 1);
        auto issue = [&](int s) {
            dma_seg(rw, rx, vw, vx0, vx1, sw + seg(s) * (64 * WROW) + 896, sx + seg(s) * 128, slot_of(s) + SLOT_W + cw * 1024, slot_of(s) + cw * 8192);
        };
        bf16x8 xs[5][4][2];
        auto grab = [&](int s) {
#pragma unroll
            for (int ks = 0; ks < 4; ++ks) {
                const unsigned a = (laneX ^ (32 * ks)) + slot_of(s);
                xs[s][ks][0] = lds_b128(a); xs[s][ks][1] = lds_b128(a + 4096);
            }
        };
        STAMP(11);
        issue(0); issue(1);
        u64 ev;
        asm volatile("global_load_dwordx2 %0, %1, %2 sc1" : "=&v"(ev) : "v"(0u), "s"(p.ws) : "memory");
        STAMP(1);
        WAIT_VM_LGKM_BAR(11);
        STAMP(2);
        grab(0); issue(2);
        __builtin_amdgcn_raw_buffer_store_b128((u32x4){~0u, ~0u, ~0u, ~0u}, rws, lane * 16, WS_KVP + ((bh * 8 + tb) * 4 + cw) * 1024, 16);
        asm volatile("s_waitcnt vmcnt(11) lgkmcnt(0)\n\ts_barrier" : "+v"(ev) :: "memory");
        const u64 tag = uniform64(ev) + 1;
        grab(1); issue(3); WAIT_VM_LGKM_BAR(10);
        if (lane == 0) __hip_atomic_store((GAS u64*)((char*)p.ws + WS_F1 + (bh * 4 + cw) * F1_GRP + tb * FS), tag, __ATOMIC_RELAXED, __HIP_MEMORY_SCOPE_AGENT);
        grab(2); issue(4); WAIT_VM_LGKM_BAR(11);
        grab(3); issue(5); WAIT_VM_LGKM_BAR(10);
        grab(4); issue(6); WAIT_VM_LGKM_BAR(10);
        WAIT_VM_LGKM_BAR(0);
        STAMP(3);
        const GAS u64* const f1p = (const GAS u64*)((const char*)p.ws + WS_F1 + (bh * 4 + cw) * F1_GRP + (lane & 7) * FS);
        u64 f1v;
        asm volatile("global_load_dwordx2 %0, %1, off sc1" : "=&v"(f1v) : "v"(f1p) : "memory");
#pragma unroll
        for (int s = 0; s < 7; ++s) { const unsigned sg = seg((s + 5) % 7); dma16(rw, vw, sw + sg * (64 * WROW), L_WQ + sg * 4096 + cw * 1024); }
        bf16x8 aphi[2];
        build_aphi(aphi, r, h2);
        PINV(aphi[0]); PINV(aphi[1]);
        asm volatile("s_waitcnt vmcnt(7)" : "+v"(f1v) :: "memory");
        if (__builtin_amdgcn_ballot_w64(f1v != tag) != 0) wait_flags(f1p, tag);
        STAMP(4);
        WAIT_VM_LGKM_BAR(0);
        STAMP(5);
        f32x16 qT[2] = {zero16, zero16};
        {
            const unsigned aq_real = L_WQ + rowpart + coloff, aq_pad = L_GT + rowpart;
            bf16x8 fq[3], xl[XLD + 1][2];
            auto seg_of = [](int n) { return n < 8 ? 5 + (n >> 2) : (n - 8) >> 2; };
            unsigned aQ = 0;
            auto rdq = [&](int n) {
                if ((n & 3) == 0) { const unsigned sg = seg(seg_of(n)); aQ = cq == 7 ? aq_pad + 8 * sg : aq_real + 4096 * sg; PINV(aQ); }
                fq[n % 3] = tr_frag(aQ + (n & 3) * 1024, aQ + (n & 3) * 1024 + 256);
            };
            auto rdx = [&](int n) { const unsigned a = (laneX ^ (32 * (n & 3))) + slot_of(seg_of(n)); xl[n % (XLD + 1)][0] = lds_b128(a); xl[n % (XLD + 1)][1] = lds_b128(a + 4096); };
            rdq(0); rdx(0); rdq(1);
            if (XLD == 2) rdx(1);
            SB();
#pragma unroll
            for (int n = 0; n < 28; ++n) {
                const int s = seg_of(n), ks = n & 3;
                if (n + XLD < 8) rdx(n + XLD);
                if (n + 2 < 28) rdq(n + 2);
                SB();
                qT[0] = MFMA(fq[n % 3], n >= 8 ? xs[n >= 8 ? s : 0][ks][0] : xl[n % (XLD + 1)][0], qT[0]);
                qT[1] = MFMA(fq[n % 3], n >= 8 ? xs[n >= 8 ? s : 0][ks][1] : xl[n % (XLD + 1)][1], qT[1]);
                SB();
                if (n == 7) {
                    asm volatile("s_waitcnt lgkmcnt(0)" ::: "memory");
                    *(volatile LAS unsigned*)lds_at(L_XFREE + 4 * cw) = 1u;
                }
            }
        }
        STAMP(6);
        bf16x8 eq[2][2][2];
        float gs[2];
        {
            f32x16 pq[2];
            const bf16x8 q00 = pack8(qT[0], 0), q10 = pack8(qT[1], 0);
            pq[0] = MFMA(aphi[0], q00, zero16); pq[1] = MFMA(aphi[0], q10, zero16);
            const bf16x8 q01 = pack8(qT[0], 1), q11 = pack8(qT[1], 1);
            pq[0] = MFMA(aphi[1], q01, pq[0]); pq[1] = MFMA(aphi[1], q11, pq[1]);
#pragma unroll
            for (int b = 0; b < 2; ++b) {
                const float lg = swap_hi(qT[b][12]);
                gs[b] = __builtin_amdgcn_rcpf(1.f + __builtin_amdgcn_exp2f(br ? lg * L2E : -lg * L2E));
                PINV(gs[b]);
            }
            float cp[2], cn[2];
            row_extrema(pq[0], pq[1], cp, cn);
            PINV(cp[0]); PINV(cp[1]); PINV(cn[0]); PINV(cn[1]);
#pragma unroll
            for (int b = 0; b < 2; ++b) {
                const f32x2 l2 = {L2E, L2E}, cp2 = {cp[b], cp[b]}, cn2 = {cn[b], cn[b]};
                unsigned pe[8], ne[8];
#pragma unroll
                for (int i = 0; i < 7; ++i) {
                    const f32x2 v = {pq[b][2 * i], pq[b][2 * i + 1]};
                    const f32x2 a = __builtin_elementwise_fma(v, l2, cp2), c = __builtin_elementwise_fma(v, -l2, cn2);
                    pe[i] = cvtpk2((f32x2){__builtin_amdgcn_exp2f(a[0]), __builtin_amdgcn_exp2f(a[1])});
                    ne[i] = cvtpk2((f32x2){__builtin_amdgcn_exp2f(c[0]), __builtin_amdgcn_exp2f(c[1])});
                }
                pe[7] = 0; ne[7] = 0;
                eq[b][0][0] = __builtin_bit_cast(bf16x8, (u32x4){pe[0], pe[1], pe[2], pe[3]}); eq[b][0][1] = __builtin_bit_cast(bf16x8, (u32x4){pe[4], pe[5], pe[6], pe[7]});
                eq[b][1][0] = __builtin_bit_cast(bf16x8, (u32x4){ne[0], ne[1], ne[2], ne[3]}); eq[b][1][1] = __builtin_bit_cast(bf16x8, (u32x4){ne[4], ne[5], ne[6], ne[7]});
            }
        }
#pragma unroll
        for (int b = 0; b < 2; ++b) asm volatile("" : "+v"(eq[b][0][0]), "+v"(eq[b][0][1]), "+v"(eq[b][1][0]), "+v"(eq[b][1][1]), "+v"(gs[b]));
        STAMP(7);
        LGKM_BAR();
        __builtin_amdgcn_s_setprio(3);
        STAMP(8);
        {
            bf16x8 akv[4];
#pragma unroll
            for (int f = 0; f < 4; ++f) akv[f] = lds_b128(L_KVA + f * 1024 + lane * 16);
            f32x16 np[2], nn[2];
#pragma unroll
            for (int b = 0; b < 2; ++b) { np[b] = MFMA(akv[0], eq[b][0][0], zero16); nn[b] = MFMA(akv[2], eq[b][1][0], zero16); }
#pragma unroll
            for (int b = 0; b < 2; ++b) { np[b] = MFMA(akv[1], eq[b][0][1], np[b]); nn[b] = MFMA(akv[3], eq[b][1][1], nn[b]); }
            SB();
            const unsigned so = rfl(WS_O + (((tb * 4 + cw) * 32 + bh) * 4) * 1024);
#pragma unroll
            for (int b = 0; b < 2; ++b) {
                const float ia = __builtin_amdgcn_rcpf(np[b][15]), ib = __builtin_amdgcn_rcpf(nn[b][15]);
                const float den = np[b][14] * ia + nn[b][14] * ib + 1e-6f;
                const float sc = gs[b] * __builtin_amdgcn_rcpf(den);
                const float ca = ia * sc, cb = ib * sc;
                float o[14];
#pragma unroll
                for (int i = 0; i < 14; ++i) o[i] = np[b][i] * ca + nn[b][i] * cb;
                const u32x4 o0 = {cvtpk(o[0], o[1]), cvtpk(o[2], o[3]), cvtpk(o[4], o[5]), cvtpk(o[6], o[7])};
                const u32x4 o1 = {cvtpk(o[8], o[9]), cvtpk(o[10], o[11]), cvtpk(o[12], o[13]), 0u};
                __builtin_amdgcn_raw_buffer_store_b128(o0, rws, lane * 16, so + b * 2048, 16);
                __builtin_amdgcn_raw_buffer_store_b128(o1, rws, lane * 16, so + b * 2048 + 1024, 16);
            }
        }
        STAMP(9);
        if (has_out) {
            const unsigned vwo = (lane >> 3) * XROW + 16 * ((lane & 7) ^ (4 * ((lane >> 4) & 1)));
            dma_rows<8>(mk_rsrc(p.woB, D_ * XROW), vwo, (640 - 448 + 64 * cw) * XROW + oc * 128, L_WO + (640 + 64 * cw) * 128);
            WAIT_VM(8);
        } else WAIT_VM(0);
        if (lane == 0) __hip_atomic_store((GAS u64*)((char*)p.ws + WS_F2 + ((tb * 4 + cw) * 2 + br) * F2_GRP + h * FS), tag, __ATOMIC_RELAXED, __HIP_MEMORY_SCOPE_AGENT);
        STAMP(10);
        if (has_out) {
            if (GATE == 1) lds_count(L_FSET, lane);
            WAIT_VM(1);
            if (lane == 0) __hip_atomic_fetch_add((LAS unsigned*)lds_at(L_WOCNT), 1u, __ATOMIC_RELAXED, __HIP_MEMORY_SCOPE_WORKGROUP);
        }
        STAMP_FLUSH();
        return;
    }

    u64 tag;
    {
        const rsrc_t rg = mk_rsrc(p.gate, XROW);
        const rsrc_t rp = mk_rsrc(br ? p.wphiB : p.wphiA, 16 * DH * DH * 2);
        unsigned g0, g1; u32x2 wp;
        u64 ev;
        asm volatile("global_load_dwordx2 %0, %1, %2 sc1" : "=&v"(ev) : "v"(0u), "s"(p.ws) : "memory");
        asm volatile("buffer_load_ushort %0, %3, %4, 0 offen\n\tbuffer_load_ushort %1, %3, %4, 0 offen offset:512\n\tbuffer_load_dwordx2 %2, %5, %6, %7 offen"
                     : "=&v"(g0), "=&v"(g1), "=&v"(wp) : "v"(tid * 2), "s"(rg), "v"(tid * 8), "s"(rp), "s"(h * (DH * DH * 2)) : "memory");
        asm volatile("s_waitcnt vmcnt(0) lgkmcnt(0)" : "+v"(g0), "+v"(g1), "+v"(wp), "+v"(ev) :: "memory");
        tag = uniform64(ev) + 1;
        *(LAS u32x2*)lds_at(L_GT + (tid & 63) * 64 + (tid >> 6) * 8) = (u32x2){g0 & 0xffffu, 0u};
        *(LAS u32x2*)lds_at(L_GT + (tid & 63) * 64 + (4 + (tid >> 6)) * 8) = (u32x2){g1 & 0xffffu, 0u};
        *(LAS u32x2*)lds_at(L_WPHI + tid * 8) = wp;
        if (tid < 20) *(volatile LAS unsigned*)lds_at(L_XFREE + 4 * tid) = 0u;
    }
    __builtin_amdgcn_s_setprio(2);
    LGKM_BAR();
    STAMP(2);
    bf16x8 aphi[2];
    build_aphi(aphi, r, h2);
    PINV(aphi[0]); PINV(aphi[1]);
    const unsigned laneW = SLOT_W + rowpart + coloff;
    f32x16 vinit;
#pragma unroll
    for (int i = 0; i < 16; ++i) vinit[i] = r == 28 ? 1.f : 0.f;
    PINV(vinit);
    f32x16 kT[2] = {zero16, zero16}, vv[2] = {vinit, vinit};
    {
        unsigned aX[4], aW, aV;
        auto addr = [&](int s) {
#pragma unroll
            for (int ks = 0; ks < 4; ++ks) { aX[ks] = (laneX ^ (32 * ks)) + slot_of(s); PINV(aX[ks]); }
            aW = laneW + slot_of(s); PINV(aW);
            aV = cq == 7 ? L_GT + rowpart + 56 : aW + 4096; PINV(aV);
        };
        Frag F[2];
#define RD_X(f, ks) do { f.x0 = lds_b128(aX[ks]); } while (0)
#define RD_X1(f, ks) do { f.x1 = lds_b128(aX[ks] + 4096); } while (0)
#define RD_K(f, ks) do { f.fk = tr_frag(aW + ks * 1024, aW + ks * 1024 + 256); } while (0)
#define RD_V(f, ks) do { f.fv = tr_frag(aV + ks * 1024, aV + ks * 1024 + 256); } while (0)
        addr(0);
        RD_X(F[0], 0); RD_K(F[0], 0); RD_X1(F[0], 0); RD_V(F[0], 0); SB();
#pragma unroll
        for (int n = 0; n < 28; ++n) {
            Frag& c = F[n & 1];
            Frag& x = F[(n + 1) & 1];
            const int nks = (n + 1) & 3;
            const bool more = n + 1 < 28;
            kT[0] = MFMA(c.fk, c.x0, kT[0]); SB();
            if (more && nks == 0) { LGKM_BAR(); addr((n + 1) >> 2); SB(); }
            if (more) { RD_X(x, nks); RD_K(x, nks); } SB();
            kT[1] = MFMA(c.fk, c.x1, kT[1]); SB();
            if (more) { RD_X1(x, nks); } SB();
            vv[0] = MFMA(c.x0, c.fv, vv[0]); SB();
            if (more) { RD_V(x, nks); } SB();
            vv[1] = MFMA(c.x1, c.fv, vv[1]); SB();
        }
    }
    STAMP(3);

    const unsigned phiW = L_PHI + cw * 8192;
    const unsigned pt0 = 4 * h2 + q4;
    const unsigned pa0 = phiW + pt0 * 64 + 8 * (cq ^ ((pt0 >> 1) & 7));
    const unsigned pa1 = (pa0 ^ 32) + 8 * 64;
    f32x16 kvp = zero16, kvn = zero16;
    f32x16 pk[2];
    {
        const bf16x8 k00 = pack8(kT[0], 0), k10 = pack8(kT[1], 0);
        pk[0] = MFMA(aphi[0], k00, zero16); pk[1] = MFMA(aphi[0], k10, zero16);
        const bf16x8 k01 = pack8(kT[0], 1), k11 = pack8(kT[1], 1);
        pk[0] = MFMA(aphi[1], k01, pk[0]); pk[1] = MFMA(aphi[1], k11, pk[1]);
    }
    bf16x8 vp[2][2];
#pragma unroll
    for (int b = 0; b < 2; ++b)
#pragma unroll
        for (int s = 0; s < 2; ++s) { vp[b][s] = pack8(vv[b], s); PINV(vp[b][s]); }
    SB();
#pragma unroll
    for (int b = 0; b < 2; ++b) {
        phi_tile(pk[b], phiW + b * 4096, r, h2);
#pragma unroll
        for (int s = 0; s < 2; ++s) {
            const unsigned o = b * 4096 + s * 1024;
            kvp = MFMA(tr_frag(pa0 + o, pa1 + o), vp[b][s], kvp);
            kvn = MFMA(tr_frag(pa0 + o + 2048, pa1 + o + 2048), vp[b][s], kvn);
        }
    }
    asm volatile("s_waitcnt lgkmcnt(0)" ::: "memory");
#pragma unroll
    for (int f = 0; f < 4; ++f)
#pragma unroll
        for (int hh = 0; hh < 2; ++hh) {
            const f32x16& t = f < 2 ? kvp : kvn;
            const int o = 8 * (f & 1) + 4 * hh;
            *(LAS f32x4*)lds_at(phiW + f * 2048 + hh * 1024 + lane * 16) = (f32x4){t[o], t[o + 1], t[o + 2], t[o + 3]};
        }
    STAMP(4);
    LGKM_BAR();
    STAMP(5);
    {
        f32x4 t[4][2];
#pragma unroll
        for (int u = 0; u < 4; ++u) {
            t[u][0] = *(const LAS f32x4*)lds_at(L_PHI + u * 8192 + cw * 2048 + lane * 16);
            t[u][1] = *(const LAS f32x4*)lds_at(L_PHI + u * 8192 + cw * 2048 + 1024 + lane * 16);
        }
        SB();
        float a[8];
#pragma unroll
        for (int i = 0; i < 8; ++i) {
            float x01 = addf(t[0][i >> 2][i & 3], t[1][i >> 2][i & 3]), x23 = addf(t[2][i >> 2][i & 3], t[3][i >> 2][i & 3]);
            a[i] = addf(x01, x23);
        }
        u32x4 o = {cvtpk(a[0], a[1]), cvtpk(a[2], a[3]), cvtpk(a[4], a[5]), cvtpk(a[6], a[7])};
        STAMPX(1, o);
        __builtin_amdgcn_raw_buffer_store_b128(o, rws, lane * 16, WS_KVP + ((bh * 8 + tb) * 4 + cw) * 1024, 16);
    }
    bf16x8 sel;
    {
        const int jj = (r & 3) + 4 * (r >> 3);
        const bool on = r < 16 && ((r >> 2) & 1) == h2;
        const unsigned one = on ? (0x3F80u << (16 * (jj & 1))) : 0u;
        sel = __builtin_bit_cast(bf16x8, (u32x4){(jj >> 1) == 0 ? one : 0u, (jj >> 1) == 1 ? one : 0u, (jj >> 1) == 2 ? one : 0u, (jj >> 1) == 3 ? one : 0u});
        PINV(sel);
    }
    {
        const rsrc_t roA = mk_rsrc(p.woA, D_ * XROW), roB = mk_rsrc(p.woB, D_ * XROW);
        const unsigned vwo = (lane >> 3) * XROW + 16 * ((lane & 7) ^ (4 * ((lane >> 4) & 1)));
        dma_rows<2>(roA, vwo, (256 + 16 * cw) * XROW + oc * 128, L_WO + (256 + 16 * cw) * 128);
        dma_rows<2>(roB, vwo, (576 - 448 + 16 * cw) * XROW + oc * 128, L_WO + (576 + 16 * cw) * 128);
#pragma unroll 1
        for (unsigned spins = 0; spins < SPIN_CAP; ++spins) { if (*(volatile LAS unsigned*)lds_at(L_XFREE + 4 * cw) != 0u) break; }
        dma_rows<6>(roA, vwo, 64 * cw * XROW + oc * 128, L_WO + 64 * cw * 128);
        const rsrc_t roC = cw < 2 ? roA : roB;
        const unsigned R = 320 + 64 * cw, Rg = cw < 2 ? R : R - 448;
        const int hr = (r >> 2) & 1, reg = (r & 3) + 4 * (r >> 3);
        const int col = reg < 14 ? 14 * hr + reg : 28;
        const unsigned vo = (h2 * 32 + col) * 16;
        u32x4 part[8];
        STAMP(9);
#pragma unroll
        for (int u = 0; u < 8; ++u) part[u] = load_sc1_asm(rws, vo, WS_KVP + ((bh * 8 + u) * 4 + cw) * 1024);
        dma_rows<2>(roA, vwo, (64 * cw + 48) * XROW + oc * 128, L_WO + (64 * cw + 48) * 128);
        dma_rows<8>(roC, vwo, Rg * XROW + oc * 128, L_WO + R * 128);
        asm volatile("s_waitcnt vmcnt(10)" : "+v"(part[0]), "+v"(part[1]), "+v"(part[2]), "+v"(part[3]), "+v"(part[4]), "+v"(part[5]), "+v"(part[6]), "+v"(part[7]) :: "memory");
        STAMP(6);
#pragma unroll 1
        for (unsigned spins = 0; spins < SPIN_CAP; ++spins) {
            unsigned m = 0;
#pragma unroll
            for (int u = 0; u < 8; ++u) { m = umax3(m, part[u][0], part[u][1]); m = umax3(m, part[u][2], part[u][3]); }
            if (__builtin_amdgcn_ballot_w64(m == ~0u) == 0) break;
#pragma unroll
            for (int u = 0; u < 8; ++u) part[u] = load_sc1_asm(rws, vo, WS_KVP + ((bh * 8 + u) * 4 + cw) * 1024);
            asm volatile("s_waitcnt vmcnt(0)" : "+v"(part[0]), "+v"(part[1]), "+v"(part[2]), "+v"(part[3]), "+v"(part[4]), "+v"(part[5]), "+v"(part[6]), "+v"(part[7]) :: "memory");
        }
        STAMP(7);
        SB();
        f32x16 acc = zero16;
#pragma unroll
        for (int u = 0; u < 8; ++u) acc = MFMA(sel, __builtin_bit_cast(bf16x8, part[u]), acc);
        u32x4 o = __builtin_bit_cast(u32x4, pack8(acc, 0));
        if (reg == 15) o = (u32x4){ONE2, ONE2, ONE2, ONE2};
        *(LAS u32x4*)lds_at(L_KVA + cw * 1024 + lane * 16) = o;
    }
    LGKM_BAR();
    STAMP(8);
    if (!has_out) { STAMP_FLUSH(); return; }
    WAIT_VM(0);
    if (lane == 0) __hip_atomic_fetch_add((LAS unsigned*)lds_at(L_WOCNT), 1u, __ATOMIC_RELAXED, __HIP_MEMORY_SCOPE_WORKGROUP);
    auto wait_wo = [&]() {
#pragma unroll 1
        for (unsigned spins = 0; spins < SPIN_CAP; ++spins) { if (*(volatile LAS unsigned*)lds_at(L_WOCNT) >= (WO2_BY_H ? 8u : 4u)) break; }
        asm volatile("" ::: "memory");
    };

    const unsigned tk = cw & 1, kh = cw >> 1;
    constexpr u64 HORD = 0xDB9642FECA875310ull;
    const GAS u64* f2 = (const GAS u64*)((const char*)p.ws + WS_F2 + (og * 2 + kh) * F2_GRP);
    f32x16 mine = zero16, other = zero16;
    {
        const unsigned colp = 32 * blk16 + 8 * p4;
        const unsigned r0 = kh * 448 + 14 * h2 + q4, r1 = kh * 448 + 14 * h2 + (q4 < 1 ? q4 : 1);
        unsigned a0 = L_WO + r0 * 128 + 64 * (kh ^ ((r0 >> 1) & 1)) + colp;
        unsigned a1 = L_WO + r1 * 128 + 64 * (kh ^ ((r1 >> 1) & 1)) + colp;
        unsigned a2 = a0 ^ 64, a3 = a1 ^ 64;
        asm volatile("" : "+v"(a0), "+v"(a1), "+v"(a2), "+v"(a3));
        u32x4 of[16][2];
        auto ld = [&](int i) {
            const unsigned hd = (unsigned)(HORD >> (4 * i)) & 15u;
#pragma unroll
            for (int s = 0; s < 2; ++s)
                of[i][s] = __builtin_amdgcn_raw_buffer_load_b128(rws, lane * 16, WS_O + (((og * 32 + kh * 16 + hd) * 2 + tk) * 2 + s) * 1024, 16);
        };
        bf16x8 A[2][4];
        auto rd = [&](bf16x8 (&f)[4], int i) {
            const unsigned o = ((unsigned)(HORD >> (4 * i)) & 15u) * 28 * 128;
            f[0] = tr_frag(a0 + o, a0 + o + 512); f[1] = tr_frag(a2 + o, a2 + o + 512);
            f[2] = tr_frag(a0 + o + 1024, a1 + o + 1536); f[3] = tr_frag(a2 + o + 1024, a3 + o + 1536);
        };
        auto step = [&](int i) {
            if (i + 1 < 16) rd(A[(i + 1) & 1], i + 1);
            SB();
            const bf16x8 b0 = __builtin_bit_cast(bf16x8, of[i][0]), b1 = __builtin_bit_cast(bf16x8, of[i][1]);
            mine = MFMA(A[i & 1][0], b0, mine); other = MFMA(A[i & 1][1], b0, other);
            mine = MFMA(A[i & 1][2], b1, mine); other = MFMA(A[i & 1][3], b1, other);
            SB();
        };
        if (GATE == 1) lds_wait_ge(L_FSET, 4); else wait_wo();
        STAMP(10);
        if (cw == 0) {
            wait_flags((const GAS u64*)((const char*)p.ws + WS_F2 + (og * 2 + ((lane >> 4) & 1)) * F2_GRP) + (FS / 8) * (lane & 15), tag);
            lds_set(L_F2OK);
        } else lds_wait_ge(L_F2OK, 1);
        STAMP(11);
        const unsigned orot = OROT ? (oc * 16) / 7 : 0;
        auto ldr = [&](int i) {
            const unsigned hd = (i + orot) & 15u;
#pragma unroll
            for (int s = 0; s < 2; ++s)
                of[i][s] = __builtin_amdgcn_raw_buffer_load_b128(rws, lane * 16, WS_O + (((og * 32 + kh * 16 + hd) * 2 + tk) * 2 + s) * 1024, 16);
        };
        auto rdr = [&](bf16x8 (&f)[4], int i) {
            const unsigned o = ((i + orot) & 15u) * (28 * 128);
            const unsigned b0 = a0 + o, b1 = a1 + o, b2 = a2 + o, b3 = a3 + o;
            f[0] = tr_frag(b0, b0 + 512); f[1] = tr_frag(b2, b2 + 512);
            f[2] = tr_frag(b0 + 1024, b1 + 1536); f[3] = tr_frag(b2 + 1024, b3 + 1536);
        };
#pragma unroll
        for (int i = 0; i < OD; ++i) ldr(i);
        SB();
        if (GATE == 1) wait_wo();
        rdr(A[0], 0); SB();
#pragma unroll
        for (int i = 0; i < 16; ++i) {
            if (i + OD < 16) ldr(i + OD);
            if (i + 1 < 16) rdr(A[(i + 1) & 1], i + 1);
            SB();
            const bf16x8 b0 = __builtin_bit_cast(bf16x8, of[i][0]), b1 = __builtin_bit_cast(bf16x8, of[i][1]);
            mine = MFMA(A[i & 1][0], b0, mine); other = MFMA(A[i & 1][1], b0, other);
            mine = MFMA(A[i & 1][2], b1, mine); other = MFMA(A[i & 1][3], b1, other);
            SB();
        }
    }
    STAMP(12);
#pragma unroll
    for (int g = 0; g < 4; ++g)
        *(LAS f32x4*)lds_at(L_PHI + cw * 4096 + g * 1024 + lane * 16) = (f32x4){other[4 * g], other[4 * g + 1], other[4 * g + 2], other[4 * g + 3]};
    LGKM_BAR();
    if (blockIdx.x == 0 && tid == 0) __hip_atomic_store((GAS u64*)((char*)p.ws + WS_EPOCH), tag, __ATOMIC_RELAXED, __HIP_MEMORY_SCOPE_AGENT);
    {
        unsigned d[8];
        f32x4 t[4];
#pragma unroll
        for (int g = 0; g < 4; ++g) t[g] = *(const LAS f32x4*)lds_at(L_PHI + (cw ^ 2) * 4096 + g * 1024 + lane * 16);
        SB();
#pragma unroll
        for (int g = 0; g < 4; ++g) {
            d[2 * g] = cvtpk(mine[4 * g] + t[g][0], mine[4 * g + 1] + t[g][1]);
            d[2 * g + 1] = cvtpk(mine[4 * g + 2] + t[g][2], mine[4 * g + 3] + t[g][3]);
        }
        const rsrc_t rout = mk_rsrc(p.out, S_ * XROW);
        const unsigned vo = r * XROW + h2 * 16, so = (og * 64 + tk * 32) * XROW + oc * 128 + kh * 64;
#pragma unroll
        for (int k = 0; k < 4; k += 2) {
            auto sx0 = __builtin_amdgcn_permlane32_swap(d[2 * k], d[2 * k + 2], false, false);
            auto sx1 = __builtin_amdgcn_permlane32_swap(d[2 * k + 1], d[2 * k + 3], false, false);
            const u32x4 o = {sx0[0], sx1[0], sx0[1], sx1[1]};
            __builtin_amdgcn_raw_buffer_store_b128(o, rout, vo, so + 16 * k, 0);
        }
    }
    STAMP(13);
    STAMP_FLUSH();
}

extern "C" void kernel_launch(void* const* d_in, const int* in_sizes, int n_in, void* d_out, int out_size, void* d_ws, size_t ws_size,
                              hipStream_t stream) {
    Params p;
    p.x = d_in[0]; p.wqkvA = d_in[1]; p.wphiA = d_in[2]; p.woA = d_in[3];
    p.wqkvB = d_in[5]; p.wphiB = d_in[6]; p.woB = d_in[7]; p.gate = d_in[8];
    p.out = d_out; p.ws = d_ws;
    (void)hipFuncSetAttribute((const void*)hedgehog_mix_kernel, hipFuncAttributeMaxDynamicSharedMemorySize, L_TOTAL);
    hedgehog_mix_kernel<<<256, 512, L_TOTAL, stream>>>(p);
}
```

```cpp
#include <hip/hip_runtime.h>
#include <stdint.h>

typedef short bf16x8 __attribute__((ext_vector_type(8)));
typedef short s16x4 __attribute__((ext_vector_type(4)));
typedef short v4i16_t __attribute__((ext_vector_type(4)));
typedef __bf16 bf2_t __attribute__((ext_vector_type(2)));
typedef float f32x2 __attribute__((ext_vector_type(2)));
typedef float f32x4 __attribute__((ext_vector_type(4)));
typedef float f32x16 __attribute__((ext_vector_type(16)));
typedef unsigned u32x2 __attribute__((ext_vector_type(2)));
typedef unsigned u32x4 __attribute__((ext_vector_type(4)));
typedef unsigned long long u64;
#define LAS __attribute__((address_space(3)))
#define GAS __attribute__((address_space(1)))
#define DI __device__ __forceinline__
#define MFMA(a, b, c) __builtin_amdgcn_mfma_f32_32x32x16_bf16((a), (b), (c), 0, 0, 0)

#define EARLYQ 0
#define B2N 10
#define PKFREE 0
#define LSLEEP 1
#define CPRIO 2
#define NSDELAY 0
#define OD 5
#define PF_CODE 1
#define PF_CODE_LINES 176

constexpr int S_ = 2048, D_ = 448, DH = 28;
constexpr unsigned XROW = D_ * 2;
constexpr unsigned WROW = 3 * D_ * 2;
constexpr unsigned FS = 128;
constexpr unsigned WS_EPOCH = 0;
constexpr unsigned WS_F1 = 4096;
constexpr unsigned WS_F2 = WS_F1 + 32 * 4 * 8 * FS;
constexpr unsigned WS_KVP = WS_F2 + 32 * 32 * FS;
constexpr unsigned WS_O = WS_KVP + 32 * 8 * 4 * 1024;
constexpr unsigned WS_END = WS_O + 32 * 32 * 4 * 1024;
constexpr unsigned WS_STAMP = 8u << 20;
constexpr unsigned SLOT = 40960, SLOT_W = 32768;
constexpr unsigned L_B = 0, L_A = SLOT, L_C = 2 * SLOT;
constexpr unsigned L_WQ = 3 * SLOT;
constexpr unsigned L_GT = L_WQ + 448 * 64;
constexpr unsigned L_WPHI = L_GT + 4096;
constexpr unsigned L_SCR = L_WPHI + 2048;
constexpr unsigned L_SYNC = L_SCR + 256;
constexpr unsigned L_XFREE = L_SYNC;
constexpr unsigned L_WOCNT = L_SYNC + 16;
constexpr unsigned L_REDCNT = L_SYNC + 20;
constexpr unsigned L_WQA = L_SYNC + 24, L_WQB = L_SYNC + 28;
constexpr unsigned L_QGO = L_SYNC + 48;
constexpr unsigned L_F2OK = L_SYNC + 32;
constexpr unsigned L_PHI = L_B;
constexpr unsigned L_KVA = L_B + SLOT_W;
constexpr unsigned L_WO = L_A;
constexpr unsigned L_TOTAL = 163840;
static_assert(L_SYNC + 64 <= L_TOTAL, "lds");
static_assert(L_WO + 896 * 128 == L_WPHI, "lds");
__host__ __device__ constexpr unsigned slot_of(int s) { return s % 3 == 0 ? L_A : (s % 3 == 1 ? L_B : L_C); }

struct Params {
    const void* x; const void* wqkvA; const void* wqkvB; const void* wphiA; const void* wphiB;
    const void* woA; const void* woB; const void* gate; void* out; void* ws;
};

typedef __amdgpu_buffer_rsrc_t rsrc_t;
DI rsrc_t mk_rsrc(const void* p, unsigned bytes) { return __builtin_amdgcn_make_buffer_rsrc((void*)p, (short)0, (int)bytes, 0x00020000); }
DI unsigned rfl(unsigned v) { return (unsigned)__builtin_amdgcn_readfirstlane((int)v); }
DI u64 uniform64(u64 v) { return (u64)rfl((unsigned)v) | ((u64)rfl((unsigned)(v >> 32)) << 32); }
DI void dma16(rsrc_t r, unsigned voff, unsigned soff, unsigned lds) {
    asm volatile("s_mov_b32 m0, %0\n\ts_nop 0\n\tbuffer_load_dwordx4 %1, %2, %3 offen lds" ::"s"(lds), "v"(voff), "s"(r), "s"(soff) : "memory");
}
DI void dma_seg(rsrc_t rw, rsrc_t rx, unsigned vw, unsigned vx0, unsigned vx1, unsigned sw, unsigned sx, unsigned mw, unsigned mx) {
#define XP(v) "s_addk_i32 m0, 0x400\n\ts_addk_i32 %1, 0x1c00\n\tbuffer_load_dwordx4 " v ", %6, %1 offen lds\n\t"
    asm volatile("s_mov_b32 m0, %7\n\ts_nop 0\n\tbuffer_load_dwordx4 %2, %5, %0 offen lds\n\t"
                 "s_addk_i32 m0, 0x1000\n\ts_addk_i32 %0, 0x380\n\tbuffer_load_dwordx4 %2, %5, %0 offen lds\n\t"
                 "s_mov_b32 m0, %8\n\ts_nop 0\n\tbuffer_load_dwordx4 %3, %6, %1 offen lds\n\t"
                 XP("%4") XP("%3") XP("%4") XP("%3") XP("%4") XP("%3") XP("%4")
                 : "+s"(sw), "+s"(sx) : "v"(vw), "v"(vx0), "v"(vx1), "s"(rw), "s"(rx), "s"(mw), "s"(mx) : "scc", "memory");
#undef XP
}
template <int N> DI void dma_rows(rsrc_t r, unsigned v, unsigned so, unsigned m) {
    static_assert(N == 2 || N == 3 || N == 5 || N == 6 || N == 8, "");
#define RP "s_addk_i32 m0, 0x400\n\ts_addk_i32 %0, 0x1c00\n\tbuffer_load_dwordx4 %1, %2, %0 offen lds\n\t"
#define R0 "s_mov_b32 m0, %3\n\ts_nop 0\n\tbuffer_load_dwordx4 %1, %2, %0 offen lds\n\t"
    if (N == 2) asm volatile(R0 RP : "+s"(so) : "v"(v), "s"(r), "s"(m) : "scc", "memory");
    if (N == 3) asm volatile(R0 RP RP : "+s"(so) : "v"(v), "s"(r), "s"(m) : "scc", "memory");
    if (N == 5) asm volatile(R0 RP RP RP RP : "+s"(so) : "v"(v), "s"(r), "s"(m) : "scc", "memory");
    if (N == 6) asm volatile(R0 RP RP RP RP RP : "+s"(so) : "v"(v), "s"(r), "s"(m) : "scc", "memory");
    if (N == 8) asm volatile(R0 RP RP RP RP RP RP RP : "+s"(so) : "v"(v), "s"(r), "s"(m) : "scc", "memory");
#undef RP
#undef R0
}
DI u32x4 load_sc1_asm(rsrc_t r, unsigned voff, unsigned soff) {
    u32x4 v; asm volatile("buffer_load_dwordx4 %0, %1, %2, %3 offen sc1" : "=&v"(v) : "v"(voff), "s"(r), "s"(soff) : "memory"); return v;
}
DI unsigned umax3(unsigned a, unsigned b, unsigned c) { const unsigned t = a > b ? a : b; return t > c ? t : c; }
DI void gdma4(const void* a, unsigned lds) {
    asm volatile("s_mov_b32 m0, %0\n\ts_nop 0\n\tglobal_load_lds_dword %1, off" ::"s"(lds), "v"(a) : "memory");
}
#define WAIT_VM(n) asm volatile("s_waitcnt vmcnt(" #n ")" ::: "memory")
#define WAIT_VM_LGKM_BAR(n) asm volatile("s_waitcnt vmcnt(" #n ") lgkmcnt(0)\n\ts_barrier" ::: "memory")
#define LGKM_BAR() asm volatile("s_waitcnt lgkmcnt(0)\n\ts_barrier" ::: "memory")
#define SB() __builtin_amdgcn_sched_barrier(0)
#define PINV(x) asm volatile("" : "+v"(x))

extern __shared__ __attribute__((aligned(16))) char smem_dyn[];
DI LAS char* lds_at(unsigned off) { return (LAS char*)(uintptr_t)off; }
DI bf16x8 lds_b128(unsigned a) { return *(const LAS bf16x8*)lds_at(a); }
DI s16x4 lds_tr(unsigned a) { return __builtin_bit_cast(s16x4, __builtin_amdgcn_ds_read_tr16_b64_v4i16((LAS v4i16_t*)lds_at(a))); }
DI bf16x8 tr_frag(unsigned a0, unsigned a1) { const s16x4 lo = lds_tr(a0), hi = lds_tr(a1); return __builtin_shufflevector(lo, hi, 0, 1, 2, 3, 4, 5, 6, 7); }
DI unsigned cvtpk(float lo, float hi) { const f32x2 v = {lo, hi}; return __builtin_bit_cast(unsigned, __builtin_convertvector(v, bf2_t)); }
DI bf16x8 pack8(const f32x16& x, int s) {
    const u32x4 p = {cvtpk(x[8 * s], x[8 * s + 1]), cvtpk(x[8 * s + 2], x[8 * s + 3]), cvtpk(x[8 * s + 4], x[8 * s + 5]), cvtpk(x[8 * s + 6], x[8 * s + 7])};
    return __builtin_bit_cast(bf16x8, p);
}
DI float addf(float a, float b) { float r; asm("v_add_f32 %0, %1, %2" : "=v"(r) : "v"(a), "v"(b)); return r; }
DI float swap_hi(float v) { auto r = __builtin_amdgcn_permlane32_swap(__float_as_uint(v), __float_as_uint(v), false, false); return __uint_as_float(r[1]); }
DI float xhalf_max(float v) { auto r = __builtin_amdgcn_permlane32_swap(__float_as_uint(v), __float_as_uint(v), false, false); return __builtin_fmaxf(__uint_as_float(r[0]), __uint_as_float(r[1])); }
DI float xhalf_min(float v) { auto r = __builtin_amdgcn_permlane32_swap(__float_as_uint(v), __float_as_uint(v), false, false); return __builtin_fminf(__uint_as_float(r[0]), __uint_as_float(r[1])); }
DI float xhalf_sum(float v) { auto r = __builtin_amdgcn_permlane32_swap(__float_as_uint(v), __float_as_uint(v), false, false); return __uint_as_float(r[0]) + __uint_as_float(r[1]); }
constexpr float L2E = 1.4426950408889634f;
constexpr unsigned ONE2 = 0x3F803F80u;
constexpr unsigned SPIN_CAP = 1u << 16;

#define STAMP_DECL do { } while (0)
#define STAMP(i) do { } while (0)
#define STAMPX(i, x) do { } while (0)
#define STAMP_FLUSH() do { } while (0)

DI void row_extrema(const f32x16& a, const f32x16& b, float (&cp)[2], float (&cn)[2]) {
    float mxa = __builtin_fmaxf(a[0], a[1]), mna = __builtin_fminf(a[0], a[1]), mxb = __builtin_fmaxf(b[0], b[1]), mnb = __builtin_fminf(b[0], b[1]);
#pragma unroll
    for (int i = 2; i < 14; i += 2) {
        mxa = __builtin_fmaxf(__builtin_fmaxf(mxa, a[i]), a[i + 1]); mxb = __builtin_fmaxf(__builtin_fmaxf(mxb, b[i]), b[i + 1]);
        mna = __builtin_fminf(__builtin_fminf(mna, a[i]), a[i + 1]); mnb = __builtin_fminf(__builtin_fminf(mnb, b[i]), b[i + 1]);
    }
    cp[0] = -xhalf_max(mxa) * L2E; cp[1] = -xhalf_max(mxb) * L2E;
    cn[0] = xhalf_min(mna) * L2E; cn[1] = xhalf_min(mnb) * L2E;
}
DI unsigned cvtpk2(f32x2 v) { return __builtin_bit_cast(unsigned, __builtin_convertvector(v, bf2_t)); }
DI void exp_pair(const f32x16& pp, float cp, float cn, f32x2 (&ep)[7], f32x2 (&en)[7]) {
#pragma unroll
    for (int i = 0; i < 7; ++i) {
        const f32x2 l2 = {L2E, L2E}, cp2 = {cp, cp}, cn2 = {cn, cn};
        const f32x2 v = {pp[2 * i], pp[2 * i + 1]};
        const f32x2 a = __builtin_elementwise_fma(v, l2, cp2), b = __builtin_elementwise_fma(v, -l2, cn2);
        ep[i] = (f32x2){__builtin_amdgcn_exp2f(a[0]), __builtin_amdgcn_exp2f(a[1])};
        en[i] = (f32x2){__builtin_amdgcn_exp2f(b[0]), __builtin_amdgcn_exp2f(b[1])};
    }
}
DI float sum14(const f32x2 (&e)[7]) {
    f32x2 s = e[0];
#pragma unroll
    for (int i = 1; i < 7; ++i) s += e[i];
    return s[0] + s[1];
}
DI unsigned scale_pack(f32x2 v, float s) {
    return cvtpk2(v * (f32x2){s, s});
}
DI void phi_write(const f32x2 (&ep)[7], const f32x2 (&en)[7], unsigned base, int r, int h2) {
    const float ip = __builtin_amdgcn_rcpf(xhalf_sum(sum14(ep))), in = __builtin_amdgcn_rcpf(xhalf_sum(sum14(en)));
#pragma unroll
    for (int g = 0; g < 4; ++g) {
        const unsigned a = base + r * 64 + 8 * ((2 * g + h2) ^ ((r >> 1) & 7));
        const u32x2 dp = {scale_pack(ep[2 * g], ip), g < 3 ? scale_pack(ep[(2 * g + 1) % 7], ip) : 0u};
        const u32x2 dn = {scale_pack(en[2 * g], in), g < 3 ? scale_pack(en[(2 * g + 1) % 7], in) : 0u};
        *(LAS u32x2*)lds_at(a) = dp;
        *(LAS u32x2*)lds_at(a + 2048) = dn;
    }
}
DI void build_aphi(bf16x8 (&aphi)[2], int r, int h2) {
    const int hr = (r >> 2) & 1, reg = (r & 3) + 4 * (r >> 3), f = 14 * hr + (reg < 14 ? reg : 0);
    const unsigned a = L_WPHI + 2 * (4 * h2 * DH + f);
#pragma unroll
    for (int s = 0; s < 2; ++s)
#pragma unroll
        for (int jj = 0; jj < 8; ++jj) {
            const int d0 = 16 * s + 8 * (jj >> 2) + (jj & 3);
            const bool ok = reg < 14 && (d0 + 4 < DH || h2 == 0);
            const unsigned short v = *(const LAS unsigned short*)lds_at(a + 2 * d0 * DH);
            aphi[s][jj] = ok ? (short)v : (short)0;
        }
}

DI void lds_wait_ge(unsigned addr, unsigned v) {
#pragma unroll 1
    for (unsigned spins = 0; spins < SPIN_CAP * 16; ++spins) {
        if (*(volatile LAS unsigned*)lds_at(addr) >= v) break;
        if (LSLEEP) __builtin_amdgcn_s_sleep(LSLEEP);
    }
    asm volatile("" ::: "memory");
}
DI void lds_set(unsigned addr) { asm volatile("s_waitcnt lgkmcnt(0)" ::: "memory"); *(volatile LAS unsigned*)lds_at(addr) = 1u; }
DI void lds_count(unsigned addr, int lane) {
    asm volatile("s_waitcnt lgkmcnt(0)" ::: "memory");
    if (lane == 0) __hip_atomic_fetch_add((LAS unsigned*)lds_at(addr), 1u, __ATOMIC_RELAXED, __HIP_MEMORY_SCOPE_WORKGROUP);
}
DI void wait_flags(const GAS u64* f, u64 tag) {
#pragma unroll 1
    for (unsigned spins = 0; spins < SPIN_CAP; ++spins) {
        const u64 v = __hip_atomic_load(f, __ATOMIC_RELAXED, __HIP_MEMORY_SCOPE_AGENT);
        if (__builtin_amdgcn_ballot_w64(v != tag) == 0) break;
        __builtin_amdgcn_s_sleep(1);
    }
    asm volatile("" ::: "memory");
}
DI void set_flag(void* ws, unsigned off, u64 tag, int lane) {
    if (lane == 0) __hip_atomic_store((GAS u64*)((char*)ws + off), tag, __ATOMIC_RELAXED, __HIP_MEMORY_SCOPE_AGENT);
}

#define DLWAIT() do { while (__builtin_amdgcn_s_memrealtime() - dl_t0 < 4000ull) __builtin_amdgcn_s_sleep(1); } while (0)
struct Frag { bf16x8 x0, x1, fk, fv; };

__global__ void __launch_bounds__(512, 2) hedgehog_mix_kernel(Params p) {
    unsigned long long dl_t0; asm volatile("s_memrealtime %0\n\ts_waitcnt lgkmcnt(0)" : "=s"(dl_t0) :: "memory");
    const int tid = threadIdx.x, lane = tid & 63;
    const unsigned w = rfl(tid >> 6), cw = w & 3;
    if (w == 4) {
        u64 pc; asm volatile("s_getpc_b64 %0" : "=s"(pc));
        const unsigned l0 = (blockIdx.x >> 3) * 6 + (lane < 6 ? lane : 5), l = l0 < PF_CODE_LINES ? l0 : 0;
        gdma4((const char*)(pc & ~127ull) + l * 128, L_SCR);
    }
    const int r = lane & 31, h2 = lane >> 5, i16 = lane & 15, q4 = i16 >> 2, p4 = i16 & 3, blk16 = (lane >> 4) & 1;
    const unsigned xcd = blockIdx.x & 7, j = blockIdx.x >> 3;
    const unsigned tb = (xcd & 1) * 4 + (j & 3), bh = (xcd >> 1) * 8 + (j >> 2), br = bh >> 4, h = bh & 15;
    const unsigned og = xcd * 4 + j / 7, oc = j % 7;
    const bool has_out = j < 28;
    const unsigned rot = ((j >> 2) + 2 * (j & 3)) % 7;
    auto seg = [&](int n) { const unsigned t = rot + n; return t >= 7 ? t - 7 : t; };
    asm volatile("" : "+s"(p.x), "+s"(p.wqkvA), "+s"(p.wqkvB), "+s"(p.wphiA), "+s"(p.wphiB), "+s"(p.woA), "+s"(p.woB), "+s"(p.gate), "+s"(p.out), "+s"(p.ws));
    STAMP_DECL;
    STAMP(0);
    const rsrc_t rws = mk_rsrc(p.ws, WS_END);
    const f32x16 zero16 = {};
    const int cq = 4 * blk16 + p4;
    const unsigned rowpart = (8 * h2 + q4) * 64;
    const unsigned coloff = (8 * (h & 1) + 8 * cq) & 63;
    const unsigned laneX = cw * 8192 + r * 128 + 16 * (h2 ^ ((r >> 1) & 7));
    const unsigned kvp_mine = WS_KVP + ((bh * 8 + tb) * 4 + cw) * 1024;
    const unsigned f1_grp = WS_F1 + (bh * 4 + cw) * 8 * FS;

    if (__builtin_expect(w >= 4, 1)) {
        const rsrc_t rx = mk_rsrc(p.x, S_ * XROW);
        const rsrc_t rw = mk_rsrc(br ? p.wqkvB : p.wqkvA, D_ * WROW);
        const unsigned vx0 = (lane >> 3) * XROW + 16 * ((lane & 7) ^ (lane >> 4));
        const unsigned vx1 = (lane >> 3) * XROW + 16 * ((lane & 7) ^ (4 + (lane >> 4)));
        const unsigned vw = (lane >> 2) * WROW + 16 * (lane & 3);
        const unsigned sx = (tb * 256 + cw * 64) * XROW;
        const unsigned sw = cw * 16 * WROW + h * 56 - 8 * (h & 1);
        auto issue = [&](int s) {
            dma_seg(rw, rx, vw, vx0, vx1, sw + seg(s) * (64 * WROW) + 896, sx + seg(s) * 128, slot_of(s) + SLOT_W + cw * 1024, slot_of(s) + cw * 8192);
        };
        auto issue_wq = [&](int s) { const unsigned sg = seg(s); dma16(rw, vw, sw + sg * (64 * WROW), L_WQ + sg * 4096 + cw * 1024); };
        bf16x8 xs[5][4][2];
        auto grab = [&](int s) {
#pragma unroll
            for (int ks = 0; ks < 4; ++ks) {
                const unsigned a = (laneX ^ (32 * ks)) + slot_of(s);
                xs[s][ks][0] = lds_b128(a); xs[s][ks][1] = lds_b128(a + 4096);
            }
        };
        if (NSDELAY && !((0x2A54u >> h) & 1)) __builtin_amdgcn_s_sleep(NSDELAY);
        issue(0); issue(1);
        u64 ev;
        asm volatile("global_load_dwordx2 %0, %1, %2 sc1" : "=&v"(ev) : "v"(0u), "s"(p.ws) : "memory");
        STAMP(1);
        WAIT_VM_LGKM_BAR(11);
        STAMP(2);
        grab(0); issue(2);
        __builtin_amdgcn_raw_buffer_store_b128((u32x4){~0u, ~0u, ~0u, ~0u}, rws, lane * 16, kvp_mine, 16);
        asm volatile("s_waitcnt vmcnt(11) lgkmcnt(0)\n\ts_barrier" : "+v"(ev) :: "memory");
        const u64 tag = uniform64(ev) + 1;
        grab(1); issue(3); WAIT_VM_LGKM_BAR(10);
        set_flag(p.ws, f1_grp + tb * FS, tag, lane);
        grab(2); issue(4); WAIT_VM_LGKM_BAR(11);
        grab(3); issue(5); WAIT_VM_LGKM_BAR(10);
        grab(4); issue(6); WAIT_VM_LGKM_BAR(10);
        WAIT_VM_LGKM_BAR(0);
        STAMP(3);
        const GAS u64* const f1p = (const GAS u64*)((const char*)p.ws + f1_grp + (lane & 7) * FS);
        u64 f1v;
        asm volatile("global_load_dwordx2 %0, %1, off sc1" : "=&v"(f1v) : "v"(f1p) : "memory");
        bf16x8 aphi[2];
        issue_wq(5); issue_wq(6); issue_wq(0); issue_wq(1); issue_wq(2); issue_wq(3); issue_wq(4);
        build_aphi(aphi, r, h2);
        PINV(aphi[0]); PINV(aphi[1]);
        asm volatile("s_waitcnt vmcnt(7)" : "+v"(f1v) :: "memory");
        if (__builtin_amdgcn_ballot_w64(f1v != tag) != 0) wait_flags(f1p, tag);
        STAMP(4);
        WAIT_VM_LGKM_BAR(0);
        STAMP(5);
        f32x16 qT[2] = {zero16, zero16};
        {
            const unsigned aq_real = L_WQ + rowpart + coloff, aq_pad = L_GT + rowpart;
            bf16x8 fq[3], xl[2][2];
            auto step_of = [](int n) { return n < 8 ? 5 + (n >> 2) : (n - 8) >> 2; };
            unsigned aQ = 0;
            auto rdq = [&](int n) {
                if ((n & 3) == 0) { const unsigned sg = seg(step_of(n)); aQ = cq == 7 ? aq_pad + 8 * sg : aq_real + 4096 * sg; PINV(aQ); }
                fq[n % 3] = tr_frag(aQ + (n & 3) * 1024, aQ + (n & 3) * 1024 + 256);
            };
            auto rdx = [&](int n) { const unsigned a = (laneX ^ (32 * (n & 3))) + slot_of(step_of(n)); xl[n & 1][0] = lds_b128(a); xl[n & 1][1] = lds_b128(a + 4096); };
            rdq(0); rdx(0); rdq(1);
            SB();
#pragma unroll
            for (int n = 0; n < 28; ++n) {
                const int s = step_of(n), ks = n & 3;
                if (n + 1 < 8) rdx(n + 1);
                if (n + 2 < 28) rdq(n + 2);
                SB();
                qT[0] = MFMA(fq[n % 3], n >= 8 ? xs[n >= 8 ? s : 0][ks][0] : xl[n & 1][0], qT[0]);
                SB();
                qT[1] = MFMA(fq[n % 3], n >= 8 ? xs[n >= 8 ? s : 0][ks][1] : xl[n & 1][1], qT[1]);
                SB();
                if (n == 7) {
                    lds_set(L_XFREE + 4 * cw);
                }
            }
        }
        STAMP(6);
        bf16x8 eq[2][2][2];
        float gs[2];
        {
            f32x16 pq[2];
            const bf16x8 q00 = pack8(qT[0], 0), q10 = pack8(qT[1], 0);
            pq[0] = MFMA(aphi[0], q00, zero16); pq[1] = MFMA(aphi[0], q10, zero16);
            const bf16x8 q01 = pack8(qT[0], 1), q11 = pack8(qT[1], 1);
            pq[0] = MFMA(aphi[1], q01, pq[0]); pq[1] = MFMA(aphi[1], q11, pq[1]);
#pragma unroll
            for (int b = 0; b < 2; ++b) {
                const float lg = swap_hi(qT[b][12]);
                gs[b] = __builtin_amdgcn_rcpf(1.f + __builtin_amdgcn_exp2f(br ? lg * L2E : -lg * L2E));
                PINV(gs[b]);
            }
            float cp[2], cn[2];
            row_extrema(pq[0], pq[1], cp, cn);
            PINV(cp[0]); PINV(cp[1]); PINV(cn[0]); PINV(cn[1]);
#pragma unroll
            for (int b = 0; b < 2; ++b) {
                const f32x2 l2 = {L2E, L2E}, cp2 = {cp[b], cp[b]}, cn2 = {cn[b], cn[b]};
                unsigned pe[8], ne[8];
#pragma unroll
                for (int i = 0; i < 7; ++i) {
                    const f32x2 v = {pq[b][2 * i], pq[b][2 * i + 1]};
                    const f32x2 a = __builtin_elementwise_fma(v, l2, cp2), c = __builtin_elementwise_fma(v, -l2, cn2);
                    pe[i] = cvtpk2((f32x2){__builtin_amdgcn_exp2f(a[0]), __builtin_amdgcn_exp2f(a[1])});
                    ne[i] = cvtpk2((f32x2){__builtin_amdgcn_exp2f(c[0]), __builtin_amdgcn_exp2f(c[1])});
                }
                pe[7] = 0; ne[7] = 0;
                eq[b][0][0] = __builtin_bit_cast(bf16x8, (u32x4){pe[0], pe[1], pe[2], pe[3]}); eq[b][0][1] = __builtin_bit_cast(bf16x8, (u32x4){pe[4], pe[5], pe[6], pe[7]});
                eq[b][1][0] = __builtin_bit_cast(bf16x8, (u32x4){ne[0], ne[1], ne[2], ne[3]}); eq[b][1][1] = __builtin_bit_cast(bf16x8, (u32x4){ne[4], ne[5], ne[6], ne[7]});
            }
        }
#pragma unroll
        for (int b = 0; b < 2; ++b) asm volatile("" : "+v"(eq[b][0][0]), "+v"(eq[b][0][1]), "+v"(eq[b][1][0]), "+v"(eq[b][1][1]), "+v"(gs[b]));
        STAMP(7);
        LGKM_BAR();
        __builtin_amdgcn_s_setprio(3);
        STAMP(8);
        {
            bf16x8 akv[4];
#pragma unroll
            for (int f = 0; f < 4; ++f) akv[f] = lds_b128(L_KVA + f * 1024 + lane * 16);
            f32x16 np[2], nn[2];
#pragma unroll
            for (int b = 0; b < 2; ++b) { np[b] = MFMA(akv[0], eq[b][0][0], zero16); nn[b] = MFMA(akv[2], eq[b][1][0], zero16); }
#pragma unroll
            for (int b = 0; b < 2; ++b) { np[b] = MFMA(akv[1], eq[b][0][1], np[b]); nn[b] = MFMA(akv[3], eq[b][1][1], nn[b]); }
            SB();
            const unsigned so = rfl(WS_O + (((tb * 4 + cw) * 32 + bh) * 4) * 1024);
#pragma unroll
            for (int b = 0; b < 2; ++b) {
                const float ia = __builtin_amdgcn_rcpf(np[b][15]), ib = __builtin_amdgcn_rcpf(nn[b][15]);
                const float den = np[b][14] * ia + nn[b][14] * ib + 1e-6f;
                const float sc = gs[b] * __builtin_amdgcn_rcpf(den);
                const float ca = ia * sc, cb = ib * sc;
                float o[14];
#pragma unroll
                for (int i = 0; i < 14; ++i) o[i] = np[b][i] * ca + nn[b][i] * cb;
                const u32x4 o0 = {cvtpk(o[0], o[1]), cvtpk(o[2], o[3]), cvtpk(o[4], o[5]), cvtpk(o[6], o[7])};
                const u32x4 o1 = {cvtpk(o[8], o[9]), cvtpk(o[10], o[11]), cvtpk(o[12], o[13]), 0u};
                __builtin_amdgcn_raw_buffer_store_b128(o0, rws, lane * 16, so + b * 2048, 16);
                __builtin_amdgcn_raw_buffer_store_b128(o1, rws, lane * 16, so + b * 2048 + 1024, 16);
            }
        }
        STAMP(9);
        if (has_out) {
            const unsigned vwo = (lane >> 3) * XROW + 16 * ((lane & 7) ^ (4 * ((lane >> 4) & 1)));
            dma_rows<8>(mk_rsrc(p.woB, D_ * XROW), vwo, (640 - 448 + 64 * cw) * XROW + oc * 128, L_WO + (640 + 64 * cw) * 128);
            WAIT_VM(8);
        } else WAIT_VM(0);
        set_flag(p.ws, WS_F2 + (((tb * 4 + cw) * 2 + br) * 16 + h) * FS, tag, lane);
        STAMP(10);
        if (has_out) { WAIT_VM(1); lds_count(L_WOCNT, lane); }
        STAMP_FLUSH();
        return;
    }

    u64 tag;
    {
        const rsrc_t rg = mk_rsrc(p.gate, XROW);
        const rsrc_t rp = mk_rsrc(br ? p.wphiB : p.wphiA, 16 * DH * DH * 2);
        unsigned g0, g1; u32x2 wp; u64 ev;
        asm volatile("global_load_dwordx2 %3, %9, %10 sc1\n\t"
                     "buffer_load_ushort %0, %4, %5, 0 offen\n\tbuffer_load_ushort %1, %4, %5, 0 offen offset:512\n\tbuffer_load_dwordx2 %2, %6, %7, %8 offen\n\t"
                     "s_waitcnt vmcnt(0)"
                     : "=&v"(g0), "=&v"(g1), "=&v"(wp), "=&v"(ev)
                     : "v"(tid * 2), "s"(rg), "v"(tid * 8), "s"(rp), "s"(h * (DH * DH * 2)), "v"(0u), "s"(p.ws) : "memory");
        tag = uniform64(ev) + 1;
        *(LAS u32x2*)lds_at(L_GT + (tid & 63) * 64 + (tid >> 6) * 8) = (u32x2){g0 & 0xffffu, 0u};
        *(LAS u32x2*)lds_at(L_GT + (tid & 63) * 64 + (4 + (tid >> 6)) * 8) = (u32x2){g1 & 0xffffu, 0u};
        *(LAS u32x2*)lds_at(L_WPHI + tid * 8) = wp;
        if (tid < 16) *(volatile LAS unsigned*)lds_at(L_SYNC + 4 * tid) = 0u;
    }
    __builtin_amdgcn_s_setprio(CPRIO);
    LGKM_BAR();
    STAMP(2);
    bf16x8 aphi[2];
    build_aphi(aphi, r, h2);
    PINV(aphi[0]); PINV(aphi[1]);
    const unsigned laneW = SLOT_W + rowpart + coloff;
    f32x16 vinit;
#pragma unroll
    for (int i = 0; i < 16; ++i) vinit[i] = r == 28 ? 1.f : 0.f;
    PINV(vinit);
    f32x16 kT[2] = {zero16, zero16}, vv[2] = {vinit, vinit};
    {
        unsigned aX[4], aK, aV;
        auto addr = [&](int s) {
#pragma unroll
            for (int ks = 0; ks < 4; ++ks) { aX[ks] = (laneX ^ (32 * ks)) + slot_of(s); PINV(aX[ks]); }
            aK = cq == 7 ? L_GT + rowpart + 56 : laneW + slot_of(s); PINV(aK);
            aV = cq == 7 ? L_GT + rowpart + 56 : laneW + slot_of(s) + 4096; PINV(aV);
        };
        Frag F[2];
#define RD_X(f, ks) do { f.x0 = lds_b128(aX[ks]); } while (0)
#define RD_X1(f, ks) do { f.x1 = lds_b128(aX[ks] + 4096); } while (0)
#define RD_K(f, ks) do { f.fk = tr_frag(aK + ks * 1024, aK + ks * 1024 + 256); } while (0)
#define RD_V(f, ks) do { f.fv = tr_frag(aV + ks * 1024, aV + ks * 1024 + 256); } while (0)
        addr(0);
        RD_X(F[0], 0); RD_K(F[0], 0); RD_X1(F[0], 0); RD_V(F[0], 0); SB();
#pragma unroll
        for (int n = 0; n < 28; ++n) {
            Frag& c = F[n & 1];
            Frag& x = F[(n + 1) & 1];
            const int nks = (n + 1) & 3;
            const bool more = n + 1 < 28;
            kT[0] = MFMA(c.fk, c.x0, kT[0]); SB();
            if (more && nks == 0) { LGKM_BAR(); addr((n + 1) >> 2); SB(); }
            if (more) { RD_X(x, nks); RD_K(x, nks); } SB();
            kT[1] = MFMA(c.fk, c.x1, kT[1]); SB();
            if (more) { RD_X1(x, nks); } SB();
            vv[0] = MFMA(c.x0, c.fv, vv[0]); SB();
            if (more) { RD_V(x, nks); } SB();
            vv[1] = MFMA(c.x1, c.fv, vv[1]); SB();
        }
    }
    STAMP(3);

    const unsigned phiW = L_PHI + cw * 8192;
    {
        const unsigned pt0 = 4 * h2 + q4;
        const unsigned pa0 = phiW + pt0 * 64 + 8 * (cq ^ ((pt0 >> 1) & 7));
        const unsigned pa1 = (pa0 ^ 32) + 8 * 64;
        f32x16 pk[2];
        const bf16x8 k00 = pack8(kT[0], 0), k10 = pack8(kT[1], 0);
        pk[0] = MFMA(aphi[0], k00, zero16); pk[1] = MFMA(aphi[0], k10, zero16);
        const bf16x8 k01 = pack8(kT[0], 1), k11 = pack8(kT[1], 1);
        pk[0] = MFMA(aphi[1], k01, pk[0]); pk[1] = MFMA(aphi[1], k11, pk[1]);
        bf16x8 vp[2][2];
#pragma unroll
        for (int b = 0; b < 2; ++b)
#pragma unroll
            for (int s = 0; s < 2; ++s) { vp[b][s] = pack8(vv[b], s); PINV(vp[b][s]); }
        SB();
        float cp[2], cn[2];
        row_extrema(pk[0], pk[1], cp, cn);
        f32x2 ep[2][7], en[2][7];
        exp_pair(pk[0], cp[0], cn[0], ep[0], en[0]);
        phi_write(ep[0], en[0], phiW, r, h2);
        exp_pair(pk[1], cp[1], cn[1], ep[1], en[1]);
        f32x16 kvp = zero16, kvn = zero16;
        auto kv = [&](int b) {
#pragma unroll
            for (int s = 0; s < 2; ++s) {
                const unsigned o = b * 4096 + s * 1024;
                kvp = MFMA(tr_frag(pa0 + o, pa1 + o), vp[b][s], kvp);
                kvn = MFMA(tr_frag(pa0 + o + 2048, pa1 + o + 2048), vp[b][s], kvn);
            }
        };
        kv(0);
        phi_write(ep[1], en[1], phiW + 4096, r, h2);
        kv(1);
        asm volatile("s_waitcnt lgkmcnt(0)" ::: "memory");
#pragma unroll
        for (int f = 0; f < 4; ++f)
#pragma unroll
            for (int hh = 0; hh < 2; ++hh) {
                const f32x16& t = f < 2 ? kvp : kvn;
                const int o = 8 * (f & 1) + 4 * hh;
                *(LAS f32x4*)lds_at(phiW + f * 2048 + hh * 1024 + lane * 16) = (f32x4){t[o], t[o + 1], t[o + 2], t[o + 3]};
            }
    }
    STAMP(4);
    LGKM_BAR();
    STAMP(5);
    {
        f32x4 t[4][2];
#pragma unroll
        for (int u = 0; u < 4; ++u) {
            t[u][0] = *(const LAS f32x4*)lds_at(L_PHI + u * 8192 + cw * 2048 + lane * 16);
            t[u][1] = *(const LAS f32x4*)lds_at(L_PHI + u * 8192 + cw * 2048 + 1024 + lane * 16);
        }
        SB();
        float a[8];
#pragma unroll
        for (int i = 0; i < 8; ++i) {
            float x01 = addf(t[0][i >> 2][i & 3], t[1][i >> 2][i & 3]), x23 = addf(t[2][i >> 2][i & 3], t[3][i >> 2][i & 3]);
            a[i] = addf(x01, x23);
        }
        u32x4 o = {cvtpk(a[0], a[1]), cvtpk(a[2], a[3]), cvtpk(a[4], a[5]), cvtpk(a[6], a[7])};
        STAMPX(1, o);
        __builtin_amdgcn_raw_buffer_store_b128(o, rws, lane * 16, kvp_mine, 16);
    }
    const rsrc_t roA = mk_rsrc(p.woA, D_ * XROW), roB = mk_rsrc(p.woB, D_ * XROW);
    const unsigned vwo = (lane >> 3) * XROW + 16 * ((lane & 7) ^ (4 * ((lane >> 4) & 1)));
    const rsrc_t roC = cw < 2 ? roA : roB;
    const unsigned R5 = 320 + 64 * cw, R5g = cw < 2 ? R5 : R5 - 448;
    {
        bf16x8 sel;
        {
            const int jj = (r & 3) + 4 * (r >> 3);
            const bool on = r < 16 && ((r >> 2) & 1) == h2;
            const unsigned one = on ? (0x3F80u << (16 * (jj & 1))) : 0u;
            sel = __builtin_bit_cast(bf16x8, (u32x4){(jj >> 1) == 0 ? one : 0u, (jj >> 1) == 1 ? one : 0u, (jj >> 1) == 2 ? one : 0u, (jj >> 1) == 3 ? one : 0u});
            PINV(sel);
        }
        dma_rows<2>(roA, vwo, (256 + 16 * cw) * XROW + oc * 128, L_WO + (256 + 16 * cw) * 128);
        dma_rows<2>(roB, vwo, (576 - 448 + 16 * cw) * XROW + oc * 128, L_WO + (576 + 16 * cw) * 128);
        lds_wait_ge(L_XFREE + 4 * cw, 1);
        dma_rows<6>(roA, vwo, 64 * cw * XROW + oc * 128, L_WO + 64 * cw * 128);
        const int hr = (r >> 2) & 1, reg = (r & 3) + 4 * (r >> 3);
        const int col = reg < 14 ? 14 * hr + reg : 28;
        const unsigned vo = (h2 * 32 + col) * 16, so = WS_KVP + (bh * 8 * 4 + cw) * 1024;
        u32x4 part[8];
        STAMP(9);
#pragma unroll
        for (int u = 0; u < 8; ++u) part[u] = load_sc1_asm(rws, vo, so + u * 4096);
#define PARTS "+v"(part[0]), "+v"(part[1]), "+v"(part[2]), "+v"(part[3]), "+v"(part[4]), "+v"(part[5]), "+v"(part[6]), "+v"(part[7])
        dma_rows<2>(roA, vwo, (64 * cw + 48) * XROW + oc * 128, L_WO + (64 * cw + 48) * 128);
        dma_rows<8>(roC, vwo, R5g * XROW + oc * 128, L_WO + R5 * 128);
        asm volatile("s_waitcnt vmcnt(10)" : PARTS :: "memory");
        STAMP(6);
        auto pattern_left = [&]() {
            unsigned m = 0;
#pragma unroll
            for (int u = 0; u < 8; ++u) { m = umax3(m, part[u][0], part[u][1]); m = umax3(m, part[u][2], part[u][3]); }
            return __builtin_amdgcn_ballot_w64(m == ~0u) != 0;
        };
        if (pattern_left()) {
            unsigned spins = 0;
#pragma unroll 1
            do {
#pragma unroll
                for (int u = 0; u < 8; ++u) part[u] = load_sc1_asm(rws, vo, so + u * 4096);
                asm volatile("s_waitcnt vmcnt(0)" : PARTS :: "memory");
            } while (pattern_left() && ++spins < SPIN_CAP);
        }
#undef PARTS
        STAMP(7);
        SB();
        f32x16 acc = zero16;
#pragma unroll
        for (int u = 0; u < 8; ++u) acc = MFMA(sel, __builtin_bit_cast(bf16x8, part[u]), acc);
        u32x4 o = __builtin_bit_cast(u32x4, pack8(acc, 0));
        if (reg == 15) o = (u32x4){ONE2, ONE2, ONE2, ONE2};
        *(LAS u32x4*)lds_at(L_KVA + cw * 1024 + lane * 16) = o;
    }
    LGKM_BAR();
    STAMP(8);
    if (!has_out) { STAMP_FLUSH(); return; }
    WAIT_VM(0); lds_count(L_WOCNT, lane);

    const unsigned tk = cw & 1, kh = cw >> 1;
    f32x16 mine = zero16, other = zero16;
    {
        const unsigned colp = 32 * blk16 + 8 * p4;
        const unsigned r0 = kh * 448 + 14 * h2 + q4, r1 = kh * 448 + 14 * h2 + (q4 < 1 ? q4 : 1);
        unsigned a0 = L_WO + r0 * 128 + 64 * (kh ^ ((r0 >> 1) & 1)) + colp;
        unsigned a1 = L_WO + r1 * 128 + 64 * (kh ^ ((r1 >> 1) & 1)) + colp;
        unsigned a2 = a0 ^ 64, a3 = a1 ^ 64;
        asm volatile("" : "+v"(a0), "+v"(a1), "+v"(a2), "+v"(a3));
        lds_wait_ge(L_WOCNT, 8);
        STAMP(10);
        if (cw == 0) {
            wait_flags((const GAS u64*)((const char*)p.ws + WS_F2 + (og * 32 + (lane & 31)) * FS), tag);
            lds_set(L_F2OK);
        } else lds_wait_ge(L_F2OK, 1);
        STAMP(11);
        u32x4 of[16][2];
        auto ld = [&](int i) {
#pragma unroll
            for (int s = 0; s < 2; ++s)
                of[i][s] = __builtin_amdgcn_raw_buffer_load_b128(rws, lane * 16, WS_O + (((og * 32 + kh * 16 + i) * 2 + tk) * 2 + s) * 1024, 16);
        };
        bf16x8 A[2][4];
        auto rd = [&](bf16x8 (&f)[4], int i) {
            const unsigned o = i * 28 * 128;
            f[0] = tr_frag(a0 + o, a0 + o + 512); f[1] = tr_frag(a2 + o, a2 + o + 512);
            f[2] = tr_frag(a0 + o + 1024, a1 + o + 1536); f[3] = tr_frag(a2 + o + 1024, a3 + o + 1536);
        };
#pragma unroll
        for (int i = 0; i < OD; ++i) ld(i);
        SB();
        rd(A[0], 0); SB();
#pragma unroll
        for (int i = 0; i < 16; ++i) {
            if (i + OD < 16) ld(i + OD);
            if (i + 1 < 16) rd(A[(i + 1) & 1], i + 1);
            SB();
            const bf16x8 b0 = __builtin_bit_cast(bf16x8, of[i][0]), b1 = __builtin_bit_cast(bf16x8, of[i][1]);
            mine = MFMA(A[i & 1][0], b0, mine); other = MFMA(A[i & 1][1], b0, other);
            mine = MFMA(A[i & 1][2], b1, mine); other = MFMA(A[i & 1][3], b1, other);
            SB();
        }
    }
    STAMP(12);
#pragma unroll
    for (int g = 0; g < 4; ++g)
        *(LAS f32x4*)lds_at(L_PHI + cw * 4096 + g * 1024 + lane * 16) = (f32x4){other[4 * g], other[4 * g + 1], other[4 * g + 2], other[4 * g + 3]};
    LGKM_BAR();
    if (blockIdx.x == 0 && tid == 0) __hip_atomic_store((GAS u64*)((char*)p.ws + WS_EPOCH), tag, __ATOMIC_RELAXED, __HIP_MEMORY_SCOPE_AGENT);
    {
        unsigned d[8];
        f32x4 t[4];
#pragma unroll
        for (int g = 0; g < 4; ++g) t[g] = *(const LAS f32x4*)lds_at(L_PHI + (cw ^ 2) * 4096 + g * 1024 + lane * 16);
        SB();
#pragma unroll
        for (int g = 0; g < 4; ++g) {
            d[2 * g] = cvtpk(mine[4 * g] + t[g][0], mine[4 * g + 1] + t[g][1]);
            d[2 * g + 1] = cvtpk(mine[4 * g + 2] + t[g][2], mine[4 * g + 3] + t[g][3]);
        }
        const rsrc_t rout = mk_rsrc(p.out, S_ * XROW);
        const unsigned vo = r * XROW + h2 * 16, so = (og * 64 + tk * 32) * XROW + oc * 128 + kh * 64;
#pragma unroll
        for (int k = 0; k < 4; k += 2) {
            auto sx0 = __builtin_amdgcn_permlane32_swap(d[2 * k], d[2 * k + 2], false, false);
            auto sx1 = __builtin_amdgcn_permlane32_swap(d[2 * k + 1], d[2 * k + 3], false, false);
            const u32x4 o = {sx0[0], sx1[0], sx0[1], sx1[1]};
            __builtin_amdgcn_raw_buffer_store_b128(o, rout, vo, so + 16 * k, 0);
        }
    }
    STAMP(13); DLWAIT();
    STAMP_FLUSH();
}

extern "C" void kernel_launch(void* const* d_in, const int* in_sizes, int n_in, void* d_out, int out_size, void* d_ws, size_t ws_size,
                              hipStream_t stream) {
    Params p;
    p.x = d_in[0]; p.wqkvA = d_in[1]; p.wphiA = d_in[2]; p.woA = d_in[3];
    p.wqkvB = d_in[5]; p.wphiB = d_in[6]; p.woB = d_in[7]; p.gate = d_in[8];
    p.out = d_out; p.ws = d_ws;
    (void)hipFuncSetAttribute((const void*)hedgehog_mix_kernel, hipFuncAttributeMaxDynamicSharedMemorySize, L_TOTAL);
    hedgehog_mix_kernel<<<256, 512, L_TOTAL, stream>>>(p);
}
```

```cpp
#define TLBW 0
#include <hip/hip_runtime.h>
#include <stdint.h>

typedef short bf16x8 __attribute__((ext_vector_type(8)));
typedef short s16x4 __attribute__((ext_vector_type(4)));
typedef short v4i16_t __attribute__((ext_vector_type(4)));
typedef __bf16 bf2_t __attribute__((ext_vector_type(2)));
typedef float f32x2 __attribute__((ext_vector_type(2)));
typedef float f32x4 __attribute__((ext_vector_type(4)));
typedef float f32x16 __attribute__((ext_vector_type(16)));
typedef unsigned u32x2 __attribute__((ext_vector_type(2)));
typedef unsigned u32x4 __attribute__((ext_vector_type(4)));
typedef unsigned long long u64;
#define LAS __attribute__((address_space(3)))
#define GAS __attribute__((address_space(1)))
#define DI __device__ __forceinline__
#define MFMA(a, b, c) __builtin_amdgcn_mfma_f32_32x32x16_bf16((a), (b), (c), 0, 0, 0)

#define EARLYQ 0
#define B2N 10
#define PKFREE 0
#define LSLEEP 1
#define CPRIO 2
#define NSDELAY 0
#define PACE 0
#define OD 5
#define PF_CODE 1
#define PF_CODE_LINES 176

constexpr int S_ = 2048, D_ = 448, DH = 28;
constexpr unsigned XROW = D_ * 2;
constexpr unsigned WROW = 3 * D_ * 2;
constexpr unsigned FS = 128;
constexpr unsigned WS_EPOCH = 0;
constexpr unsigned WS_F1 = 4096;
constexpr unsigned WS_F2 = WS_F1 + 32 * 4 * 8 * FS;
constexpr unsigned WS_KVP = WS_F2 + 32 * 32 * FS;
constexpr unsigned WS_O = WS_KVP + 32 * 8 * 4 * 1024;
constexpr unsigned WS_END = WS_O + 32 * 32 * 4 * 1024;
constexpr unsigned WS_STAMP = 8u << 20;
constexpr unsigned SLOT = 40960, SLOT_W = 32768;
constexpr unsigned L_B = 0, L_A = SLOT, L_C = 2 * SLOT;
constexpr unsigned L_WQ = 3 * SLOT;
constexpr unsigned L_GT = L_WQ + 448 * 64;
constexpr unsigned L_WPHI = L_GT + 4096;
constexpr unsigned L_SCR = L_WPHI + 2048;
constexpr unsigned L_SYNC = L_SCR + 256;
constexpr unsigned L_XFREE = L_SYNC;
constexpr unsigned L_WOCNT = L_SYNC + 16;
constexpr unsigned L_REDCNT = L_SYNC + 20;
constexpr unsigned L_WQA = L_SYNC + 24, L_WQB = L_SYNC + 28;
constexpr unsigned L_QGO = L_SYNC + 48;
constexpr unsigned L_F2OK = L_SYNC + 32;
constexpr unsigned L_PHI = L_B;
constexpr unsigned L_KVA = L_B + SLOT_W;
constexpr unsigned L_WO = L_A;
constexpr unsigned L_TOTAL = 163840;
static_assert(L_SYNC + 64 <= L_TOTAL, "lds");
static_assert(L_WO + 896 * 128 == L_WPHI, "lds");
__host__ __device__ constexpr unsigned slot_of(int s) { return s % 3 == 0 ? L_A : (s % 3 == 1 ? L_B : L_C); }

struct Params {
    const void* x; const void* wqkvA; const void* wqkvB; const void* wphiA; const void* wphiB;
    const void* woA; const void* woB; const void* gate; void* out; void* ws;
};

typedef __amdgpu_buffer_rsrc_t rsrc_t;
DI rsrc_t mk_rsrc(const void* p, unsigned bytes) { return __builtin_amdgcn_make_buffer_rsrc((void*)p, (short)0, (int)bytes, 0x00020000); }
DI unsigned rfl(unsigned v) { return (unsigned)__builtin_amdgcn_readfirstlane((int)v); }
DI u64 uniform64(u64 v) { return (u64)rfl((unsigned)v) | ((u64)rfl((unsigned)(v >> 32)) << 32); }
DI void dma16(rsrc_t r, unsigned voff, unsigned soff, unsigned lds) {
    asm volatile("s_mov_b32 m0, %0\n\ts_nop 0\n\tbuffer_load_dwordx4 %1, %2, %3 offen lds" ::"s"(lds), "v"(voff), "s"(r), "s"(soff) : "memory");
}
DI void dma_seg(rsrc_t rw, rsrc_t rx, unsigned vw, unsigned vx0, unsigned vx1, unsigned sw, unsigned sx, unsigned mw, unsigned mx) {
#define XP(v) "s_addk_i32 m0, 0x400\n\ts_addk_i32 %1, 0x1c00\n\tbuffer_load_dwordx4 " v ", %6, %1 offen lds\n\t"
    asm volatile("s_mov_b32 m0, %7\n\ts_nop 0\n\tbuffer_load_dwordx4 %2, %5, %0 offen lds\n\t"
                 "s_addk_i32 m0, 0x1000\n\ts_addk_i32 %0, 0x380\n\tbuffer_load_dwordx4 %2, %5, %0 offen lds\n\t"
                 "s_mov_b32 m0, %8\n\ts_nop 0\n\tbuffer_load_dwordx4 %3, %6, %1 offen lds\n\t"
                 XP("%4") XP("%3") XP("%4") XP("%3") XP("%4") XP("%3") XP("%4")
                 : "+s"(sw), "+s"(sx) : "v"(vw), "v"(vx0), "v"(vx1), "s"(rw), "s"(rx), "s"(mw), "s"(mx) : "scc", "memory");
#undef XP
}
template <int N> DI void dma_rows(rsrc_t r, unsigned v, unsigned so, unsigned m) {
    static_assert(N == 2 || N == 3 || N == 5 || N == 6 || N == 8, "");
#define RP "s_addk_i32 m0, 0x400\n\ts_addk_i32 %0, 0x1c00\n\tbuffer_load_dwordx4 %1, %2, %0 offen lds\n\t"
#define R0 "s_mov_b32 m0, %3\n\ts_nop 0\n\tbuffer_load_dwordx4 %1, %2, %0 offen lds\n\t"
    if (N == 2) asm volatile(R0 RP : "+s"(so) : "v"(v), "s"(r), "s"(m) : "scc", "memory");
    if (N == 3) asm volatile(R0 RP RP : "+s"(so) : "v"(v), "s"(r), "s"(m) : "scc", "memory");
    if (N == 5) asm volatile(R0 RP RP RP RP : "+s"(so) : "v"(v), "s"(r), "s"(m) : "scc", "memory");
    if (N == 6) asm volatile(R0 RP RP RP RP RP : "+s"(so) : "v"(v), "s"(r), "s"(m) : "scc", "memory");
    if (N == 8) asm volatile(R0 RP RP RP RP RP RP RP : "+s"(so) : "v"(v), "s"(r), "s"(m) : "scc", "memory");
#undef RP
#undef R0
}
DI u32x4 load_sc1_asm(rsrc_t r, unsigned voff, unsigned soff) {
    u32x4 v; asm volatile("buffer_load_dwordx4 %0, %1, %2, %3 offen sc1" : "=&v"(v) : "v"(voff), "s"(r), "s"(soff) : "memory"); return v;
}
DI unsigned umax3(unsigned a, unsigned b, unsigned c) { const unsigned t = a > b ? a : b; return t > c ? t : c; }
DI void gdma4(const void* a, unsigned lds) {
    asm volatile("s_mov_b32 m0, %0\n\ts_nop 0\n\tglobal_load_lds_dword %1, off" ::"s"(lds), "v"(a) : "memory");
}
#define WAIT_VM(n) asm volatile("s_waitcnt vmcnt(" #n ")" ::: "memory")
#define WAIT_VM_LGKM_BAR(n) asm volatile("s_waitcnt vmcnt(" #n ") lgkmcnt(0)\n\ts_barrier" ::: "memory")
#define LGKM_BAR() asm volatile("s_waitcnt lgkmcnt(0)\n\ts_barrier" ::: "memory")
#define SB() __builtin_amdgcn_sched_barrier(0)
#define PINV(x) asm volatile("" : "+v"(x))

extern __shared__ __attribute__((aligned(16))) char smem_dyn[];
DI LAS char* lds_at(unsigned off) { return (LAS char*)(uintptr_t)off; }
DI bf16x8 lds_b128(unsigned a) { return *(const LAS bf16x8*)lds_at(a); }
DI s16x4 lds_tr(unsigned a) { return __builtin_bit_cast(s16x4, __builtin_amdgcn_ds_read_tr16_b64_v4i16((LAS v4i16_t*)lds_at(a))); }
DI bf16x8 tr_frag(unsigned a0, unsigned a1) { const s16x4 lo = lds_tr(a0), hi = lds_tr(a1); return __builtin_shufflevector(lo, hi, 0, 1, 2, 3, 4, 5, 6, 7); }
DI unsigned cvtpk(float lo, float hi) { const f32x2 v = {lo, hi}; return __builtin_bit_cast(unsigned, __builtin_convertvector(v, bf2_t)); }
DI bf16x8 pack8(const f32x16& x, int s) {
    const u32x4 p = {cvtpk(x[8 * s], x[8 * s + 1]), cvtpk(x[8 * s + 2], x[8 * s + 3]), cvtpk(x[8 * s + 4], x[8 * s + 5]), cvtpk(x[8 * s + 6], x[8 * s + 7])};
    return __builtin_bit_cast(bf16x8, p);
}
DI float addf(float a, float b) { float r; asm("v_add_f32 %0, %1, %2" : "=v"(r) : "v"(a), "v"(b)); return r; }
DI float swap_hi(float v) { auto r = __builtin_amdgcn_permlane32_swap(__float_as_uint(v), __float_as_uint(v), false, false); return __uint_as_float(r[1]); }
DI float xhalf_max(float v) { auto r = __builtin_amdgcn_permlane32_swap(__float_as_uint(v), __float_as_uint(v), false, false); return __builtin_fmaxf(__uint_as_float(r[0]), __uint_as_float(r[1])); }
DI float xhalf_min(float v) { auto r = __builtin_amdgcn_permlane32_swap(__float_as_uint(v), __float_as_uint(v), false, false); return __builtin_fminf(__uint_as_float(r[0]), __uint_as_float(r[1])); }
DI float xhalf_sum(float v) { auto r = __builtin_amdgcn_permlane32_swap(__float_as_uint(v), __float_as_uint(v), false, false); return __uint_as_float(r[0]) + __uint_as_float(r[1]); }
constexpr float L2E = 1.4426950408889634f;
constexpr unsigned ONE2 = 0x3F803F80u;
constexpr unsigned SPIN_CAP = 1u << 16;

#define STAMP_DECL do { } while (0)
#define STAMP(i) do { } while (0)
#define STAMPX(i, x) do { } while (0)
#define STAMP_FLUSH() do { } while (0)

DI void row_extrema(const f32x16& a, const f32x16& b, float (&cp)[2], float (&cn)[2]) {
    float mxa = __builtin_fmaxf(a[0], a[1]), mna = __builtin_fminf(a[0], a[1]), mxb = __builtin_fmaxf(b[0], b[1]), mnb = __builtin_fminf(b[0], b[1]);
#pragma unroll
    for (int i = 2; i < 14; i += 2) {
        mxa = __builtin_fmaxf(__builtin_fmaxf(mxa, a[i]), a[i + 1]); mxb = __builtin_fmaxf(__builtin_fmaxf(mxb, b[i]), b[i + 1]);
        mna = __builtin_fminf(__builtin_fminf(mna, a[i]), a[i + 1]); mnb = __builtin_fminf(__builtin_fminf(mnb, b[i]), b[i + 1]);
    }
    cp[0] = -xhalf_max(mxa) * L2E; cp[1] = -xhalf_max(mxb) * L2E;
    cn[0] = xhalf_min(mna) * L2E; cn[1] = xhalf_min(mnb) * L2E;
}
DI unsigned cvtpk2(f32x2 v) { return __builtin_bit_cast(unsigned, __builtin_convertvector(v, bf2_t)); }
DI void exp_pair(const f32x16& pp, float cp, float cn, f32x2 (&ep)[7], f32x2 (&en)[7]) {
#pragma unroll
    for (int i = 0; i < 7; ++i) {
        const f32x2 l2 = {L2E, L2E}, cp2 = {cp, cp}, cn2 = {cn, cn};
        const f32x2 v = {pp[2 * i], pp[2 * i + 1]};
        const f32x2 a = __builtin_elementwise_fma(v, l2, cp2), b = __builtin_elementwise_fma(v, -l2, cn2);
        ep[i] = (f32x2){__builtin_amdgcn_exp2f(a[0]), __builtin_amdgcn_exp2f(a[1])};
        en[i] = (f32x2){__builtin_amdgcn_exp2f(b[0]), __builtin_amdgcn_exp2f(b[1])};
    }
}
DI float sum14(const f32x2 (&e)[7]) {
    f32x2 s = e[0];
#pragma unroll
    for (int i = 1; i < 7; ++i) s += e[i];
    return s[0] + s[1];
}
DI unsigned scale_pack(f32x2 v, float s) {
    return cvtpk2(v * (f32x2){s, s});
}
DI void phi_write(const f32x2 (&ep)[7], const f32x2 (&en)[7], unsigned base, int r, int h2) {
    const float ip = __builtin_amdgcn_rcpf(xhalf_sum(sum14(ep))), in = __builtin_amdgcn_rcpf(xhalf_sum(sum14(en)));
#pragma unroll
    for (int g = 0; g < 4; ++g) {
        const unsigned a = base + r * 64 + 8 * ((2 * g + h2) ^ ((r >> 1) & 7));
        const u32x2 dp = {scale_pack(ep[2 * g], ip), g < 3 ? scale_pack(ep[(2 * g + 1) % 7], ip) : 0u};
        const u32x2 dn = {scale_pack(en[2 * g], in), g < 3 ? scale_pack(en[(2 * g + 1) % 7], in) : 0u};
        *(LAS u32x2*)lds_at(a) = dp;
        *(LAS u32x2*)lds_at(a + 2048) = dn;
    }
}
DI void build_aphi(bf16x8 (&aphi)[2], int r, int h2) {
    const int hr = (r >> 2) & 1, reg = (r & 3) + 4 * (r >> 3), f = 14 * hr + (reg < 14 ? reg : 0);
    const unsigned a = L_WPHI + 2 * (4 * h2 * DH + f);
#pragma unroll
    for (int s = 0; s < 2; ++s)
#pragma unroll
        for (int jj = 0; jj < 8; ++jj) {
            const int d0 = 16 * s + 8 * (jj >> 2) + (jj & 3);
            const bool ok = reg < 14 && (d0 + 4 < DH || h2 == 0);
            const unsigned short v = *(const LAS unsigned short*)lds_at(a + 2 * d0 * DH);
            aphi[s][jj] = ok ? (short)v : (short)0;
        }
}

DI void lds_wait_ge(unsigned addr, unsigned v) {
#pragma unroll 1
    for (unsigned spins = 0; spins < SPIN_CAP * 16; ++spins) {
        if (*(volatile LAS unsigned*)lds_at(addr) >= v) break;
        if (LSLEEP) __builtin_amdgcn_s_sleep(LSLEEP);
    }
    asm volatile("" ::: "memory");
}
DI void lds_set(unsigned addr) { asm volatile("s_waitcnt lgkmcnt(0)" ::: "memory"); *(volatile LAS unsigned*)lds_at(addr) = 1u; }
DI void lds_count(unsigned addr, int lane) {
    asm volatile("s_waitcnt lgkmcnt(0)" ::: "memory");
    if (lane == 0) __hip_atomic_fetch_add((LAS unsigned*)lds_at(addr), 1u, __ATOMIC_RELAXED, __HIP_MEMORY_SCOPE_WORKGROUP);
}
DI void wait_flags(const GAS u64* f, u64 tag) {
#pragma unroll 1
    for (unsigned spins = 0; spins < SPIN_CAP; ++spins) {
        const u64 v = __hip_atomic_load(f, __ATOMIC_RELAXED, __HIP_MEMORY_SCOPE_AGENT);
        if (__builtin_amdgcn_ballot_w64(v != tag) == 0) break;
        __builtin_amdgcn_s_sleep(1);
    }
    asm volatile("" ::: "memory");
}
DI void set_flag(void* ws, unsigned off, u64 tag, int lane) {
    if (lane == 0) __hip_atomic_store((GAS u64*)((char*)ws + off), tag, __ATOMIC_RELAXED, __HIP_MEMORY_SCOPE_AGENT);
}

struct Frag { bf16x8 x0, x1, fk, fv; };

__global__ void __launch_bounds__(512, 2) hedgehog_mix_kernel(Params p) {
    const int tid = threadIdx.x, lane = tid & 63;
    const unsigned w = rfl(tid >> 6), cw = w & 3;
    if (w == 4) {
        u64 pc; asm volatile("s_getpc_b64 %0" : "=s"(pc));
        const unsigned l0 = (blockIdx.x >> 3) * 6 + (lane < 6 ? lane : 5), l = l0 < PF_CODE_LINES ? l0 : 0;
        gdma4((const char*)(pc & ~127ull) + l * 128, L_SCR);
    }
    const int r = lane & 31, h2 = lane >> 5, i16 = lane & 15, q4 = i16 >> 2, p4 = i16 & 3, blk16 = (lane >> 4) & 1;
    const unsigned xcd = blockIdx.x & 7, j = blockIdx.x >> 3;
    const unsigned tb = (xcd & 1) * 4 + (j & 3), bh = (xcd >> 1) * 8 + (j >> 2), br = bh >> 4, h = bh & 15;
    const unsigned og = xcd * 4 + j / 7, oc = j % 7;
    const bool has_out = j < 28;
    const unsigned rot = ((j >> 2) + 2 * (j & 3)) % 7;
    auto seg = [&](int n) { const unsigned t = rot + n; return t >= 7 ? t - 7 : t; };
    asm volatile("" : "+s"(p.x), "+s"(p.wqkvA), "+s"(p.wqkvB), "+s"(p.wphiA), "+s"(p.wphiB), "+s"(p.woA), "+s"(p.woB), "+s"(p.gate), "+s"(p.out), "+s"(p.ws));
    STAMP_DECL;
    STAMP(0);
    const rsrc_t rws = mk_rsrc(p.ws, WS_END);
    const f32x16 zero16 = {};
    const int cq = 4 * blk16 + p4;
    const unsigned rowpart = (8 * h2 + q4) * 64;
    const unsigned coloff = (8 * (h & 1) + 8 * cq) & 63;
    const unsigned laneX = cw * 8192 + r * 128 + 16 * (h2 ^ ((r >> 1) & 7));
    const unsigned kvp_mine = WS_KVP + ((bh * 8 + tb) * 4 + cw) * 1024;
    const unsigned f1_grp = WS_F1 + (bh * 4 + cw) * 8 * FS;

    if (__builtin_expect(w >= 4, 1)) {
        const rsrc_t rx = mk_rsrc(p.x, S_ * XROW);
        const rsrc_t rw = mk_rsrc(br ? p.wqkvB : p.wqkvA, D_ * WROW);
        const unsigned vx0 = (lane >> 3) * XROW + 16 * ((lane & 7) ^ (lane >> 4));
        const unsigned vx1 = (lane >> 3) * XROW + 16 * ((lane & 7) ^ (4 + (lane >> 4)));
        const unsigned vw = (lane >> 2) * WROW + 16 * (lane & 3);
        const unsigned sx = (tb * 256 + cw * 64) * XROW;
        const unsigned sw = cw * 16 * WROW + h * 56 - 8 * (h & 1);
        auto issue = [&](int s) {
            dma_seg(rw, rx, vw, vx0, vx1, sw + seg(s) * (64 * WROW) + 896, sx + seg(s) * 128, slot_of(s) + SLOT_W + cw * 1024, slot_of(s) + cw * 8192);
        };
        auto issue_wq = [&](int s) { const unsigned sg = seg(s); dma16(rw, vw, sw + sg * (64 * WROW), L_WQ + sg * 4096 + cw * 1024); };
        bf16x8 xs[5][4][2];
        auto grab = [&](int s) {
#pragma unroll
            for (int ks = 0; ks < 4; ++ks) {
                const unsigned a = (laneX ^ (32 * ks)) + slot_of(s);
                xs[s][ks][0] = lds_b128(a); xs[s][ks][1] = lds_b128(a + 4096);
            }
        };
        if (NSDELAY && !((0x2A54u >> h) & 1)) __builtin_amdgcn_s_sleep(NSDELAY);
        issue(0); issue(1);
        u64 ev;
        asm volatile("global_load_dwordx2 %0, %1, %2 sc1" : "=&v"(ev) : "v"(0u), "s"(p.ws) : "memory");
        STAMP(1);
        WAIT_VM_LGKM_BAR(11);
        STAMP(2);
        grab(0); issue(2);
        __builtin_amdgcn_raw_buffer_store_b128((u32x4){~0u, ~0u, ~0u, ~0u}, rws, lane * 16, kvp_mine, 16);
        asm volatile("s_waitcnt vmcnt(11) lgkmcnt(0)\n\ts_barrier" : "+v"(ev) :: "memory");
        const u64 tag = uniform64(ev) + 1;
        grab(1); issue(3); WAIT_VM_LGKM_BAR(10);
        set_flag(p.ws, f1_grp + tb * FS, tag, lane);
        grab(2); issue(4); WAIT_VM_LGKM_BAR(11);
        grab(3); issue(5); WAIT_VM_LGKM_BAR(10);
        grab(4); issue(6); WAIT_VM_LGKM_BAR(10);
        WAIT_VM_LGKM_BAR(0);
        STAMP(3);
        const GAS u64* const f1p = (const GAS u64*)((const char*)p.ws + f1_grp + (lane & 7) * FS);
        u64 f1v;
        asm volatile("global_load_dwordx2 %0, %1, off sc1" : "=&v"(f1v) : "v"(f1p) : "memory");
        bf16x8 aphi[2];
        issue_wq(5); issue_wq(6); issue_wq(0); issue_wq(1); issue_wq(2); issue_wq(3); issue_wq(4);
        build_aphi(aphi, r, h2);
        PINV(aphi[0]); PINV(aphi[1]);
        asm volatile("s_waitcnt vmcnt(7)" : "+v"(f1v) :: "memory");
        if (__builtin_amdgcn_ballot_w64(f1v != tag) != 0) wait_flags(f1p, tag);
        STAMP(4);
        WAIT_VM_LGKM_BAR(0);
        STAMP(5);
        f32x16 qT[2] = {zero16, zero16};
        {
            const unsigned aq_real = L_WQ + rowpart + coloff, aq_pad = L_GT + rowpart;
            bf16x8 fq[3], xl[2][2];
            auto step_of = [](int n) { return n < 8 ? 5 + (n >> 2) : (n - 8) >> 2; };
            unsigned aQ = 0;
            auto rdq = [&](int n) {
                if ((n & 3) == 0) { const unsigned sg = seg(step_of(n)); aQ = cq == 7 ? aq_pad + 8 * sg : aq_real + 4096 * sg; PINV(aQ); }
                fq[n % 3] = tr_frag(aQ + (n & 3) * 1024, aQ + (n & 3) * 1024 + 256);
            };
            auto rdx = [&](int n) { const unsigned a = (laneX ^ (32 * (n & 3))) + slot_of(step_of(n)); xl[n & 1][0] = lds_b128(a); xl[n & 1][1] = lds_b128(a + 4096); };
            rdq(0); rdx(0); rdq(1);
            SB();
#pragma unroll
            for (int n = 0; n < 28; ++n) {
                const int s = step_of(n), ks = n & 3;
                if (n + 1 < 8) rdx(n + 1);
                if (n + 2 < 28) rdq(n + 2);
                SB();
                qT[0] = MFMA(fq[n % 3], n >= 8 ? xs[n >= 8 ? s : 0][ks][0] : xl[n & 1][0], qT[0]);
                SB();
                qT[1] = MFMA(fq[n % 3], n >= 8 ? xs[n >= 8 ? s : 0][ks][1] : xl[n & 1][1], qT[1]);
                SB();
                if (n == 7) {
                    lds_set(L_XFREE + 4 * cw);
                }
            }
        }
        STAMP(6);
        bf16x8 eq[2][2][2];
        float gs[2];
        {
            f32x16 pq[2];
            const bf16x8 q00 = pack8(qT[0], 0), q10 = pack8(qT[1], 0);
            pq[0] = MFMA(aphi[0], q00, zero16); pq[1] = MFMA(aphi[0], q10, zero16);
            const bf16x8 q01 = pack8(qT[0], 1), q11 = pack8(qT[1], 1);
            pq[0] = MFMA(aphi[1], q01, pq[0]); pq[1] = MFMA(aphi[1], q11, pq[1]);
#pragma unroll
            for (int b = 0; b < 2; ++b) {
                const float lg = swap_hi(qT[b][12]);
                gs[b] = __builtin_amdgcn_rcpf(1.f + __builtin_amdgcn_exp2f(br ? lg * L2E : -lg * L2E));
                PINV(gs[b]);
            }
            float cp[2], cn[2];
            row_extrema(pq[0], pq[1], cp, cn);
            PINV(cp[0]); PINV(cp[1]); PINV(cn[0]); PINV(cn[1]);
#pragma unroll
            for (int b = 0; b < 2; ++b) {
                const f32x2 l2 = {L2E, L2E}, cp2 = {cp[b], cp[b]}, cn2 = {cn[b], cn[b]};
                unsigned pe[8], ne[8];
#pragma unroll
                for (int i = 0; i < 7; ++i) {
                    const f32x2 v = {pq[b][2 * i], pq[b][2 * i + 1]};
                    const f32x2 a = __builtin_elementwise_fma(v, l2, cp2), c = __builtin_elementwise_fma(v, -l2, cn2);
                    pe[i] = cvtpk2((f32x2){__builtin_amdgcn_exp2f(a[0]), __builtin_amdgcn_exp2f(a[1])});
                    ne[i] = cvtpk2((f32x2){__builtin_amdgcn_exp2f(c[0]), __builtin_amdgcn_exp2f(c[1])});
                }
                pe[7] = 0; ne[7] = 0;
                eq[b][0][0] = __builtin_bit_cast(bf16x8, (u32x4){pe[0], pe[1], pe[2], pe[3]}); eq[b][0][1] = __builtin_bit_cast(bf16x8, (u32x4){pe[4], pe[5], pe[6], pe[7]});
                eq[b][1][0] = __builtin_bit_cast(bf16x8, (u32x4){ne[0], ne[1], ne[2], ne[3]}); eq[b][1][1] = __builtin_bit_cast(bf16x8, (u32x4){ne[4], ne[5], ne[6], ne[7]});
            }
        }
#pragma unroll
        for (int b = 0; b < 2; ++b) asm volatile("" : "+v"(eq[b][0][0]), "+v"(eq[b][0][1]), "+v"(eq[b][1][0]), "+v"(eq[b][1][1]), "+v"(gs[b]));
        STAMP(7);
        LGKM_BAR();
        __builtin_amdgcn_s_setprio(3);
        STAMP(8);
        {
            bf16x8 akv[4];
#pragma unroll
            for (int f = 0; f < 4; ++f) akv[f] = lds_b128(L_KVA + f * 1024 + lane * 16);
            f32x16 np[2], nn[2];
#pragma unroll
            for (int b = 0; b < 2; ++b) { np[b] = MFMA(akv[0], eq[b][0][0], zero16); nn[b] = MFMA(akv[2], eq[b][1][0], zero16); }
#pragma unroll
            for (int b = 0; b < 2; ++b) { np[b] = MFMA(akv[1], eq[b][0][1], np[b]); nn[b] = MFMA(akv[3], eq[b][1][1], nn[b]); }
            SB();
            const unsigned so = rfl(WS_O + (((tb * 4 + cw) * 32 + bh) * 4) * 1024);
#pragma unroll
            for (int b = 0; b < 2; ++b) {
                const float ia = __builtin_amdgcn_rcpf(np[b][15]), ib = __builtin_amdgcn_rcpf(nn[b][15]);
                const float den = np[b][14] * ia + nn[b][14] * ib + 1e-6f;
                const float sc = gs[b] * __builtin_amdgcn_rcpf(den);
                const float ca = ia * sc, cb = ib * sc;
                float o[14];
#pragma unroll
                for (int i = 0; i < 14; ++i) o[i] = np[b][i] * ca + nn[b][i] * cb;
                const u32x4 o0 = {cvtpk(o[0], o[1]), cvtpk(o[2], o[3]), cvtpk(o[4], o[5]), cvtpk(o[6], o[7])};
                const u32x4 o1 = {cvtpk(o[8], o[9]), cvtpk(o[10], o[11]), cvtpk(o[12], o[13]), 0u};
                __builtin_amdgcn_raw_buffer_store_b128(o0, rws, lane * 16, so + b * 2048, 16);
                __builtin_amdgcn_raw_buffer_store_b128(o1, rws, lane * 16, so + b * 2048 + 1024, 16);
            }
        }
        STAMP(9);
        if (has_out) {
            const unsigned vwo = (lane >> 3) * XROW + 16 * ((lane & 7) ^ (4 * ((lane >> 4) & 1)));
            dma_rows<8>(mk_rsrc(p.woB, D_ * XROW), vwo, (640 - 448 + 64 * cw) * XROW + oc * 128, L_WO + (640 + 64 * cw) * 128);
            WAIT_VM(8);
        } else WAIT_VM(0);
        set_flag(p.ws, WS_F2 + (((tb * 4 + cw) * 2 + br) * 16 + h) * FS, tag, lane);
        STAMP(10);
        if (has_out) { WAIT_VM(1); lds_count(L_WOCNT, lane); }
        STAMP_FLUSH();
        return;
    }

    u64 tag;
    {
        const rsrc_t rg = mk_rsrc(p.gate, XROW);
        const rsrc_t rp = mk_rsrc(br ? p.wphiB : p.wphiA, 16 * DH * DH * 2);
        unsigned g0, g1; u32x2 wp; u64 ev;
        asm volatile("global_load_dwordx2 %3, %9, %10 sc1\n\t"
                     "buffer_load_ushort %0, %4, %5, 0 offen\n\tbuffer_load_ushort %1, %4, %5, 0 offen offset:512\n\tbuffer_load_dwordx2 %2, %6, %7, %8 offen\n\t"
                     "s_waitcnt vmcnt(0)"
                     : "=&v"(g0), "=&v"(g1), "=&v"(wp), "=&v"(ev)
                     : "v"(tid * 2), "s"(rg), "v"(tid * 8), "s"(rp), "s"(h * (DH * DH * 2)), "v"(0u), "s"(p.ws) : "memory");
        tag = uniform64(ev) + 1;
        *(LAS u32x2*)lds_at(L_GT + (tid & 63) * 64 + (tid >> 6) * 8) = (u32x2){g0 & 0xffffu, 0u};
        *(LAS u32x2*)lds_at(L_GT + (tid & 63) * 64 + (4 + (tid >> 6)) * 8) = (u32x2){g1 & 0xffffu, 0u};
        *(LAS u32x2*)lds_at(L_WPHI + tid * 8) = wp;
        if (tid < 16) *(volatile LAS unsigned*)lds_at(L_SYNC + 4 * tid) = 0u;
    }
    __builtin_amdgcn_s_setprio(CPRIO);
    LGKM_BAR();
    STAMP(2);
    bf16x8 aphi[2];
    build_aphi(aphi, r, h2);
    PINV(aphi[0]); PINV(aphi[1]);
    const unsigned laneW = SLOT_W + rowpart + coloff;
    f32x16 vinit;
#pragma unroll
    for (int i = 0; i < 16; ++i) vinit[i] = r == 28 ? 1.f : 0.f;
    PINV(vinit);
    f32x16 kT[2] = {zero16, zero16}, vv[2] = {vinit, vinit};
    {
        unsigned aX[4], aK, aV;
        auto addr = [&](int s) {
#pragma unroll
            for (int ks = 0; ks < 4; ++ks) { aX[ks] = (laneX ^ (32 * ks)) + slot_of(s); PINV(aX[ks]); }
            aK = cq == 7 ? L_GT + rowpart + 56 : laneW + slot_of(s); PINV(aK);
            aV = cq == 7 ? L_GT + rowpart + 56 : laneW + slot_of(s) + 4096; PINV(aV);
        };
        Frag F[2];
#define RD_X(f, ks) do { f.x0 = lds_b128(aX[ks]); } while (0)
#define RD_X1(f, ks) do { f.x1 = lds_b128(aX[ks] + 4096); } while (0)
#define RD_K(f, ks) do { f.fk = tr_frag(aK + ks * 1024, aK + ks * 1024 + 256); } while (0)
#define RD_V(f, ks) do { f.fv = tr_frag(aV + ks * 1024, aV + ks * 1024 + 256); } while (0)
        addr(0);
        RD_X(F[0], 0); RD_K(F[0], 0); RD_X1(F[0], 0); RD_V(F[0], 0); SB();
#pragma unroll
        for (int n = 0; n < 28; ++n) {
            Frag& c = F[n & 1];
            Frag& x = F[(n + 1) & 1];
            const int nks = (n + 1) & 3;
            const bool more = n + 1 < 28;
#define PACE_() do { if (PACE && n < 24) { _Pragma("unroll") for (int q_ = 0; q_ < PACE; ++q_) asm volatile("s_nop 7"); SB(); } } while (0)
            kT[0] = MFMA(c.fk, c.x0, kT[0]); SB();
            if (more && nks == 0) { LGKM_BAR(); addr((n + 1) >> 2); SB(); }
            if (more) { RD_X(x, nks); RD_K(x, nks); } SB();
            PACE_();
            kT[1] = MFMA(c.fk, c.x1, kT[1]); SB();
            if (more) { RD_X1(x, nks); } SB();
            PACE_();
            vv[0] = MFMA(c.x0, c.fv, vv[0]); SB();
            if (more) { RD_V(x, nks); } SB();
            PACE_();
            vv[1] = MFMA(c.x1, c.fv, vv[1]); SB();
            PACE_();
        }
    }
    STAMP(3);

    const unsigned phiW = L_PHI + cw * 8192;
    {
        const unsigned pt0 = 4 * h2 + q4;
        const unsigned pa0 = phiW + pt0 * 64 + 8 * (cq ^ ((pt0 >> 1) & 7));
        const unsigned pa1 = (pa0 ^ 32) + 8 * 64;
        f32x16 pk[2];
        const bf16x8 k00 = pack8(kT[0], 0), k10 = pack8(kT[1], 0);
        pk[0] = MFMA(aphi[0], k00, zero16); pk[1] = MFMA(aphi[0], k10, zero16);
        const bf16x8 k01 = pack8(kT[0], 1), k11 = pack8(kT[1], 1);
        pk[0] = MFMA(aphi[1], k01, pk[0]); pk[1] = MFMA(aphi[1], k11, pk[1]);
        bf16x8 vp[2][2];
#pragma unroll
        for (int b = 0; b < 2; ++b)
#pragma unroll
            for (int s = 0; s < 2; ++s) { vp[b][s] = pack8(vv[b], s); PINV(vp[b][s]); }
        SB();
        float cp[2], cn[2];
        row_extrema(pk[0], pk[1], cp, cn);
        f32x2 ep[2][7], en[2][7];
        exp_pair(pk[0], cp[0], cn[0], ep[0], en[0]);
        phi_write(ep[0], en[0], phiW, r, h2);
        exp_pair(pk[1], cp[1], cn[1], ep[1], en[1]);
        f32x16 kvp = zero16, kvn = zero16;
        auto kv = [&](int b) {
#pragma unroll
            for (int s = 0; s < 2; ++s) {
                const unsigned o = b * 4096 + s * 1024;
                kvp = MFMA(tr_frag(pa0 + o, pa1 + o), vp[b][s], kvp);
                kvn = MFMA(tr_frag(pa0 + o + 2048, pa1 + o + 2048), vp[b][s], kvn);
            }
        };
        kv(0);
        phi_write(ep[1], en[1], phiW + 4096, r, h2);
        kv(1);
        asm volatile("s_waitcnt lgkmcnt(0)" ::: "memory");
#pragma unroll
        for (int f = 0; f < 4; ++f)
#pragma unroll
            for (int hh = 0; hh < 2; ++hh) {
                const f32x16& t = f < 2 ? kvp : kvn;
                const int o = 8 * (f & 1) + 4 * hh;
                *(LAS f32x4*)lds_at(phiW + f * 2048 + hh * 1024 + lane * 16) = (f32x4){t[o], t[o + 1], t[o + 2], t[o + 3]};
            }
    }
    STAMP(4);
    LGKM_BAR();
    STAMP(5);
    {
        f32x4 t[4][2];
#pragma unroll
        for (int u = 0; u < 4; ++u) {
            t[u][0] = *(const LAS f32x4*)lds_at(L_PHI + u * 8192 + cw * 2048 + lane * 16);
            t[u][1] = *(const LAS f32x4*)lds_at(L_PHI + u * 8192 + cw * 2048 + 1024 + lane * 16);
        }
        SB();
        float a[8];
#pragma unroll
        for (int i = 0; i < 8; ++i) {
            float x01 = addf(t[0][i >> 2][i & 3], t[1][i >> 2][i & 3]), x23 = addf(t[2][i >> 2][i & 3], t[3][i >> 2][i & 3]);
            a[i] = addf(x01, x23);
        }
        u32x4 o = {cvtpk(a[0], a[1]), cvtpk(a[2], a[3]), cvtpk(a[4], a[5]), cvtpk(a[6], a[7])};
        STAMPX(1, o);
        __builtin_amdgcn_raw_buffer_store_b128(o, rws, lane * 16, kvp_mine, 16);
    }
    const rsrc_t roA = mk_rsrc(p.woA, D_ * XROW), roB = mk_rsrc(p.woB, D_ * XROW);
    const unsigned vwo = (lane >> 3) * XROW + 16 * ((lane & 7) ^ (4 * ((lane >> 4) & 1)));
    const rsrc_t roC = cw < 2 ? roA : roB;
    const unsigned R5 = 320 + 64 * cw, R5g = cw < 2 ? R5 : R5 - 448;
    {
        bf16x8 sel;
        {
            const int jj = (r & 3) + 4 * (r >> 3);
            const bool on = r < 16 && ((r >> 2) & 1) == h2;
            const unsigned one = on ? (0x3F80u << (16 * (jj & 1))) : 0u;
            sel = __builtin_bit_cast(bf16x8, (u32x4){(jj >> 1) == 0 ? one : 0u, (jj >> 1) == 1 ? one : 0u, (jj >> 1) == 2 ? one : 0u, (jj >> 1) == 3 ? one : 0u});
            PINV(sel);
        }
        dma_rows<2>(roA, vwo, (256 + 16 * cw) * XROW + oc * 128, L_WO + (256 + 16 * cw) * 128);
        dma_rows<2>(roB, vwo, (576 - 448 + 16 * cw) * XROW + oc * 128, L_WO + (576 + 16 * cw) * 128);
        lds_wait_ge(L_XFREE + 4 * cw, 1);
        dma_rows<6>(roA, vwo, 64 * cw * XROW + oc * 128, L_WO + 64 * cw * 128);
        const int hr = (r >> 2) & 1, reg = (r & 3) + 4 * (r >> 3);
        const int col = reg < 14 ? 14 * hr + reg : 28;
        const unsigned vo = (h2 * 32 + col) * 16, so = WS_KVP + (bh * 8 * 4 + cw) * 1024;
        u32x4 part[8];
        STAMP(9);
#pragma unroll
        for (int u = 0; u < 8; ++u) part[u] = load_sc1_asm(rws, vo, so + u * 4096);
#define PARTS "+v"(part[0]), "+v"(part[1]), "+v"(part[2]), "+v"(part[3]), "+v"(part[4]), "+v"(part[5]), "+v"(part[6]), "+v"(part[7])
        dma_rows<2>(roA, vwo, (64 * cw + 48) * XROW + oc * 128, L_WO + (64 * cw + 48) * 128);
        dma_rows<8>(roC, vwo, R5g * XROW + oc * 128, L_WO + R5 * 128);
        asm volatile("s_waitcnt vmcnt(10)" : PARTS :: "memory");
        STAMP(6);
        auto pattern_left = [&]() {
            unsigned m = 0;
#pragma unroll
            for (int u = 0; u < 8; ++u) { m = umax3(m, part[u][0], part[u][1]); m = umax3(m, part[u][2], part[u][3]); }
            return __builtin_amdgcn_ballot_w64(m == ~0u) != 0;
        };
        if (pattern_left()) {
            unsigned spins = 0;
#pragma unroll 1
            do {
#pragma unroll
                for (int u = 0; u < 8; ++u) part[u] = load_sc1_asm(rws, vo, so + u * 4096);
                asm volatile("s_waitcnt vmcnt(0)" : PARTS :: "memory");
            } while (pattern_left() && ++spins < SPIN_CAP);
        }
#undef PARTS
        STAMP(7);
        SB();
        f32x16 acc = zero16;
#pragma unroll
        for (int u = 0; u < 8; ++u) acc = MFMA(sel, __builtin_bit_cast(bf16x8, part[u]), acc);
        u32x4 o = __builtin_bit_cast(u32x4, pack8(acc, 0));
        if (reg == 15) o = (u32x4){ONE2, ONE2, ONE2, ONE2};
        *(LAS u32x4*)lds_at(L_KVA + cw * 1024 + lane * 16) = o;
    }
    LGKM_BAR();
    STAMP(8);
    if (!has_out) { STAMP_FLUSH(); return; }
    WAIT_VM(0); lds_count(L_WOCNT, lane);

    const unsigned tk = cw & 1, kh = cw >> 1;
    f32x16 mine = zero16, other = zero16;
    {
        const unsigned colp = 32 * blk16 + 8 * p4;
        const unsigned r0 = kh * 448 + 14 * h2 + q4, r1 = kh * 448 + 14 * h2 + (q4 < 1 ? q4 : 1);
        unsigned a0 = L_WO + r0 * 128 + 64 * (kh ^ ((r0 >> 1) & 1)) + colp;
        unsigned a1 = L_WO + r1 * 128 + 64 * (kh ^ ((r1 >> 1) & 1)) + colp;
        unsigned a2 = a0 ^ 64, a3 = a1 ^ 64;
        asm volatile("" : "+v"(a0), "+v"(a1), "+v"(a2), "+v"(a3));
        lds_wait_ge(L_WOCNT, 8);
        STAMP(10);
        if (cw == 0) {
            wait_flags((const GAS u64*)((const char*)p.ws + WS_F2 + (og * 32 + (lane & 31)) * FS), tag);
            lds_set(L_F2OK);
        } else lds_wait_ge(L_F2OK, 1);
        STAMP(11);
        u32x4 of[16][2];
        auto ld = [&](int i) {
#pragma unroll
            for (int s = 0; s < 2; ++s)
                of[i][s] = __builtin_amdgcn_raw_buffer_load_b128(rws, lane * 16, WS_O + (((og * 32 + kh * 16 + i) * 2 + tk) * 2 + s) * 1024, 16);
        };
        bf16x8 A[2][4];
        auto rd = [&](bf16x8 (&f)[4], int i) {
            const unsigned o = i * 28 * 128;
            f[0] = tr_frag(a0 + o, a0 + o + 512); f[1] = tr_frag(a2 + o, a2 + o + 512);
            f[2] = tr_frag(a0 + o + 1024, a1 + o + 1536); f[3] = tr_frag(a2 + o + 1024, a3 + o + 1536);
        };
#pragma unroll
        for (int i = 0; i < OD; ++i) ld(i);
        SB();
        rd(A[0], 0); SB();
#pragma unroll
        for (int i = 0; i < 16; ++i) {
            if (i + OD < 16) ld(i + OD);
            if (i + 1 < 16) rd(A[(i + 1) & 1], i + 1);
            SB();
            const bf16x8 b0 = __builtin_bit_cast(bf16x8, of[i][0]), b1 = __builtin_bit_cast(bf16x8, of[i][1]);
            mine = MFMA(A[i & 1][0], b0, mine); other = MFMA(A[i & 1][1], b0, other);
            mine = MFMA(A[i & 1][2], b1, mine); other = MFMA(A[i & 1][3], b1, other);
            SB();
        }
    }
    STAMP(12);
#pragma unroll
    for (int g = 0; g < 4; ++g)
        *(LAS f32x4*)lds_at(L_PHI + cw * 4096 + g * 1024 + lane * 16) = (f32x4){other[4 * g], other[4 * g + 1], other[4 * g + 2], other[4 * g + 3]};
    LGKM_BAR();
    if (blockIdx.x == 0 && tid == 0) __hip_atomic_store((GAS u64*)((char*)p.ws + WS_EPOCH), tag, __ATOMIC_RELAXED, __HIP_MEMORY_SCOPE_AGENT);
    {
        unsigned d[8];
        f32x4 t[4];
#pragma unroll
        for (int g = 0; g < 4; ++g) t[g] = *(const LAS f32x4*)lds_at(L_PHI + (cw ^ 2) * 4096 + g * 1024 + lane * 16);
        SB();
#pragma unroll
        for (int g = 0; g < 4; ++g) {
            d[2 * g] = cvtpk(mine[4 * g] + t[g][0], mine[4 * g + 1] + t[g][1]);
            d[2 * g + 1] = cvtpk(mine[4 * g + 2] + t[g][2], mine[4 * g + 3] + t[g][3]);
        }
        const rsrc_t rout = mk_rsrc(p.out, S_ * XROW);
        const unsigned vo = r * XROW + h2 * 16, so = (og * 64 + tk * 32) * XROW + oc * 128 + kh * 64;
#pragma unroll
        for (int k = 0; k < 4; k += 2) {
            auto sx0 = __builtin_amdgcn_permlane32_swap(d[2 * k], d[2 * k + 2], false, false);
            auto sx1 = __builtin_amdgcn_permlane32_swap(d[2 * k + 1], d[2 * k + 3], false, false);
            const u32x4 o = {sx0[0], sx1[0], sx0[1], sx1[1]};
            __builtin_amdgcn_raw_buffer_store_b128(o, rout, vo, so + 16 * k, 0);
        }
    }
    STAMP(13);
    STAMP_FLUSH();
}

extern "C" void kernel_launch(void* const* d_in, const int* in_sizes, int n_in, void* d_out, int out_size, void* d_ws, size_t ws_size,
                              hipStream_t stream) {
    Params p;
    p.x = d_in[0]; p.wqkvA = d_in[1]; p.wphiA = d_in[2]; p.woA = d_in[3];
    p.wqkvB = d_in[5]; p.wphiB = d_in[6]; p.woB = d_in[7]; p.gate = d_in[8];
    p.out = d_out; p.ws = d_ws;
    (void)hipFuncSetAttribute((const void*)hedgehog_mix_kernel, hipFuncAttributeMaxDynamicSharedMemorySize, L_TOTAL);
    hedgehog_mix_kernel<<<256, 512, L_TOTAL, stream>>>(p);
}
```

```cpp
#define PREFETCH 2
#define PF_WO_SEG 3
#define PF_CODE 1
#define POLL_DEPTH 3
#define FS 8
#include <hip/hip_runtime.h>
#include <stdint.h>

typedef short bf16x8 __attribute__((ext_vector_type(8)));
typedef short s16x4 __attribute__((ext_vector_type(4)));
typedef short v4i16_t __attribute__((ext_vector_type(4)));
typedef __bf16 bf2_t __attribute__((ext_vector_type(2)));
typedef float f32x2 __attribute__((ext_vector_type(2)));
typedef float f32x4 __attribute__((ext_vector_type(4)));
typedef float f32x16 __attribute__((ext_vector_type(16)));
typedef unsigned u32x2 __attribute__((ext_vector_type(2)));
typedef unsigned u32x4 __attribute__((ext_vector_type(4)));
typedef unsigned long long u64;
#define LAS __attribute__((address_space(3)))
#define GAS __attribute__((address_space(1)))
#define DI __device__ __forceinline__
#define MFMA(a, b, c) __builtin_amdgcn_mfma_f32_32x32x16_bf16((a), (b), (c), 0, 0, 0)

constexpr int S_ = 2048, D_ = 448, DH = 28;
constexpr unsigned XROW = D_ * 2;
constexpr unsigned WROW = 3 * D_ * 2;
constexpr unsigned WS_EPOCH = 0;
constexpr unsigned F1_GRP = FS * 8 < 128 ? 128 : FS * 8, F2_GRP = FS * 16;
constexpr unsigned WS_F1 = 4096;
constexpr unsigned WS_F2 = WS_F1 + 32 * 4 * 1024;
constexpr unsigned WS_KVP = WS_F2 + 64 * 2048;
constexpr unsigned WS_O = WS_KVP + 32 * 8 * 4 * 1024;
constexpr unsigned WS_END = WS_O + 32 * 32 * 4 * 1024;
constexpr unsigned WS_STAMP = 8u << 20;
constexpr unsigned SLOT = 40960, SLOT_W = 32768;
constexpr unsigned L_B = 0, L_A = SLOT, L_C = 2 * SLOT;
constexpr unsigned L_WQ = 3 * SLOT;
constexpr unsigned L_GT = L_WQ + 448 * 64;
constexpr unsigned L_WPHI = L_GT + 4096;
constexpr unsigned L_SCR = L_WPHI + 2048;
constexpr unsigned L_XFREE = L_SCR + 512;
constexpr unsigned L_WOCNT = L_XFREE + 16;
constexpr unsigned L_PHI = L_B;
constexpr unsigned L_KVA = L_B + SLOT_W;
constexpr unsigned L_WO = L_A;
constexpr unsigned L_TOTAL = 163840;
static_assert(L_WPHI + 2048 <= L_TOTAL, "lds");
static_assert(L_WO + 896 * 128 == L_WPHI, "lds");
__host__ __device__ constexpr unsigned slot_of(int s) { return s % 3 == 0 ? L_A : (s % 3 == 1 ? L_B : L_C); }

struct Params {
    const void* x; const void* wqkvA; const void* wqkvB; const void* wphiA; const void* wphiB;
    const void* woA; const void* woB; const void* gate; void* out; void* ws;
};

typedef __amdgpu_buffer_rsrc_t rsrc_t;
DI rsrc_t mk_rsrc(const void* p, unsigned bytes) { return __builtin_amdgcn_make_buffer_rsrc((void*)p, (short)0, (int)bytes, 0x00020000); }
DI unsigned rfl(unsigned v) { return (unsigned)__builtin_amdgcn_readfirstlane((int)v); }
DI void dma16(rsrc_t r, unsigned voff, unsigned soff, unsigned lds) {
    asm volatile("s_mov_b32 m0, %0\n\ts_nop 0\n\tbuffer_load_dwordx4 %1, %2, %3 offen lds" ::"s"(lds), "v"(voff), "s"(r), "s"(soff) : "memory");
}
DI void dma_seg(rsrc_t rw, rsrc_t rx, unsigned vw, unsigned vx0, unsigned vx1, unsigned sw, unsigned sx, unsigned mw, unsigned mx) {
#define XP(v) "s_addk_i32 m0, 0x400\n\ts_addk_i32 %1, 0x1c00\n\tbuffer_load_dwordx4 " v ", %6, %1 offen lds\n\t"
    asm volatile("s_mov_b32 m0, %7\n\ts_nop 0\n\tbuffer_load_dwordx4 %2, %5, %0 offen lds\n\t"
                 "s_addk_i32 m0, 0x1000\n\ts_addk_i32 %0, 0x380\n\tbuffer_load_dwordx4 %2, %5, %0 offen lds\n\t"
                 "s_mov_b32 m0, %8\n\ts_nop 0\n\tbuffer_load_dwordx4 %3, %6, %1 offen lds\n\t"
                 XP("%4") XP("%3") XP("%4") XP("%3") XP("%4") XP("%3") XP("%4")
                 : "+s"(sw), "+s"(sx) : "v"(vw), "v"(vx0), "v"(vx1), "s"(rw), "s"(rx), "s"(mw), "s"(mx) : "scc", "memory");
#undef XP
}
template <int N> DI void dma_rows(rsrc_t r, unsigned v, unsigned so, unsigned m) {
    static_assert(N == 2 || N == 6 || N == 8, "");
#define RP "s_addk_i32 m0, 0x400\n\ts_addk_i32 %0, 0x1c00\n\tbuffer_load_dwordx4 %1, %2, %0 offen lds\n\t"
#define R0 "s_mov_b32 m0, %3\n\ts_nop 0\n\tbuffer_load_dwordx4 %1, %2, %0 offen lds\n\t"
    if (N == 2) asm volatile(R0 RP : "+s"(so) : "v"(v), "s"(r), "s"(m) : "scc", "memory");
    if (N == 6) asm volatile(R0 RP RP RP RP RP : "+s"(so) : "v"(v), "s"(r), "s"(m) : "scc", "memory");
    if (N == 8) asm volatile(R0 RP RP RP RP RP RP RP : "+s"(so) : "v"(v), "s"(r), "s"(m) : "scc", "memory");
#undef RP
#undef R0
}
DI void dma4(rsrc_t r, unsigned voff, unsigned soff, unsigned lds) {
    asm volatile("s_mov_b32 m0, %0\n\ts_nop 0\n\tbuffer_load_dword %1, %2, %3 offen lds" ::"s"(lds), "v"(voff), "s"(r), "s"(soff) : "memory");
}
#define PF_CODE_LINES 168
DI void gdma4(const void* a, unsigned lds) {
    asm volatile("s_mov_b32 m0, %0\n\ts_nop 0\n\tglobal_load_lds_dword %1, off" ::"s"(lds), "v"(a) : "memory");
}
#define PF_STAG 0
#define PF_WQ_SEG 1
#define WAIT_VM(n) asm volatile("s_waitcnt vmcnt(" #n ")" ::: "memory")
#define WAIT_VM_LGKM_BAR(n) asm volatile("s_waitcnt vmcnt(" #n ") lgkmcnt(0)\n\ts_barrier" ::: "memory")
#define LGKM_BAR() asm volatile("s_waitcnt lgkmcnt(0)\n\ts_barrier" ::: "memory")

extern __shared__ __attribute__((aligned(16))) char smem_dyn[];
DI LAS char* lds_at(unsigned off) { return (LAS char*)(uintptr_t)off; }
DI bf16x8 lds_b128(unsigned a) { return *(const LAS bf16x8*)lds_at(a); }
DI s16x4 lds_tr(unsigned a) { return __builtin_bit_cast(s16x4, __builtin_amdgcn_ds_read_tr16_b64_v4i16((LAS v4i16_t*)lds_at(a))); }
DI bf16x8 tr_frag(unsigned a0, unsigned a1) { const s16x4 lo = lds_tr(a0), hi = lds_tr(a1); return __builtin_shufflevector(lo, hi, 0, 1, 2, 3, 4, 5, 6, 7); }
DI unsigned cvtpk(float lo, float hi) { const f32x2 v = {lo, hi}; return __builtin_bit_cast(unsigned, __builtin_convertvector(v, bf2_t)); }
DI bf16x8 pack8(const f32x16& x, int s) {
    const u32x4 p = {cvtpk(x[8 * s], x[8 * s + 1]), cvtpk(x[8 * s + 2], x[8 * s + 3]), cvtpk(x[8 * s + 4], x[8 * s + 5]), cvtpk(x[8 * s + 6], x[8 * s + 7])};
    return __builtin_bit_cast(bf16x8, p);
}
DI float addf(float a, float b) { float r; asm("v_add_f32 %0, %1, %2" : "=v"(r) : "v"(a), "v"(b)); return r; }
DI float swap_lo(float v) { auto r = __builtin_amdgcn_permlane32_swap(__float_as_uint(v), __float_as_uint(v), false, false); return __uint_as_float(r[0]); }
DI float swap_hi(float v) { auto r = __builtin_amdgcn_permlane32_swap(__float_as_uint(v), __float_as_uint(v), false, false); return __uint_as_float(r[1]); }
DI float xhalf_max(float v) { auto r = __builtin_amdgcn_permlane32_swap(__float_as_uint(v), __float_as_uint(v), false, false); return __builtin_fmaxf(__uint_as_float(r[0]), __uint_as_float(r[1])); }
DI float xhalf_min(float v) { auto r = __builtin_amdgcn_permlane32_swap(__float_as_uint(v), __float_as_uint(v), false, false); return __builtin_fminf(__uint_as_float(r[0]), __uint_as_float(r[1])); }
DI float xhalf_sum(float v) { auto r = __builtin_amdgcn_permlane32_swap(__float_as_uint(v), __float_as_uint(v), false, false); return __uint_as_float(r[0]) + __uint_as_float(r[1]); }
constexpr float L2E = 1.4426950408889634f;
constexpr unsigned ONE2 = 0x3F803F80u;
constexpr unsigned SPIN_CAP = 1u << 16;

#define STAMP_DECL do { } while (0)
#define STAMP(i) do { } while (0)
#define STAMPX(i, x) do { } while (0)
#define STAMP_FLUSH() do { } while (0)

DI void row_extrema(const f32x16& a, const f32x16& b, float (&cp)[2], float (&cn)[2]) {
    float mxa = __builtin_fmaxf(a[0], a[1]), mna = __builtin_fminf(a[0], a[1]), mxb = __builtin_fmaxf(b[0], b[1]), mnb = __builtin_fminf(b[0], b[1]);
#pragma unroll
    for (int i = 2; i < 14; i += 2) {
        mxa = __builtin_fmaxf(__builtin_fmaxf(mxa, a[i]), a[i + 1]); mxb = __builtin_fmaxf(__builtin_fmaxf(mxb, b[i]), b[i + 1]);
        mna = __builtin_fminf(__builtin_fminf(mna, a[i]), a[i + 1]); mnb = __builtin_fminf(__builtin_fminf(mnb, b[i]), b[i + 1]);
    }
    cp[0] = -xhalf_max(mxa) * L2E; cp[1] = -xhalf_max(mxb) * L2E;
    cn[0] = xhalf_min(mna) * L2E; cn[1] = xhalf_min(mnb) * L2E;
}
DI void exp_pair(const f32x16& pp, float cp, float cn, f32x2 (&ep)[7], f32x2 (&en)[7]) {
    const f32x2 l2 = {L2E, L2E}, cp2 = {cp, cp}, cn2 = {cn, cn};
#pragma unroll
    for (int i = 0; i < 7; ++i) {
        const f32x2 v = {pp[2 * i], pp[2 * i + 1]};
        const f32x2 a = __builtin_elementwise_fma(v, l2, cp2), b = __builtin_elementwise_fma(v, -l2, cn2);
        ep[i] = (f32x2){__builtin_amdgcn_exp2f(a[0]), __builtin_amdgcn_exp2f(a[1])};
        en[i] = (f32x2){__builtin_amdgcn_exp2f(b[0]), __builtin_amdgcn_exp2f(b[1])};
    }
}
DI unsigned cvtpk2(f32x2 v) { return __builtin_bit_cast(unsigned, __builtin_convertvector(v, bf2_t)); }

DI void wait_flags(const GAS u64* f, u64 tag) {
    u64 a, b, c; unsigned n = SPIN_CAP;
    asm volatile(
        "global_load_dwordx2 %0, %4, off sc1\n\t"
        "s_sleep 3\n\t"
        "global_load_dwordx2 %1, %4, off sc1\n\t"
        "s_sleep 3\n\t"
        "global_load_dwordx2 %2, %4, off sc1\n"
        "1:\n\t"
        "s_waitcnt vmcnt(2)\n\t"
        "v_cmp_ne_u64 vcc, %0, %5\n\t"
        "s_cbranch_vccz 2f\n\t"
        "global_load_dwordx2 %0, %4, off sc1\n\t"
        "s_waitcnt vmcnt(2)\n\t"
        "v_cmp_ne_u64 vcc, %1, %5\n\t"
        "s_cbranch_vccz 2f\n\t"
        "global_load_dwordx2 %1, %4, off sc1\n\t"
        "s_waitcnt vmcnt(2)\n\t"
        "v_cmp_ne_u64 vcc, %2, %5\n\t"
        "s_cbranch_vccz 2f\n\t"
        "global_load_dwordx2 %2, %4, off sc1\n\t"
        "s_sub_u32 %3, %3, 1\n\t"
        "s_cmp_lg_u32 %3, 0\n\t"
        "s_cbranch_scc1 1b\n"
        "2:\n\t"
        "s_waitcnt vmcnt(0)"
        : "=&v"(a), "=&v"(b), "=&v"(c), "+s"(n) : "v"(f), "s"(tag) : "vcc", "scc", "memory");
}

#define SB() __builtin_amdgcn_sched_barrier(0)
#define PINV(x) asm volatile("" : "+v"(x))
struct Frag { bf16x8 x0, x1, fk, fv; };

DI void build_aphi(bf16x8 (&aphi)[2], int r, int h2) {
    const int hr = (r >> 2) & 1, reg = (r & 3) + 4 * (r >> 3), f = 14 * hr + (reg < 14 ? reg : 0);
    const unsigned a = L_WPHI + 2 * (4 * h2 * DH + f);
#pragma unroll
    for (int s = 0; s < 2; ++s)
#pragma unroll
        for (int jj = 0; jj < 8; ++jj) {
            const int d0 = 16 * s + 8 * (jj >> 2) + (jj & 3);
            const bool ok = reg < 14 && (d0 + 4 < DH || h2 == 0);
            const unsigned short v = *(const LAS unsigned short*)lds_at(a + 2 * d0 * DH);
            aphi[s][jj] = ok ? (short)v : (short)0;
        }
}

__global__ void __launch_bounds__(512, 2) hedgehog_mix_kernel(Params p) {
    const int tid = threadIdx.x, lane = tid & 63;
    const unsigned w = rfl(tid >> 6), cw = w & 3;
    if (w == 4) {
        u64 pc; asm volatile("s_getpc_b64 %0" : "=s"(pc));
        const unsigned l0 = (blockIdx.x >> 3) * 6 + (lane < 6 ? lane : 5), l = l0 < PF_CODE_LINES ? l0 : 0;
        gdma4((const char*)(pc & ~127ull) + l * 128, L_SCR + 256);
    }
    const int r = lane & 31, h2 = lane >> 5, i16 = lane & 15, q4 = i16 >> 2, p4 = i16 & 3, blk16 = (lane >> 4) & 1;
    const unsigned xcd = blockIdx.x & 7, j = blockIdx.x >> 3;
    const unsigned tb = (xcd & 1) * 4 + (j & 3), bh = (xcd >> 1) * 8 + (j >> 2), br = bh >> 4, h = bh & 15;
    const unsigned og = xcd * 4 + j / 7, oc = j % 7;
    const bool has_out = j < 28;
#define PIN_ARGS() asm volatile("" : "+s"(p.x), "+s"(p.wqkvA), "+s"(p.wqkvB), "+s"(p.wphiA), "+s"(p.wphiB), "+s"(p.woA), "+s"(p.woB), "+s"(p.gate), "+s"(p.out), "+s"(p.ws))
    PIN_ARGS();
    STAMP_DECL;
    STAMP(0);
    const rsrc_t rws = mk_rsrc(p.ws, WS_END);
    const f32x16 zero16 = {};
    const int cq = 4 * blk16 + p4;
    const unsigned rowpart = (8 * h2 + q4) * 64;
    const unsigned coloff = (8 * (h & 1) + 8 * cq) & 63;
    const unsigned laneX = cw * 8192 + r * 128 + 16 * (h2 ^ ((r >> 1) & 7));

    const char* const pfW = (const char*)(br ? p.wqkvB : p.wqkvA);
    const unsigned wcol = ((xcd >> 1) & 1) * 384;
    auto pf_addr = [&](unsigned l, int s) -> const char* {
        const unsigned m = (l - 32) & 15;
        return l < 32 ? (const char*)p.x + ((xcd & 1) * 1024 + j * 32 + l) * XROW + s * 128
                      : pfW + (64 * s + 2 * j + (m >> 3)) * WROW + 896 + ((m >> 2) & 1) * 896 + wcol + (m & 3) * 128;
    };
    if (__builtin_expect(w >= 4, 1)) {
        const rsrc_t rx = mk_rsrc(p.x, S_ * XROW);
        const rsrc_t rw = mk_rsrc(br ? p.wqkvB : p.wqkvA, D_ * WROW);
        const unsigned vx0 = (lane >> 3) * XROW + 16 * ((lane & 7) ^ (lane >> 4));
        const unsigned vx1 = (lane >> 3) * XROW + 16 * ((lane & 7) ^ (4 + (lane >> 4)));
        const unsigned vw = (lane >> 2) * WROW + 16 * (lane & 3);
        const unsigned sx = (tb * 256 + cw * 64) * XROW;
        const unsigned sw = cw * 16 * WROW + h * 56 - 8 * (h & 1);
        auto issue = [&](int s) {
            dma_seg(rw, rx, vw, vx0, vx1, sw + s * 64 * WROW + 896, sx + s * 128, slot_of(s) + SLOT_W + cw * 1024, slot_of(s) + cw * 8192);
        };
        bf16x8 xs[5][4][2];
        auto grab = [&](int s) {
#pragma unroll
            for (int ks = 0; ks < 4; ++ks) {
                const unsigned a = (laneX ^ (32 * ks)) + slot_of(s);
                xs[s][ks][0] = lds_b128(a); xs[s][ks][1] = lds_b128(a + 4096);
            }
        };
        asm volatile("s_barrier" ::: "memory");
        STAMP(11);
        issue(0); issue(1);
        STAMP(1);
        WAIT_VM_LGKM_BAR(10);
        STAMP(2);
        grab(0); issue(2); WAIT_VM_LGKM_BAR(10);
        grab(1); issue(3); WAIT_VM_LGKM_BAR(10);
        grab(2); issue(4); WAIT_VM_LGKM_BAR(10);
        grab(3); issue(5); WAIT_VM_LGKM_BAR(10);
        grab(4); issue(6); WAIT_VM_LGKM_BAR(10);
        WAIT_VM_LGKM_BAR(0);
        STAMP(3);
#pragma unroll
        for (int s = 0; s < 7; ++s) dma16(rw, vw, sw + s * 64 * WROW, L_WQ + s * 4096 + cw * 1024);
        bf16x8 aphi[2];
        build_aphi(aphi, r, h2);
        PINV(aphi[0]); PINV(aphi[1]);
        u64 tag;
        asm volatile("s_waitcnt lgkmcnt(0)\n\ts_load_dwordx2 %0, %1, 0x0\n\ts_waitcnt lgkmcnt(0)" : "=&s"(tag) : "s"(p.ws) : "memory");
        tag += 1;
        STAMP(4);
        WAIT_VM_LGKM_BAR(0);
        STAMP(5);
        f32x16 qT[2] = {zero16, zero16};
        {
            const unsigned aq_real = L_WQ + rowpart + coloff, aq_pad = L_GT + rowpart;
            bf16x8 fq[3], xl[2][2];
            auto seg_of = [](int n) { return n < 8 ? 5 + (n >> 2) : (n - 8) >> 2; };
            unsigned aQ = 0;
            auto rdq = [&](int n) {
                if ((n & 3) == 0) { aQ = cq == 7 ? aq_pad + 8 * seg_of(n) : aq_real + 4096 * seg_of(n); PINV(aQ); }
                fq[n % 3] = tr_frag(aQ + (n & 3) * 1024, aQ + (n & 3) * 1024 + 256);
            };
            auto rdx = [&](int n) { const unsigned a = (laneX ^ (32 * (n & 3))) + slot_of(seg_of(n)); xl[n & 1][0] = lds_b128(a); xl[n & 1][1] = lds_b128(a + 4096); };
            rdq(0); rdx(0); rdq(1);
            SB();
#pragma unroll
            for (int n = 0; n < 28; ++n) {
                const int s = seg_of(n), ks = n & 3;
                if (n + 1 < 8) rdx(n + 1);
                if (n + 2 < 28) rdq(n + 2);
                SB();
                qT[0] = MFMA(fq[n % 3], n >= 8 ? xs[n >= 8 ? s : 0][ks][0] : xl[n & 1][0], qT[0]);
                qT[1] = MFMA(fq[n % 3], n >= 8 ? xs[n >= 8 ? s : 0][ks][1] : xl[n & 1][1], qT[1]);
                SB();
                if (n == 7) {
                    asm volatile("s_waitcnt lgkmcnt(0)" ::: "memory");
                    *(volatile LAS unsigned*)lds_at(L_XFREE + 4 * cw) = 1u;
                }
            }
        }
        STAMP(6);
        bf16x8 eq[2][2][2];
        float gs[2];
        {
            f32x16 pq[2];
            const bf16x8 q00 = pack8(qT[0], 0), q10 = pack8(qT[1], 0);
            pq[0] = MFMA(aphi[0], q00, zero16); pq[1] = MFMA(aphi[0], q10, zero16);
            const bf16x8 q01 = pack8(qT[0], 1), q11 = pack8(qT[1], 1);
            pq[0] = MFMA(aphi[1], q01, pq[0]); pq[1] = MFMA(aphi[1], q11, pq[1]);
#pragma unroll
            for (int b = 0; b < 2; ++b) {
                const float lg = swap_hi(qT[b][12]);
                gs[b] = __builtin_amdgcn_rcpf(1.f + __builtin_amdgcn_exp2f(br ? lg * L2E : -lg * L2E));
                PINV(gs[b]);
            }
            float cp[2], cn[2];
            row_extrema(pq[0], pq[1], cp, cn);
            PINV(cp[0]); PINV(cp[1]); PINV(cn[0]); PINV(cn[1]);
#pragma unroll
            for (int b = 0; b < 2; ++b) {
                const f32x2 l2 = {L2E, L2E}, cp2 = {cp[b], cp[b]}, cn2 = {cn[b], cn[b]};
                unsigned pe[8], ne[8];
#pragma unroll
                for (int i = 0; i < 7; ++i) {
                    const f32x2 v = {pq[b][2 * i], pq[b][2 * i + 1]};
                    const f32x2 a = __builtin_elementwise_fma(v, l2, cp2), c = __builtin_elementwise_fma(v, -l2, cn2);
                    pe[i] = cvtpk2((f32x2){__builtin_amdgcn_exp2f(a[0]), __builtin_amdgcn_exp2f(a[1])});
                    ne[i] = cvtpk2((f32x2){__builtin_amdgcn_exp2f(c[0]), __builtin_amdgcn_exp2f(c[1])});
                }
                pe[7] = 0; ne[7] = 0;
                eq[b][0][0] = __builtin_bit_cast(bf16x8, (u32x4){pe[0], pe[1], pe[2], pe[3]}); eq[b][0][1] = __builtin_bit_cast(bf16x8, (u32x4){pe[4], pe[5], pe[6], pe[7]});
                eq[b][1][0] = __builtin_bit_cast(bf16x8, (u32x4){ne[0], ne[1], ne[2], ne[3]}); eq[b][1][1] = __builtin_bit_cast(bf16x8, (u32x4){ne[4], ne[5], ne[6], ne[7]});
            }
        }
#pragma unroll
        for (int b = 0; b < 2; ++b) asm volatile("" : "+v"(eq[b][0][0]), "+v"(eq[b][0][1]), "+v"(eq[b][1][0]), "+v"(eq[b][1][1]), "+v"(gs[b]));
        STAMP(7);
        LGKM_BAR();
        __builtin_amdgcn_s_setprio(3);
        STAMP(8);
        {
            bf16x8 akv[4];
#pragma unroll
            for (int f = 0; f < 4; ++f) akv[f] = lds_b128(L_KVA + f * 1024 + lane * 16);
#pragma unroll
            for (int b = 0; b < 2; ++b) {
                f32x16 np = MFMA(akv[0], eq[b][0][0], zero16); np = MFMA(akv[1], eq[b][0][1], np);
                f32x16 nn = MFMA(akv[2], eq[b][1][0], zero16); nn = MFMA(akv[3], eq[b][1][1], nn);
                const float ia = __builtin_amdgcn_rcpf(np[15]), ib = __builtin_amdgcn_rcpf(nn[15]);
                const float den = np[14] * ia + nn[14] * ib + 1e-6f;
                const float sc = gs[b] * __builtin_amdgcn_rcpf(den);
                const float ca = ia * sc, cb = ib * sc;
                float o[14];
#pragma unroll
                for (int i = 0; i < 14; ++i) o[i] = np[i] * ca + nn[i] * cb;
                const u32x4 o0 = {cvtpk(o[0], o[1]), cvtpk(o[2], o[3]), cvtpk(o[4], o[5]), cvtpk(o[6], o[7])};
                const u32x4 o1 = {cvtpk(o[8], o[9]), cvtpk(o[10], o[11]), cvtpk(o[12], o[13]), 0u};
                const unsigned so = WS_O + (((tb * 4 + cw) * 32 + bh) * 4 + b * 2) * 1024;
                __builtin_amdgcn_raw_buffer_store_b128(o0, rws, lane * 16, so, 16);
                __builtin_amdgcn_raw_buffer_store_b128(o1, rws, lane * 16, so + 1024, 16);
            }
        }
        STAMP(9);
        WAIT_VM(0);
        if (lane == 0) __hip_atomic_store((GAS u64*)((char*)p.ws + WS_F2 + ((tb * 4 + cw) * 2 + br) * F2_GRP + h * FS), tag, __ATOMIC_RELAXED, __HIP_MEMORY_SCOPE_AGENT);
        STAMP(10);
        STAMP_FLUSH();
        return;
    }

    auto pf_seg = [&](int s) { gdma4(pf_addr(lane < 48 ? lane : 0, s), L_SCR + 256); };
    auto pf_wq = [&]() { const unsigned l = lane < 56 ? lane : 0; gdma4(pfW + (j * 14 + (l >> 2)) * WROW + wcol + (l & 3) * 128, L_SCR + 256); };
    auto pf_wo = [&](int part) {
        const unsigned i0 = part * 64 + lane, i = i0 < 196 ? i0 : 0, R = j * 28 + i / 7;
        gdma4((const char*)(j < 16 ? p.woA : p.woB) + (R - (j < 16 ? 0 : 448)) * XROW + (i % 7) * 128, L_SCR + 256);
    };
    if (cw < 2) pf_seg(cw); else gdma4(p.gate, L_SCR + 256);
    u64 tag;
    {
        const rsrc_t rg = mk_rsrc(p.gate, XROW);
        const rsrc_t rp = mk_rsrc(br ? p.wphiB : p.wphiA, 16 * DH * DH * 2);
        unsigned g0, g1; u32x2 wp;
        asm volatile("s_load_dwordx2 %0, %1, 0x0" : "=&s"(tag) : "s"(p.ws) : "memory");
        asm volatile("buffer_load_ushort %0, %3, %4, 0 offen\n\tbuffer_load_ushort %1, %3, %4, 0 offen offset:512\n\tbuffer_load_dwordx2 %2, %5, %6, %7 offen"
                     : "=&v"(g0), "=&v"(g1), "=&v"(wp) : "v"(tid * 2), "s"(rg), "v"(tid * 8), "s"(rp), "s"(h * (DH * DH * 2)) : "memory");
        if (cw == 0) WAIT_VM(3);
        asm volatile("s_barrier" ::: "memory");
        if (cw >= 2) pf_seg(cw); else gdma4(p.gate, L_SCR + 256);
        asm volatile("s_waitcnt vmcnt(1) lgkmcnt(0)" : "+v"(g0), "+v"(g1), "+v"(wp), "+s"(tag) :: "memory");
        *(LAS u32x2*)lds_at(L_GT + (tid & 63) * 64 + (tid >> 6) * 8) = (u32x2){g0 & 0xffffu, 0u};
        *(LAS u32x2*)lds_at(L_GT + (tid & 63) * 64 + (4 + (tid >> 6)) * 8) = (u32x2){g1 & 0xffffu, 0u};
        *(LAS u32x2*)lds_at(L_WPHI + tid * 8) = wp;
        if (tid < 5) *(volatile LAS unsigned*)lds_at(L_XFREE + 4 * tid) = 0u;
    }
    tag += 1;
    __builtin_amdgcn_s_setprio(2);
    LGKM_BAR();
    STAMP(2);
    bf16x8 aphi[2];
    build_aphi(aphi, r, h2);
    PINV(aphi[0]); PINV(aphi[1]);
    const unsigned laneW = SLOT_W + rowpart + coloff;
    f32x16 vinit;
#pragma unroll
    for (int i = 0; i < 16; ++i) vinit[i] = r == 28 ? 1.f : 0.f;
    PINV(vinit);
    f32x16 kT[2] = {zero16, zero16}, vv[2] = {vinit, vinit};
    {
        unsigned aX[4], aW, aV;
        auto addr = [&](int s) {
#pragma unroll
            for (int ks = 0; ks < 4; ++ks) { aX[ks] = (laneX ^ (32 * ks)) + slot_of(s); PINV(aX[ks]); }
            aW = laneW + slot_of(s); PINV(aW);
            aV = cq == 7 ? L_GT + rowpart + 56 : aW + 4096; PINV(aV);
        };
        Frag F[2];
#define RD_X(f, ks) do { f.x0 = lds_b128(aX[ks]); } while (0)
#define RD_X1(f, ks) do { f.x1 = lds_b128(aX[ks] + 4096); } while (0)
#define RD_K(f, ks) do { f.fk = tr_frag(aW + ks * 1024, aW + ks * 1024 + 256); } while (0)
#define RD_V(f, ks) do { f.fv = tr_frag(aV + ks * 1024, aV + ks * 1024 + 256); } while (0)
        addr(0);
        RD_X(F[0], 0); RD_K(F[0], 0); RD_X1(F[0], 0); RD_V(F[0], 0); SB();
#pragma unroll
        for (int n = 0; n < 28; ++n) {
            Frag& c = F[n & 1];
            Frag& x = F[(n + 1) & 1];
            const int nks = (n + 1) & 3;
            const bool more = n + 1 < 28;
            kT[0] = MFMA(c.fk, c.x0, kT[0]); SB();
            if (more && nks == 0) { LGKM_BAR(); addr((n + 1) >> 2); SB(); }
            if (n == 0) { if (cw < 2) pf_seg(4 + cw); SB(); }
            if (n == 4) { if (cw == 2) pf_seg(6); if (cw == 3) pf_wq(); SB(); }
            if (n == 1 + 4 * PF_WO_SEG) { pf_wo(cw); SB(); }
            if (more) { RD_X(x, nks); RD_K(x, nks); } SB();
            kT[1] = MFMA(c.fk, c.x1, kT[1]); SB();
            if (more) { RD_X1(x, nks); } SB();
            vv[0] = MFMA(c.x0, c.fv, vv[0]); SB();
            if (more) { RD_V(x, nks); } SB();
            vv[1] = MFMA(c.x1, c.fv, vv[1]); SB();
        }
    }
    STAMP(3);

    const unsigned phiW = L_PHI + cw * 8192;
    f32x16 pk[2];
    {
        const bf16x8 k00 = pack8(kT[0], 0), k10 = pack8(kT[1], 0);
        pk[0] = MFMA(aphi[0], k00, zero16); pk[1] = MFMA(aphi[0], k10, zero16);
        const bf16x8 k01 = pack8(kT[0], 1), k11 = pack8(kT[1], 1);
        pk[0] = MFMA(aphi[1], k01, pk[0]); pk[1] = MFMA(aphi[1], k11, pk[1]);
    }
    bf16x8 vp[2][2];
#pragma unroll
    for (int b = 0; b < 2; ++b)
#pragma unroll
        for (int s = 0; s < 2; ++s) { vp[b][s] = pack8(vv[b], s); PINV(vp[b][s]); }
    SB();
    float cp[2], cn[2];
    row_extrema(pk[0], pk[1], cp, cn);
    const unsigned pt0 = 4 * h2 + q4;
    const unsigned pa0 = phiW + pt0 * 64 + 8 * (cq ^ ((pt0 >> 1) & 7));
    const unsigned pa1 = (pa0 ^ 32) + 8 * 64;
    f32x16 kvp = zero16, kvn = zero16;
#pragma unroll
    for (int b = 0; b < 2; ++b) {
        f32x2 ep[7], en[7];
        exp_pair(pk[b], cp[b], cn[b], ep, en);
        f32x2 sp = ep[0], sn = en[0];
#pragma unroll
        for (int i = 1; i < 7; ++i) { sp += ep[i]; sn += en[i]; }
        const float ip = __builtin_amdgcn_rcpf(xhalf_sum(sp[0] + sp[1])), in = __builtin_amdgcn_rcpf(xhalf_sum(sn[0] + sn[1]));
        const f32x2 ip2 = {ip, ip}, in2 = {in, in};
        const unsigned base = phiW + b * 4096 + r * 64;
#pragma unroll
        for (int g = 0; g < 4; ++g) {
            const unsigned a = base + 8 * ((2 * g + h2) ^ ((r >> 1) & 7));
            const u32x2 dp = {cvtpk2(ep[2 * g] * ip2), g < 3 ? cvtpk2(ep[(2 * g + 1) % 7] * ip2) : 0u};
            const u32x2 dn = {cvtpk2(en[2 * g] * in2), g < 3 ? cvtpk2(en[(2 * g + 1) % 7] * in2) : 0u};
            *(LAS u32x2*)lds_at(a) = dp;
            *(LAS u32x2*)lds_at(a + 2048) = dn;
        }
#pragma unroll
        for (int s = 0; s < 2; ++s) {
            const unsigned o = b * 4096 + s * 1024;
            kvp = MFMA(tr_frag(pa0 + o, pa1 + o), vp[b][s], kvp);
            kvn = MFMA(tr_frag(pa0 + o + 2048, pa1 + o + 2048), vp[b][s], kvn);
        }
    }
    asm volatile("s_waitcnt lgkmcnt(0)" ::: "memory");
#pragma unroll
    for (int f = 0; f < 4; ++f)
#pragma unroll
        for (int hh = 0; hh < 2; ++hh) {
            const f32x16& t = f < 2 ? kvp : kvn;
            const int o = 8 * (f & 1) + 4 * hh;
            *(LAS f32x4*)lds_at(phiW + f * 2048 + hh * 1024 + lane * 16) = (f32x4){t[o], t[o + 1], t[o + 2], t[o + 3]};
        }
    STAMP(4);
    LGKM_BAR();
    STAMP(5);
    {
        f32x4 t[4][2];
#pragma unroll
        for (int u = 0; u < 4; ++u) {
            t[u][0] = *(const LAS f32x4*)lds_at(L_PHI + u * 8192 + cw * 2048 + lane * 16);
            t[u][1] = *(const LAS f32x4*)lds_at(L_PHI + u * 8192 + cw * 2048 + 1024 + lane * 16);
        }
        SB();
        float a[8];
#pragma unroll
        for (int i = 0; i < 8; ++i) {
            float x01 = addf(t[0][i >> 2][i & 3], t[1][i >> 2][i & 3]), x23 = addf(t[2][i >> 2][i & 3], t[3][i >> 2][i & 3]);
            a[i] = addf(x01, x23);
        }
        u32x4 o = {cvtpk(a[0], a[1]), cvtpk(a[2], a[3]), cvtpk(a[4], a[5]), cvtpk(a[6], a[7])};
        STAMPX(1, o);
        __builtin_amdgcn_raw_buffer_store_b128(o, rws, lane * 16, WS_KVP + ((bh * 8 + tb) * 4 + cw) * 1024, 16);
    }
    {
        const rsrc_t roA = mk_rsrc(p.woA, D_ * XROW), roB = mk_rsrc(p.woB, D_ * XROW);
        const unsigned vwo = (lane >> 3) * XROW + 16 * ((lane & 7) ^ (4 * ((lane >> 4) & 1)));
        dma_rows<2>(roA, vwo, (256 + 16 * cw) * XROW + oc * 128, L_WO + (256 + 16 * cw) * 128);
        dma_rows<2>(roB, vwo, (576 - 448 + 16 * cw) * XROW + oc * 128, L_WO + (576 + 16 * cw) * 128);
#pragma unroll 1
        for (unsigned spins = 0; spins < SPIN_CAP; ++spins) { if (*(volatile LAS unsigned*)lds_at(L_XFREE + 4 * cw) != 0u) break; }
        dma_rows<6>(roA, vwo, 64 * cw * XROW + oc * 128, L_WO + 64 * cw * 128);
        WAIT_VM(10);
        if (lane == 0) __hip_atomic_store((GAS u64*)((char*)p.ws + WS_F1 + (bh * 4 + cw) * F1_GRP + tb * FS), tag, __ATOMIC_RELAXED, __HIP_MEMORY_SCOPE_AGENT);
        STAMP(9);
        dma_rows<2>(roA, vwo, (64 * cw + 48) * XROW + oc * 128, L_WO + (64 * cw + 48) * 128);
        const rsrc_t roC = cw < 2 ? roA : roB;
        const unsigned R = 320 + 64 * cw, Rg = cw < 2 ? R : R - 448;
        dma_rows<8>(roC, vwo, Rg * XROW + oc * 128, L_WO + R * 128);
    }
    bf16x8 sel;
    {
        const int jj = (r & 3) + 4 * (r >> 3);
        const bool on = r < 16 && ((r >> 2) & 1) == h2;
        const unsigned one = on ? (0x3F80u << (16 * (jj & 1))) : 0u;
        sel = __builtin_bit_cast(bf16x8, (u32x4){(jj >> 1) == 0 ? one : 0u, (jj >> 1) == 1 ? one : 0u, (jj >> 1) == 2 ? one : 0u, (jj >> 1) == 3 ? one : 0u});
        PINV(sel);
    }
    STAMP(6);
    wait_flags((const GAS u64*)((const char*)p.ws + WS_F1 + (bh * 4 + cw) * F1_GRP + (lane & 7) * FS), tag);
    STAMP(7);
    {
        const int hr = (r >> 2) & 1, reg = (r & 3) + 4 * (r >> 3);
        const int col = reg < 14 ? 14 * hr + reg : 28;
        const unsigned vo = (h2 * 32 + col) * 16;
        u32x4 part[8];
#pragma unroll
        for (int u = 0; u < 8; ++u) part[u] = __builtin_amdgcn_raw_buffer_load_b128(rws, vo, WS_KVP + ((bh * 8 + u) * 4 + cw) * 1024, 16);
        SB();
        f32x16 acc = zero16;
#pragma unroll
        for (int u = 0; u < 8; ++u) acc = MFMA(sel, __builtin_bit_cast(bf16x8, part[u]), acc);
        u32x4 o = __builtin_bit_cast(u32x4, pack8(acc, 0));
        if (reg == 15) o = (u32x4){ONE2, ONE2, ONE2, ONE2};
        *(LAS u32x4*)lds_at(L_KVA + cw * 1024 + lane * 16) = o;
    }
    LGKM_BAR();
    STAMP(8);
    if (!has_out) { STAMP_FLUSH(); return; }
    {
        const rsrc_t ro = mk_rsrc(p.woB, D_ * XROW);
        const unsigned vo = (lane >> 3) * XROW + 16 * ((lane & 7) ^ (4 * ((lane >> 4) & 1)));
        dma_rows<8>(ro, vo, (640 - 448 + 64 * cw) * XROW + oc * 128, L_WO + (640 + 64 * cw) * 128);
    }
    WAIT_VM(0);
    if (lane == 0) __hip_atomic_fetch_add((LAS unsigned*)lds_at(L_WOCNT), 1u, __ATOMIC_RELAXED, __HIP_MEMORY_SCOPE_WORKGROUP);
#pragma unroll 1
    for (unsigned spins = 0; spins < SPIN_CAP; ++spins) { if (*(volatile LAS unsigned*)lds_at(L_WOCNT) >= 4u) break; }
    asm volatile("" ::: "memory");
    STAMP(10);

    const unsigned tk = cw & 1, kh = cw >> 1;
    constexpr u64 HORD = 0xDB9642FECA875310ull;
    const GAS u64* f2 = (const GAS u64*)((const char*)p.ws + WS_F2 + (og * 2 + kh) * F2_GRP);
    f32x16 mine = zero16, other = zero16;
    {
        const unsigned colp = 32 * blk16 + 8 * p4;
        const unsigned r0 = kh * 448 + 14 * h2 + q4, r1 = kh * 448 + 14 * h2 + (q4 < 1 ? q4 : 1);
        unsigned a0 = L_WO + r0 * 128 + 64 * (kh ^ ((r0 >> 1) & 1)) + colp;
        unsigned a1 = L_WO + r1 * 128 + 64 * (kh ^ ((r1 >> 1) & 1)) + colp;
        unsigned a2 = a0 ^ 64, a3 = a1 ^ 64;
        asm volatile("" : "+v"(a0), "+v"(a1), "+v"(a2), "+v"(a3));
        u32x4 of[16][2];
        auto ld = [&](int i) {
            const unsigned hd = (unsigned)(HORD >> (4 * i)) & 15u;
#pragma unroll
            for (int s = 0; s < 2; ++s)
                of[i][s] = __builtin_amdgcn_raw_buffer_load_b128(rws, lane * 16, WS_O + (((og * 32 + kh * 16 + hd) * 2 + tk) * 2 + s) * 1024, 16);
        };
        bf16x8 A[2][4];
        auto rd = [&](bf16x8 (&f)[4], int i) {
            const unsigned o = ((unsigned)(HORD >> (4 * i)) & 15u) * 28 * 128;
            f[0] = tr_frag(a0 + o, a0 + o + 512); f[1] = tr_frag(a2 + o, a2 + o + 512);
            f[2] = tr_frag(a0 + o + 1024, a1 + o + 1536); f[3] = tr_frag(a2 + o + 1024, a3 + o + 1536);
        };
        auto step = [&](int i) {
            if (i + 1 < 16) rd(A[(i + 1) & 1], i + 1);
            SB();
            const bf16x8 b0 = __builtin_bit_cast(bf16x8, of[i][0]), b1 = __builtin_bit_cast(bf16x8, of[i][1]);
            mine = MFMA(A[i & 1][0], b0, mine); other = MFMA(A[i & 1][1], b0, other);
            mine = MFMA(A[i & 1][2], b1, mine); other = MFMA(A[i & 1][3], b1, other);
            SB();
        };
        wait_flags(f2 + (FS / 8) * ((HORD >> (4 * (lane % 10))) & 15), tag);
        STAMP(14);
#pragma unroll
        for (int i = 0; i < 10; ++i) ld(i);
        SB();
        rd(A[0], 0); SB();
#pragma unroll
        for (int i = 0; i < 10; ++i) step(i);
        STAMP(15);
        wait_flags(f2 + (FS / 8) * ((HORD >> (4 * (10 + lane % 6))) & 15), tag);
        STAMP(11);
        if (blockIdx.x == 0 && tid == 0) __hip_atomic_store((GAS u64*)((char*)p.ws + WS_EPOCH), tag, __ATOMIC_RELAXED, __HIP_MEMORY_SCOPE_AGENT);
#pragma unroll
        for (int i = 10; i < 16; ++i) ld(i);
        SB();
#pragma unroll
        for (int i = 10; i < 16; ++i) step(i);
    }
    STAMP(12);
#pragma unroll
    for (int g = 0; g < 4; ++g)
        *(LAS f32x4*)lds_at(L_PHI + cw * 4096 + g * 1024 + lane * 16) = (f32x4){other[4 * g], other[4 * g + 1], other[4 * g + 2], other[4 * g + 3]};
    LGKM_BAR();
    {
        unsigned d[8];
#pragma unroll
        for (int g = 0; g < 4; ++g) {
            const f32x4 t = *(const LAS f32x4*)lds_at(L_PHI + (cw ^ 2) * 4096 + g * 1024 + lane * 16);
            d[2 * g] = cvtpk(mine[4 * g] + t[0], mine[4 * g + 1] + t[1]);
            d[2 * g + 1] = cvtpk(mine[4 * g + 2] + t[2], mine[4 * g + 3] + t[3]);
        }
        const rsrc_t rout = mk_rsrc(p.out, S_ * XROW);
        const unsigned vo = r * XROW + h2 * 16, so = (og * 64 + tk * 32) * XROW + oc * 128 + kh * 64;
#pragma unroll
        for (int k = 0; k < 4; k += 2) {
            auto sx0 = __builtin_amdgcn_permlane32_swap(d[2 * k], d[2 * k + 2], false, false);
            auto sx1 = __builtin_amdgcn_permlane32_swap(d[2 * k + 1], d[2 * k + 3], false, false);
            const u32x4 o = {sx0[0], sx1[0], sx0[1], sx1[1]};
            __builtin_amdgcn_raw_buffer_store_b128(o, rout, vo, so + 16 * k, 0);
        }
    }
    STAMP(13);
    STAMP_FLUSH();
}

extern "C" void kernel_launch(void* const* d_in, const int* in_sizes, int n_in, void* d_out, int out_size, void* d_ws, size_t ws_size,
                              hipStream_t stream) {
    Params p;
    p.x = d_in[0]; p.wqkvA = d_in[1]; p.wphiA = d_in[2]; p.woA = d_in[3];
    p.wqkvB = d_in[5]; p.wphiB = d_in[6]; p.woB = d_in[7]; p.gate = d_in[8];
    p.out = d_out; p.ws = d_ws;
    (void)hipFuncSetAttribute((const void*)hedgehog_mix_kernel, hipFuncAttributeMaxDynamicSharedMemorySize, L_TOTAL);
    hedgehog_mix_kernel<<<256, 512, L_TOTAL, stream>>>(p);
}
```

```cpp
#include <hip/hip_runtime.h>
#include <stdint.h>

typedef short bf16x8 __attribute__((ext_vector_type(8)));
typedef short s16x4 __attribute__((ext_vector_type(4)));
typedef short v4i16_t __attribute__((ext_vector_type(4)));
typedef __bf16 bf2_t __attribute__((ext_vector_type(2)));
typedef float f32x2 __attribute__((ext_vector_type(2)));
typedef float f32x4 __attribute__((ext_vector_type(4)));
typedef float f32x16 __attribute__((ext_vector_type(16)));
typedef unsigned u32x2 __attribute__((ext_vector_type(2)));
typedef unsigned u32x3 __attribute__((ext_vector_type(3)));
typedef unsigned u32x4 __attribute__((ext_vector_type(4)));
typedef unsigned long long u64;
#define LAS __attribute__((address_space(3)))
#define GAS __attribute__((address_space(1)))
#define DI __device__ __forceinline__
#define MFMA(a, b, c) __builtin_amdgcn_mfma_f32_32x32x16_bf16((a), (b), (c), 0, 0, 0)

constexpr int OD = 5;
constexpr unsigned PF_CODE_LINES = 168;

constexpr int S_ = 2048, D_ = 448, DH = 28;
constexpr unsigned XROW = D_ * 2;
constexpr unsigned WROW = 3 * D_ * 2;
constexpr unsigned FS = 128;
constexpr unsigned WS_EPOCH = 0;
constexpr unsigned WS_F1 = 4096;
constexpr unsigned WS_F2 = WS_F1 + 32 * 4 * 8 * FS;
constexpr unsigned WS_KVP = WS_F2 + 32 * 32 * FS;
constexpr unsigned WS_O = WS_KVP + 32 * 8 * 4 * 1024;
constexpr unsigned WS_END = WS_O + 32 * 32 * 4 * 1024;
constexpr unsigned SLOT = 40960, SLOT_W = 32768;
constexpr unsigned L_B = 0, L_A = SLOT, L_C = 2 * SLOT;
constexpr unsigned L_WQ = 3 * SLOT;
constexpr unsigned L_GT = L_WQ + 448 * 64;
constexpr unsigned L_WPHI = L_GT + 4096;
constexpr unsigned L_SCR = L_WPHI + 2048;
constexpr unsigned L_SYNC = L_SCR + 256;
constexpr unsigned L_XFREE = L_SYNC;
constexpr unsigned L_WOCNT = L_SYNC + 16;
constexpr unsigned L_F2OK = L_SYNC + 32;
constexpr unsigned L_PHI = L_B;
constexpr unsigned L_KVA = L_B + SLOT_W;
constexpr unsigned L_WO = L_A;
constexpr unsigned L_TOTAL = 163840;
static_assert(L_SYNC + 64 <= L_TOTAL, "lds");
static_assert(L_WO + 896 * 128 == L_WPHI, "lds");
__host__ __device__ constexpr unsigned slot_of(int s) { return s % 3 == 0 ? L_A : (s % 3 == 1 ? L_B : L_C); }

struct Params {
    const void* x; const void* wqkvA; const void* wqkvB; const void* wphiA; const void* wphiB;
    const void* woA; const void* woB; const void* gate; void* out; void* ws;
};

typedef __amdgpu_buffer_rsrc_t rsrc_t;
DI rsrc_t mk_rsrc(const void* p, unsigned bytes) { return __builtin_amdgcn_make_buffer_rsrc((void*)p, (short)0, (int)bytes, 0x00020000); }
DI unsigned rfl(unsigned v) { return (unsigned)__builtin_amdgcn_readfirstlane((int)v); }
DI u64 uniform64(u64 v) { return (u64)rfl((unsigned)v) | ((u64)rfl((unsigned)(v >> 32)) << 32); }
DI void dma16(rsrc_t r, unsigned voff, unsigned soff, unsigned lds) {
    asm volatile("s_mov_b32 m0, %0\n\ts_nop 0\n\tbuffer_load_dwordx4 %1, %2, %3 offen lds" ::"s"(lds), "v"(voff), "s"(r), "s"(soff) : "memory");
}
DI void dma_seg(rsrc_t rw, rsrc_t rx, unsigned vw, unsigned vx0, unsigned vx1, unsigned sw, unsigned sx, unsigned mw, unsigned mx) {
#define XP(v) "s_addk_i32 m0, 0x400\n\ts_addk_i32 %1, 0x1c00\n\tbuffer_load_dwordx4 " v ", %6, %1 offen lds\n\t"
    asm volatile("s_mov_b32 m0, %7\n\ts_nop 0\n\tbuffer_load_dwordx4 %2, %5, %0 offen lds\n\t"
                 "s_addk_i32 m0, 0x1000\n\ts_addk_i32 %0, 0x380\n\tbuffer_load_dwordx4 %2, %5, %0 offen lds\n\t"
                 "s_mov_b32 m0, %8\n\ts_nop 0\n\tbuffer_load_dwordx4 %3, %6, %1 offen lds\n\t"
                 XP("%4") XP("%3") XP("%4") XP("%3") XP("%4") XP("%3") XP("%4")
                 : "+s"(sw), "+s"(sx) : "v"(vw), "v"(vx0), "v"(vx1), "s"(rw), "s"(rx), "s"(mw), "s"(mx) : "scc", "memory");
#undef XP
}
template <int N> DI void dma_rows(rsrc_t r, unsigned v, unsigned so, unsigned m) {
    static_assert(N == 2 || N == 3 || N == 5 || N == 6 || N == 8, "");
#define RP "s_addk_i32 m0, 0x400\n\ts_addk_i32 %0, 0x1c00\n\tbuffer_load_dwordx4 %1, %2, %0 offen lds\n\t"
#define R0 "s_mov_b32 m0, %3\n\ts_nop 0\n\tbuffer_load_dwordx4 %1, %2, %0 offen lds\n\t"
    if (N == 2) asm volatile(R0 RP : "+s"(so) : "v"(v), "s"(r), "s"(m) : "scc", "memory");
    if (N == 3) asm volatile(R0 RP RP : "+s"(so) : "v"(v), "s"(r), "s"(m) : "scc", "memory");
    if (N == 5) asm volatile(R0 RP RP RP RP : "+s"(so) : "v"(v), "s"(r), "s"(m) : "scc", "memory");
    if (N == 6) asm volatile(R0 RP RP RP RP RP : "+s"(so) : "v"(v), "s"(r), "s"(m) : "scc", "memory");
    if (N == 8) asm volatile(R0 RP RP RP RP RP RP RP : "+s"(so) : "v"(v), "s"(r), "s"(m) : "scc", "memory");
#undef RP
#undef R0
}
DI u32x4 load_sc1_asm(rsrc_t r, unsigned voff, unsigned soff) {
    u32x4 v; asm volatile("buffer_load_dwordx4 %0, %1, %2, %3 offen sc1" : "=&v"(v) : "v"(voff), "s"(r), "s"(soff) : "memory"); return v;
}
DI unsigned umax3(unsigned a, unsigned b, unsigned c) { const unsigned t = a > b ? a : b; return t > c ? t : c; }
DI void gdma4(const void* a, unsigned lds) {
    asm volatile("s_mov_b32 m0, %0\n\ts_nop 0\n\tglobal_load_lds_dword %1, off" ::"s"(lds), "v"(a) : "memory");
}
#define WAIT_VM(n) asm volatile("s_waitcnt vmcnt(" #n ")" ::: "memory")
#define WAIT_VM_LGKM_BAR(n) asm volatile("s_waitcnt vmcnt(" #n ") lgkmcnt(0)\n\ts_barrier" ::: "memory")
#define LGKM_BAR() asm volatile("s_waitcnt lgkmcnt(0)\n\ts_barrier" ::: "memory")
#define SB() __builtin_amdgcn_sched_barrier(0)
#define PINV(x) asm volatile("" : "+v"(x))

extern __shared__ __attribute__((aligned(16))) char smem_dyn[];
DI LAS char* lds_at(unsigned off) { return (LAS char*)(uintptr_t)off; }
DI bf16x8 lds_b128(unsigned a) { return *(const LAS bf16x8*)lds_at(a); }
DI s16x4 lds_tr(unsigned a) { return __builtin_bit_cast(s16x4, __builtin_amdgcn_ds_read_tr16_b64_v4i16((LAS v4i16_t*)lds_at(a))); }
DI bf16x8 tr_frag(unsigned a0, unsigned a1) { const s16x4 lo = lds_tr(a0), hi = lds_tr(a1); return __builtin_shufflevector(lo, hi, 0, 1, 2, 3, 4, 5, 6, 7); }
DI unsigned cvtpk(float lo, float hi) { const f32x2 v = {lo, hi}; return __builtin_bit_cast(unsigned, __builtin_convertvector(v, bf2_t)); }
DI bf16x8 pack8(const f32x16& x, int s) {
    const u32x4 p = {cvtpk(x[8 * s], x[8 * s + 1]), cvtpk(x[8 * s + 2], x[8 * s + 3]), cvtpk(x[8 * s + 4], x[8 * s + 5]), cvtpk(x[8 * s + 6], x[8 * s + 7])};
    return __builtin_bit_cast(bf16x8, p);
}
DI float addf(float a, float b) { float r; asm("v_add_f32 %0, %1, %2" : "=v"(r) : "v"(a), "v"(b)); return r; }
DI float swap_hi(float v) { auto r = __builtin_amdgcn_permlane32_swap(__float_as_uint(v), __float_as_uint(v), false, false); return __uint_as_float(r[1]); }
DI float xhalf_max(float v) { auto r = __builtin_amdgcn_permlane32_swap(__float_as_uint(v), __float_as_uint(v), false, false); return __builtin_fmaxf(__uint_as_float(r[0]), __uint_as_float(r[1])); }
DI float xhalf_min(float v) { auto r = __builtin_amdgcn_permlane32_swap(__float_as_uint(v), __float_as_uint(v), false, false); return __builtin_fminf(__uint_as_float(r[0]), __uint_as_float(r[1])); }
DI float xhalf_sum(float v) { auto r = __builtin_amdgcn_permlane32_swap(__float_as_uint(v), __float_as_uint(v), false, false); return __uint_as_float(r[0]) + __uint_as_float(r[1]); }
constexpr float L2E = 1.4426950408889634f;
constexpr unsigned ONE2 = 0x3F803F80u;
constexpr unsigned SPIN_CAP = 1u << 16;


DI void row_extrema(const f32x16& a, const f32x16& b, float (&cp)[2], float (&cn)[2]) {
    float mxa = __builtin_fmaxf(a[0], a[1]), mna = __builtin_fminf(a[0], a[1]), mxb = __builtin_fmaxf(b[0], b[1]), mnb = __builtin_fminf(b[0], b[1]);
#pragma unroll
    for (int i = 2; i < 14; i += 2) {
        mxa = __builtin_fmaxf(__builtin_fmaxf(mxa, a[i]), a[i + 1]); mxb = __builtin_fmaxf(__builtin_fmaxf(mxb, b[i]), b[i + 1]);
        mna = __builtin_fminf(__builtin_fminf(mna, a[i]), a[i + 1]); mnb = __builtin_fminf(__builtin_fminf(mnb, b[i]), b[i + 1]);
    }
    cp[0] = -xhalf_max(mxa) * L2E; cp[1] = -xhalf_max(mxb) * L2E;
    cn[0] = xhalf_min(mna) * L2E; cn[1] = xhalf_min(mnb) * L2E;
}
DI unsigned cvtpk2(f32x2 v) { return __builtin_bit_cast(unsigned, __builtin_convertvector(v, bf2_t)); }
DI void exp_pair(const f32x16& pp, float cp, float cn, f32x2 (&ep)[7], f32x2 (&en)[7]) {
#pragma unroll
    for (int i = 0; i < 7; ++i) {
        const f32x2 l2 = {L2E, L2E}, cp2 = {cp, cp}, cn2 = {cn, cn};
        const f32x2 v = {pp[2 * i], pp[2 * i + 1]};
        const f32x2 a = __builtin_elementwise_fma(v, l2, cp2), b = __builtin_elementwise_fma(v, -l2, cn2);
        ep[i] = (f32x2){__builtin_amdgcn_exp2f(a[0]), __builtin_amdgcn_exp2f(a[1])};
        en[i] = (f32x2){__builtin_amdgcn_exp2f(b[0]), __builtin_amdgcn_exp2f(b[1])};
    }
}
DI float sum14(const f32x2 (&e)[7]) {
    f32x2 s = e[0];
#pragma unroll
    for (int i = 1; i < 7; ++i) s += e[i];
    return s[0] + s[1];
}
DI unsigned scale_pack(f32x2 v, float s) {
    return cvtpk2(v * (f32x2){s, s});
}
DI void phi_write(const f32x2 (&ep)[7], const f32x2 (&en)[7], unsigned base, int r, int h2) {
    const float ip = __builtin_amdgcn_rcpf(xhalf_sum(sum14(ep))), in = __builtin_amdgcn_rcpf(xhalf_sum(sum14(en)));
#pragma unroll
    for (int g = 0; g < 4; ++g) {
        const unsigned a = base + r * 64 + 8 * ((2 * g + h2) ^ ((r >> 1) & 7));
        const u32x2 dp = {scale_pack(ep[2 * g], ip), g < 3 ? scale_pack(ep[(2 * g + 1) % 7], ip) : 0u};
        const u32x2 dn = {scale_pack(en[2 * g], in), g < 3 ? scale_pack(en[(2 * g + 1) % 7], in) : 0u};
        *(LAS u32x2*)lds_at(a) = dp;
        *(LAS u32x2*)lds_at(a + 2048) = dn;
    }
}
DI void build_aphi(bf16x8 (&aphi)[2], int r, int h2) {
    const int hr = (r >> 2) & 1, reg = (r & 3) + 4 * (r >> 3), f = 14 * hr + (reg < 14 ? reg : 0);
    const unsigned a = L_WPHI + 2 * (4 * h2 * DH + f);
#pragma unroll
    for (int s = 0; s < 2; ++s)
#pragma unroll
        for (int jj = 0; jj < 8; ++jj) {
            const int d0 = 16 * s + 8 * (jj >> 2) + (jj & 3);
            const bool ok = reg < 14 && (d0 + 4 < DH || h2 == 0);
            const unsigned short v = *(const LAS unsigned short*)lds_at(a + 2 * d0 * DH);
            aphi[s][jj] = ok ? (short)v : (short)0;
        }
}

DI void lds_wait_ge(unsigned addr, unsigned v) {
#pragma unroll 1
    for (unsigned spins = 0; spins < SPIN_CAP * 16; ++spins) {
        if (*(volatile LAS unsigned*)lds_at(addr) >= v) break;
        __builtin_amdgcn_s_sleep(1);
    }
    asm volatile("" ::: "memory");
}
DI void lds_set(unsigned addr) { asm volatile("s_waitcnt lgkmcnt(0)" ::: "memory"); *(volatile LAS unsigned*)lds_at(addr) = 1u; }
DI void lds_count(unsigned addr, int lane) {
    asm volatile("s_waitcnt lgkmcnt(0)" ::: "memory");
    if (lane == 0) __hip_atomic_fetch_add((LAS unsigned*)lds_at(addr), 1u, __ATOMIC_RELAXED, __HIP_MEMORY_SCOPE_WORKGROUP);
}
DI void wait_flags(const GAS u64* f, u64 tag) {
#pragma unroll 1
    for (unsigned spins = 0; spins < SPIN_CAP; ++spins) {
        const u64 v = __hip_atomic_load(f, __ATOMIC_RELAXED, __HIP_MEMORY_SCOPE_AGENT);
        if (__builtin_amdgcn_ballot_w64(v != tag) == 0) break;
        __builtin_amdgcn_s_sleep(1);
    }
    asm volatile("" ::: "memory");
}
DI void set_flag(void* ws, unsigned off, u64 tag, int lane) {
    if (lane == 0) __hip_atomic_store((GAS u64*)((char*)ws + off), tag, __ATOMIC_RELAXED, __HIP_MEMORY_SCOPE_AGENT);
}

struct Frag { bf16x8 x0, x1, fk, fv; };

__global__ void __launch_bounds__(512, 2) hedgehog_mix_kernel(Params p) {
    const int tid = threadIdx.x, lane = tid & 63;
    const unsigned w = rfl(tid >> 6), cw = w & 3;
    if (w == 4) {
        u64 pc; asm volatile("s_getpc_b64 %0" : "=s"(pc));
        const unsigned l0 = (blockIdx.x >> 3) * 6 + (lane < 6 ? lane : 5), l = l0 < PF_CODE_LINES ? l0 : 0;
        gdma4((const char*)(pc & ~127ull) + l * 128, L_SCR);
    }
    const int r = lane & 31, h2 = lane >> 5, i16 = lane & 15, q4 = i16 >> 2, p4 = i16 & 3, blk16 = (lane >> 4) & 1;
    const unsigned xcd = blockIdx.x & 7, j = blockIdx.x >> 3;
    const unsigned tb = (xcd & 1) * 4 + (j & 3), bh = (xcd >> 1) * 8 + (j >> 2), br = bh >> 4, h = bh & 15;
    const unsigned og = xcd * 4 + j / 7, oc = j % 7;
    const bool has_out = j < 28;
    const unsigned rot = ((j >> 2) + 2 * (j & 3)) % 7;
    auto seg = [&](int n) { const unsigned t = rot + n; return t >= 7 ? t - 7 : t; };
    asm volatile("" : "+s"(p.x), "+s"(p.wqkvA), "+s"(p.wqkvB), "+s"(p.wphiA), "+s"(p.wphiB), "+s"(p.woA), "+s"(p.woB), "+s"(p.gate), "+s"(p.out), "+s"(p.ws));
    const rsrc_t rws = mk_rsrc(p.ws, WS_END);
    const f32x16 zero16 = {};
    const int cq = 4 * blk16 + p4;
    const unsigned rowpart = (8 * h2 + q4) * 64;
    const unsigned coloff = (8 * (h & 1) + 8 * cq) & 63;
    const unsigned laneX = cw * 8192 + r * 128 + 16 * (h2 ^ ((r >> 1) & 7));
    const unsigned kvp_mine = WS_KVP + ((bh * 8 + tb) * 4 + cw) * 1024;
    const unsigned f1_grp = WS_F1 + (bh * 4 + cw) * 8 * FS;

    if (__builtin_expect(w >= 4, 1)) {
        const rsrc_t rx = mk_rsrc(p.x, S_ * XROW);
        const rsrc_t rw = mk_rsrc(br ? p.wqkvB : p.wqkvA, D_ * WROW);
        const unsigned vx0 = (lane >> 3) * XROW + 16 * ((lane & 7) ^ (lane >> 4));
        const unsigned vx1 = (lane >> 3) * XROW + 16 * ((lane & 7) ^ (4 + (lane >> 4)));
        const unsigned vw = (lane >> 2) * WROW + 16 * (lane & 3);
        const unsigned sx = (tb * 256 + cw * 64) * XROW;
        const unsigned sw = cw * 16 * WROW + h * 56 - 8 * (h & 1);
        auto issue = [&](int s) {
            dma_seg(rw, rx, vw, vx0, vx1, sw + seg(s) * (64 * WROW) + 896, sx + seg(s) * 128, slot_of(s) + SLOT_W + cw * 1024, slot_of(s) + cw * 8192);
        };
        auto issue_wq = [&](int s) { const unsigned sg = seg(s); dma16(rw, vw, sw + sg * (64 * WROW), L_WQ + sg * 4096 + cw * 1024); };
        bf16x8 xs[5][4][2];
        auto grab = [&](int s) {
#pragma unroll
            for (int ks = 0; ks < 4; ++ks) {
                const unsigned a = (laneX ^ (32 * ks)) + slot_of(s);
                xs[s][ks][0] = lds_b128(a); xs[s][ks][1] = lds_b128(a + 4096);
            }
        };
        issue(0); issue(1);
        u64 ev;
        asm volatile("global_load_dwordx2 %0, %1, %2 sc1" : "=&v"(ev) : "v"(0u), "s"(p.ws) : "memory");
        WAIT_VM_LGKM_BAR(11);
        grab(0); issue(2);
        __builtin_amdgcn_raw_buffer_store_b128((u32x4){~0u, ~0u, ~0u, ~0u}, rws, lane * 16, kvp_mine, 16);
        asm volatile("s_waitcnt vmcnt(11) lgkmcnt(0)\n\ts_barrier" : "+v"(ev) :: "memory");
        const u64 tag = uniform64(ev) + 1;
        grab(1); issue(3); WAIT_VM_LGKM_BAR(10);
        set_flag(p.ws, f1_grp + tb * FS, tag, lane);
        grab(2); issue(4); WAIT_VM_LGKM_BAR(11);
        grab(3); issue(5); WAIT_VM_LGKM_BAR(10);
        grab(4); issue(6); WAIT_VM_LGKM_BAR(10);
        WAIT_VM_LGKM_BAR(0);
        const GAS u64* const f1p = (const GAS u64*)((const char*)p.ws + f1_grp + (lane & 7) * FS);
        u64 f1v;
        asm volatile("global_load_dwordx2 %0, %1, off sc1" : "=&v"(f1v) : "v"(f1p) : "memory");
        bf16x8 aphi[2];
        issue_wq(5); issue_wq(6); issue_wq(0); issue_wq(1); issue_wq(2); issue_wq(3); issue_wq(4);
        build_aphi(aphi, r, h2);
        PINV(aphi[0]); PINV(aphi[1]);
        asm volatile("s_waitcnt vmcnt(7)" : "+v"(f1v) :: "memory");
        if (__builtin_amdgcn_ballot_w64(f1v != tag) != 0) wait_flags(f1p, tag);
        WAIT_VM_LGKM_BAR(0);
        f32x16 qT[2] = {zero16, zero16};
        {
            const unsigned aq_real = L_WQ + rowpart + coloff, aq_pad = L_GT + rowpart;
            bf16x8 fq[3], xl[2][2];
            auto step_of = [](int n) { return n < 8 ? 5 + (n >> 2) : (n - 8) >> 2; };
            unsigned aQ = 0;
            auto rdq = [&](int n) {
                if ((n & 3) == 0) { const unsigned sg = seg(step_of(n)); aQ = cq == 7 ? aq_pad + 8 * sg : aq_real + 4096 * sg; PINV(aQ); }
                fq[n % 3] = tr_frag(aQ + (n & 3) * 1024, aQ + (n & 3) * 1024 + 256);
            };
            auto rdx = [&](int n) { const unsigned a = (laneX ^ (32 * (n & 3))) + slot_of(step_of(n)); xl[n & 1][0] = lds_b128(a); xl[n & 1][1] = lds_b128(a + 4096); };
            rdq(0); rdx(0); rdq(1);
            SB();
#pragma unroll
            for (int n = 0; n < 28; ++n) {
                const int s = step_of(n), ks = n & 3;
                if (n + 1 < 8) rdx(n + 1);
                if (n + 2 < 28) rdq(n + 2);
                SB();
                qT[0] = MFMA(fq[n % 3], n >= 8 ? xs[n >= 8 ? s : 0][ks][0] : xl[n & 1][0], qT[0]);
                SB();
                qT[1] = MFMA(fq[n % 3], n >= 8 ? xs[n >= 8 ? s : 0][ks][1] : xl[n & 1][1], qT[1]);
                SB();
                if (n == 7) {
                    lds_set(L_XFREE + 4 * cw);
                }
            }
        }
        bf16x8 eq[2][2][2];
        float gs[2];
        {
            f32x16 pq[2];
            const bf16x8 q00 = pack8(qT[0], 0), q10 = pack8(qT[1], 0);
            pq[0] = MFMA(aphi[0], q00, zero16); pq[1] = MFMA(aphi[0], q10, zero16);
            const bf16x8 q01 = pack8(qT[0], 1), q11 = pack8(qT[1], 1);
            pq[0] = MFMA(aphi[1], q01, pq[0]); pq[1] = MFMA(aphi[1], q11, pq[1]);
#pragma unroll
            for (int b = 0; b < 2; ++b) {
                const float lg = swap_hi(qT[b][12]);
                gs[b] = __builtin_amdgcn_rcpf(1.f + __builtin_amdgcn_exp2f(br ? lg * L2E : -lg * L2E));
                PINV(gs[b]);
            }
            float cp[2], cn[2];
            row_extrema(pq[0], pq[1], cp, cn);
            PINV(cp[0]); PINV(cp[1]); PINV(cn[0]); PINV(cn[1]);
#pragma unroll
            for (int b = 0; b < 2; ++b) {
                const f32x2 l2 = {L2E, L2E}, cp2 = {cp[b], cp[b]}, cn2 = {cn[b], cn[b]};
                unsigned pe[8], ne[8];
#pragma unroll
                for (int i = 0; i < 7; ++i) {
                    const f32x2 v = {pq[b][2 * i], pq[b][2 * i + 1]};
                    const f32x2 a = __builtin_elementwise_fma(v, l2, cp2), c = __builtin_elementwise_fma(v, -l2, cn2);
                    pe[i] = cvtpk2((f32x2){__builtin_amdgcn_exp2f(a[0]), __builtin_amdgcn_exp2f(a[1])});
                    ne[i] = cvtpk2((f32x2){__builtin_amdgcn_exp2f(c[0]), __builtin_amdgcn_exp2f(c[1])});
                }
                pe[7] = 0; ne[7] = 0;
                eq[b][0][0] = __builtin_bit_cast(bf16x8, (u32x4){pe[0], pe[1], pe[2], pe[3]}); eq[b][0][1] = __builtin_bit_cast(bf16x8, (u32x4){pe[4], pe[5], pe[6], pe[7]});
                eq[b][1][0] = __builtin_bit_cast(bf16x8, (u32x4){ne[0], ne[1], ne[2], ne[3]}); eq[b][1][1] = __builtin_bit_cast(bf16x8, (u32x4){ne[4], ne[5], ne[6], ne[7]});
            }
        }
#pragma unroll
        for (int b = 0; b < 2; ++b) asm volatile("" : "+v"(eq[b][0][0]), "+v"(eq[b][0][1]), "+v"(eq[b][1][0]), "+v"(eq[b][1][1]), "+v"(gs[b]));
        LGKM_BAR();
        __builtin_amdgcn_s_setprio(3);
        {
            bf16x8 akv[4];
#pragma unroll
            for (int f = 0; f < 4; ++f) akv[f] = lds_b128(L_KVA + f * 1024 + lane * 16);
            f32x16 np[2], nn[2];
#pragma unroll
            for (int b = 0; b < 2; ++b) { np[b] = MFMA(akv[0], eq[b][0][0], zero16); nn[b] = MFMA(akv[2], eq[b][1][0], zero16); }
#pragma unroll
            for (int b = 0; b < 2; ++b) { np[b] = MFMA(akv[1], eq[b][0][1], np[b]); nn[b] = MFMA(akv[3], eq[b][1][1], nn[b]); }
            SB();
            const unsigned so = rfl(WS_O + (((tb * 4 + cw) * 32 + bh) * 4) * 1024);
#pragma unroll
            for (int b = 0; b < 2; ++b) {
                const float ia = __builtin_amdgcn_rcpf(np[b][15]), ib = __builtin_amdgcn_rcpf(nn[b][15]);
                const float den = np[b][14] * ia + nn[b][14] * ib + 1e-6f;
                const float sc = gs[b] * __builtin_amdgcn_rcpf(den);
                const float ca = ia * sc, cb = ib * sc;
                float o[14];
#pragma unroll
                for (int i = 0; i < 14; ++i) o[i] = np[b][i] * ca + nn[b][i] * cb;
                const u32x4 o0 = {cvtpk(o[0], o[1]), cvtpk(o[2], o[3]), cvtpk(o[4], o[5]), cvtpk(o[6], o[7])};
                const u32x4 o1 = {cvtpk(o[8], o[9]), cvtpk(o[10], o[11]), cvtpk(o[12], o[13]), 0u};
                __builtin_amdgcn_raw_buffer_store_b128(o0, rws, lane * 16, so + b * 2048, 16);
                __builtin_amdgcn_raw_buffer_store_b96((u32x3){o1[0], o1[1], o1[2]}, rws, lane * 12, so + b * 2048 + 1024, 16);
            }
        }
        if (has_out) {
            const unsigned vwo = (lane >> 3) * XROW + 16 * ((lane & 7) ^ (4 * ((lane >> 4) & 1)));
            dma_rows<8>(mk_rsrc(p.woB, D_ * XROW), vwo, (640 - 448 + 64 * cw) * XROW + oc * 128, L_WO + (640 + 64 * cw) * 128);
            WAIT_VM(8);
        } else WAIT_VM(0);
        set_flag(p.ws, WS_F2 + (((tb * 4 + cw) * 2 + br) * 16 + h) * FS, tag, lane);
        if (has_out) { WAIT_VM(1); lds_count(L_WOCNT, lane); }
        return;
    }

    u64 tag;
    {
        const rsrc_t rg = mk_rsrc(p.gate, XROW);
        const rsrc_t rp = mk_rsrc(br ? p.wphiB : p.wphiA, 16 * DH * DH * 2);
        unsigned g0, g1; u32x2 wp; u64 ev;
        asm volatile("global_load_dwordx2 %3, %9, %10 sc1\n\t"
                     "buffer_load_ushort %0, %4, %5, 0 offen\n\tbuffer_load_ushort %1, %4, %5, 0 offen offset:512\n\tbuffer_load_dwordx2 %2, %6, %7, %8 offen\n\t"
                     "s_waitcnt vmcnt(0)"
                     : "=&v"(g0), "=&v"(g1), "=&v"(wp), "=&v"(ev)
                     : "v"(tid * 2), "s"(rg), "v"(tid * 8), "s"(rp), "s"(h * (DH * DH * 2)), "v"(0u), "s"(p.ws) : "memory");
        tag = uniform64(ev) + 1;
        *(LAS u32x2*)lds_at(L_GT + (tid & 63) * 64 + (tid >> 6) * 8) = (u32x2){g0 & 0xffffu, 0u};
        *(LAS u32x2*)lds_at(L_GT + (tid & 63) * 64 + (4 + (tid >> 6)) * 8) = (u32x2){g1 & 0xffffu, 0u};
        *(LAS u32x2*)lds_at(L_WPHI + tid * 8) = wp;
        if (tid < 16) *(volatile LAS unsigned*)lds_at(L_SYNC + 4 * tid) = 0u;
    }
    __builtin_amdgcn_s_setprio(2);
    LGKM_BAR();
    bf16x8 aphi[2];
    build_aphi(aphi, r, h2);
    PINV(aphi[0]); PINV(aphi[1]);
    const unsigned laneW = SLOT_W + rowpart + coloff;
    f32x16 vinit;
#pragma unroll
    for (int i = 0; i < 16; ++i) vinit[i] = r == 28 ? 1.f : 0.f;
    PINV(vinit);
    f32x16 kT[2] = {zero16, zero16}, vv[2] = {vinit, vinit};
    {
        unsigned aX[4], aK, aV;
        auto addr = [&](int s) {
#pragma unroll
            for (int ks = 0; ks < 4; ++ks) { aX[ks] = (laneX ^ (32 * ks)) + slot_of(s); PINV(aX[ks]); }
            aK = cq == 7 ? L_GT + rowpart + 56 : laneW + slot_of(s); PINV(aK);
            aV = cq == 7 ? L_GT + rowpart + 56 : laneW + slot_of(s) + 4096; PINV(aV);
        };
        Frag F[2];
#define RD_X(f, ks) do { f.x0 = lds_b128(aX[ks]); } while (0)
#define RD_X1(f, ks) do { f.x1 = lds_b128(aX[ks] + 4096); } while (0)
#define RD_K(f, ks) do { f.fk = tr_frag(aK + ks * 1024, aK + ks * 1024 + 256); } while (0)
#define RD_V(f, ks) do { f.fv = tr_frag(aV + ks * 1024, aV + ks * 1024 + 256); } while (0)
        addr(0);
        RD_X(F[0], 0); RD_K(F[0], 0); RD_X1(F[0], 0); RD_V(F[0], 0); SB();
#pragma unroll
        for (int n = 0; n < 28; ++n) {
            Frag& c = F[n & 1];
            Frag& x = F[(n + 1) & 1];
            const int nks = (n + 1) & 3;
            const bool more = n + 1 < 28;
            kT[0] = MFMA(c.fk, c.x0, kT[0]); SB();
            if (more && nks == 0) { LGKM_BAR(); addr((n + 1) >> 2); SB(); }
            if (more) { RD_X(x, nks); RD_K(x, nks); } SB();
            kT[1] = MFMA(c.fk, c.x1, kT[1]); SB();
            if (more) { RD_X1(x, nks); } SB();
            vv[0] = MFMA(c.x0, c.fv, vv[0]); SB();
            if (more) { RD_V(x, nks); } SB();
            vv[1] = MFMA(c.x1, c.fv, vv[1]); SB();
        }
    }

    const unsigned phiW = L_PHI + cw * 8192;
    {
        const unsigned pt0 = 4 * h2 + q4;
        const unsigned pa0 = phiW + pt0 * 64 + 8 * (cq ^ ((pt0 >> 1) & 7));
        const unsigned pa1 = (pa0 ^ 32) + 8 * 64;
        f32x16 pk[2];
        const bf16x8 k00 = pack8(kT[0], 0), k10 = pack8(kT[1], 0);
        pk[0] = MFMA(aphi[0], k00, zero16); pk[1] = MFMA(aphi[0], k10, zero16);
        const bf16x8 k01 = pack8(kT[0], 1), k11 = pack8(kT[1], 1);
        pk[0] = MFMA(aphi[1], k01, pk[0]); pk[1] = MFMA(aphi[1], k11, pk[1]);
        bf16x8 vp[2][2];
#pragma unroll
        for (int b = 0; b < 2; ++b)
#pragma unroll
            for (int s = 0; s < 2; ++s) { vp[b][s] = pack8(vv[b], s); PINV(vp[b][s]); }
        SB();
        float cp[2], cn[2];
        row_extrema(pk[0], pk[1], cp, cn);
        f32x2 ep[2][7], en[2][7];
        exp_pair(pk[0], cp[0], cn[0], ep[0], en[0]);
        phi_write(ep[0], en[0], phiW, r, h2);
        exp_pair(pk[1], cp[1], cn[1], ep[1], en[1]);
        f32x16 kvp = zero16, kvn = zero16;
        auto kv = [&](int b) {
#pragma unroll
            for (int s = 0; s < 2; ++s) {
                const unsigned o = b * 4096 + s * 1024;
                kvp = MFMA(tr_frag(pa0 + o, pa1 + o), vp[b][s], kvp);
                kvn = MFMA(tr_frag(pa0 + o + 2048, pa1 + o + 2048), vp[b][s], kvn);
            }
        };
        kv(0);
        phi_write(ep[1], en[1], phiW + 4096, r, h2);
        kv(1);
        asm volatile("s_waitcnt lgkmcnt(0)" ::: "memory");
#pragma unroll
        for (int f = 0; f < 4; ++f)
#pragma unroll
            for (int hh = 0; hh < 2; ++hh) {
                const f32x16& t = f < 2 ? kvp : kvn;
                const int o = 8 * (f & 1) + 4 * hh;
                *(LAS f32x4*)lds_at(phiW + f * 2048 + hh * 1024 + lane * 16) = (f32x4){t[o], t[o + 1], t[o + 2], t[o + 3]};
            }
    }
    LGKM_BAR();
    {
        f32x4 t[4][2];
#pragma unroll
        for (int u = 0; u < 4; ++u) {
            t[u][0] = *(const LAS f32x4*)lds_at(L_PHI + u * 8192 + cw * 2048 + lane * 16);
            t[u][1] = *(const LAS f32x4*)lds_at(L_PHI + u * 8192 + cw * 2048 + 1024 + lane * 16);
        }
        SB();
        float a[8];
#pragma unroll
        for (int i = 0; i < 8; ++i) {
            float x01 = addf(t[0][i >> 2][i & 3], t[1][i >> 2][i & 3]), x23 = addf(t[2][i >> 2][i & 3], t[3][i >> 2][i & 3]);
            a[i] = addf(x01, x23);
        }
        u32x4 o = {cvtpk(a[0], a[1]), cvtpk(a[2], a[3]), cvtpk(a[4], a[5]), cvtpk(a[6], a[7])};
        __builtin_amdgcn_raw_buffer_store_b128(o, rws, lane * 16, kvp_mine, 16);
    }
    const rsrc_t roA = mk_rsrc(p.woA, D_ * XROW), roB = mk_rsrc(p.woB, D_ * XROW);
    const unsigned vwo = (lane >> 3) * XROW + 16 * ((lane & 7) ^ (4 * ((lane >> 4) & 1)));
    const rsrc_t roC = cw < 2 ? roA : roB;
    const unsigned R5 = 320 + 64 * cw, R5g = cw < 2 ? R5 : R5 - 448;
    {
        bf16x8 sel;
        {
            const int jj = (r & 3) + 4 * (r >> 3);
            const bool on = r < 16 && ((r >> 2) & 1) == h2;
            const unsigned one = on ? (0x3F80u << (16 * (jj & 1))) : 0u;
            sel = __builtin_bit_cast(bf16x8, (u32x4){(jj >> 1) == 0 ? one : 0u, (jj >> 1) == 1 ? one : 0u, (jj >> 1) == 2 ? one : 0u, (jj >> 1) == 3 ? one : 0u});
            PINV(sel);
        }
        dma_rows<2>(roA, vwo, (256 + 16 * cw) * XROW + oc * 128, L_WO + (256 + 16 * cw) * 128);
        dma_rows<2>(roB, vwo, (576 - 448 + 16 * cw) * XROW + oc * 128, L_WO + (576 + 16 * cw) * 128);
        lds_wait_ge(L_XFREE + 4 * cw, 1);
        dma_rows<6>(roA, vwo, 64 * cw * XROW + oc * 128, L_WO + 64 * cw * 128);
        const int hr = (r >> 2) & 1, reg = (r & 3) + 4 * (r >> 3);
        const int col = reg < 14 ? 14 * hr + reg : 28;
        const unsigned vo = (h2 * 32 + col) * 16, so = WS_KVP + (bh * 8 * 4 + cw) * 1024;
        u32x4 part[8];
#pragma unroll
        for (int u = 0; u < 8; ++u) part[u] = load_sc1_asm(rws, vo, so + u * 4096);
#define PARTS "+v"(part[0]), "+v"(part[1]), "+v"(part[2]), "+v"(part[3]), "+v"(part[4]), "+v"(part[5]), "+v"(part[6]), "+v"(part[7])
        dma_rows<2>(roA, vwo, (64 * cw + 48) * XROW + oc * 128, L_WO + (64 * cw + 48) * 128);
        dma_rows<8>(roC, vwo, R5g * XROW + oc * 128, L_WO + R5 * 128);
        asm volatile("s_waitcnt vmcnt(10)" : PARTS :: "memory");
        auto pattern_left = [&]() {
            unsigned m = 0;
#pragma unroll
            for (int u = 0; u < 8; ++u) { m = umax3(m, part[u][0], part[u][1]); m = umax3(m, part[u][2], part[u][3]); }
            return __builtin_amdgcn_ballot_w64(m == ~0u) != 0;
        };
        if (pattern_left()) {
            unsigned spins = 0;
#pragma unroll 1
            do {
#pragma unroll
                for (int u = 0; u < 8; ++u) part[u] = load_sc1_asm(rws, vo, so + u * 4096);
                asm volatile("s_waitcnt vmcnt(0)" : PARTS :: "memory");
            } while (pattern_left() && ++spins < SPIN_CAP);
        }
#undef PARTS
        SB();
        f32x16 acc = zero16;
#pragma unroll
        for (int u = 0; u < 8; ++u) acc = MFMA(sel, __builtin_bit_cast(bf16x8, part[u]), acc);
        u32x4 o = __builtin_bit_cast(u32x4, pack8(acc, 0));
        if (reg == 15) o = (u32x4){ONE2, ONE2, ONE2, ONE2};
        *(LAS u32x4*)lds_at(L_KVA + cw * 1024 + lane * 16) = o;
    }
    LGKM_BAR();
    if (!has_out) return;
    WAIT_VM(0); lds_count(L_WOCNT, lane);

    const unsigned tk = cw & 1, kh = cw >> 1;
    f32x16 mine = zero16, other = zero16;
    {
        const unsigned colp = 32 * blk16 + 8 * p4;
        const unsigned r0 = kh * 448 + 14 * h2 + q4, r1 = kh * 448 + 14 * h2 + (q4 < 2 ? 12 + q4 : 34 + q4);
        unsigned a0 = L_WO + r0 * 128 + 64 * (kh ^ ((r0 >> 1) & 1)) + colp, b0 = L_WO + r1 * 128 + 64 * (kh ^ ((r1 >> 1) & 1)) + colp;
        unsigned a2 = a0 ^ 64, b2 = b0 ^ 64;
        asm volatile("" : "+v"(a0), "+v"(a2), "+v"(b0), "+v"(b2));
        lds_wait_ge(L_WOCNT, 8);
        if (cw == 0) {
            wait_flags((const GAS u64*)((const char*)p.ws + WS_F2 + (og * 32 + (lane & 31)) * FS), tag);
            lds_set(L_F2OK);
        } else lds_wait_ge(L_F2OK, 1);
        u32x4 of[16][2];
        auto ld = [&](int i) {
            const unsigned so = WS_O + (((og * 32 + kh * 16 + i) * 2 + tk) * 2) * 1024;
            of[i][0] = __builtin_amdgcn_raw_buffer_load_b128(rws, lane * 16, so, 16);
            const u32x3 t = __builtin_amdgcn_raw_buffer_load_b96(rws, lane * 12, so + 1024, 16);
            of[i][1] = (u32x4){t[0], t[1], t[2], 0u};
        };
        bf16x8 A[2][2];
        auto rd = [&](bf16x8 (&f)[2], int n) {
            const int g = n / 7, m = n % 7;
            const unsigned o = g * 4 * 28 * 128;
            if (m < 4) { const unsigned u = o + m * 28 * 128; f[0] = tr_frag(a0 + u, a0 + u + 512); f[1] = tr_frag(a2 + u, a2 + u + 512); }
            if (m == 4) { f[0] = tr_frag(a0 + o + 8 * 128, b0 + o); f[1] = tr_frag(a2 + o + 8 * 128, b2 + o); }
            if (m == 5) { f[0] = tr_frag(a2 + o + 38 * 128, a0 + o + 64 * 128); f[1] = tr_frag(a0 + o + 38 * 128, a2 + o + 64 * 128); }
            if (m == 6) { f[0] = tr_frag(b0 + o + 56 * 128, a2 + o + 94 * 128); f[1] = tr_frag(b2 + o + 56 * 128, a0 + o + 94 * 128); }
        };
#pragma unroll
        for (int i = 0; i < OD; ++i) ld(i);
        SB();
        rd(A[0], 0); SB();
#pragma unroll
        for (int n = 0; n < 28; ++n) {
            const int g = n / 7, m = n % 7, h0 = 4 * g;
            if (m < 4 && h0 + m + OD < 16) ld(h0 + m + OD);
            if (n + 1 < 28) rd(A[(n + 1) & 1], n + 1);
            SB();
            u32x4 bq;
            if (m < 4) bq = of[h0 + m][0];
            if (m == 4) bq = (u32x4){of[h0][1][0], of[h0][1][1], of[h0][1][2], of[h0 + 1][1][0]};
            if (m == 5) bq = (u32x4){of[h0 + 1][1][1], of[h0 + 1][1][2], of[h0 + 2][1][0], of[h0 + 2][1][1]};
            if (m == 6) bq = (u32x4){of[h0 + 2][1][2], of[h0 + 3][1][0], of[h0 + 3][1][1], of[h0 + 3][1][2]};
            const bf16x8 bb = __builtin_bit_cast(bf16x8, bq);
            mine = MFMA(A[n & 1][0], bb, mine); other = MFMA(A[n & 1][1], bb, other);
            SB();
        }
    }
#pragma unroll
    for (int g = 0; g < 4; ++g)
        *(LAS f32x4*)lds_at(L_PHI + cw * 4096 + g * 1024 + lane * 16) = (f32x4){other[4 * g], other[4 * g + 1], other[4 * g + 2], other[4 * g + 3]};
    LGKM_BAR();
    if (blockIdx.x == 0 && tid == 0) __hip_atomic_store((GAS u64*)((char*)p.ws + WS_EPOCH), tag, __ATOMIC_RELAXED, __HIP_MEMORY_SCOPE_AGENT);
    {
        unsigned d[8];
        f32x4 t[4];
#pragma unroll
        for (int g = 0; g < 4; ++g) t[g] = *(const LAS f32x4*)lds_at(L_PHI + (cw ^ 2) * 4096 + g * 1024 + lane * 16);
        SB();
#pragma unroll
        for (int g = 0; g < 4; ++g) {
            d[2 * g] = cvtpk(mine[4 * g] + t[g][0], mine[4 * g + 1] + t[g][1]);
            d[2 * g + 1] = cvtpk(mine[4 * g + 2] + t[g][2], mine[4 * g + 3] + t[g][3]);
        }
        const rsrc_t rout = mk_rsrc(p.out, S_ * XROW);
        const unsigned img = L_PHI + 16384;
#pragma unroll
        for (int k = 0; k < 4; k += 2) {
            auto sx0 = __builtin_amdgcn_permlane32_swap(d[2 * k], d[2 * k + 2], false, false);
            auto sx1 = __builtin_amdgcn_permlane32_swap(d[2 * k + 1], d[2 * k + 3], false, false);
            *(LAS u32x4*)lds_at(img + (tk * 32 + r) * 128 + 16 * ((4 * kh + k + h2) ^ (r & 7))) = (u32x4){sx0[0], sx1[0], sx0[1], sx1[1]};
        }
        LGKM_BAR();
#pragma unroll
        for (int i = 0; i < 2; ++i) {
            const unsigned row = 16 * cw + 8 * i + (lane >> 3);
            const u32x4 o = *(const LAS u32x4*)lds_at(img + row * 128 + 16 * ((lane & 7) ^ (row & 7)));
            __builtin_amdgcn_raw_buffer_store_b128(o, rout, (8 * i + (lane >> 3)) * XROW + (lane & 7) * 16, (og * 64 + 16 * cw) * XROW + oc * 128, 0);
        }
    }
}

extern "C" void kernel_launch(void* const* d_in, const int* in_sizes, int n_in, void* d_out, int out_size, void* d_ws, size_t ws_size,
                              hipStream_t stream) {
    Params p;
    p.x = d_in[0]; p.wqkvA = d_in[1]; p.wphiA = d_in[2]; p.woA = d_in[3];
    p.wqkvB = d_in[5]; p.wphiB = d_in[6]; p.woB = d_in[7]; p.gate = d_in[8];
    p.out = d_out; p.ws = d_ws;
    (void)hipFuncSetAttribute((const void*)hedgehog_mix_kernel, hipFuncAttributeMaxDynamicSharedMemorySize, L_TOTAL);
    hedgehog_mix_kernel<<<256, 512, L_TOTAL, stream>>>(p);
}
```
